```python
import torch

def _softmax(x, axis=-1):
    return torch.softmax(x, dim=axis)
def _gelu(x):
    return torch.nn.functional.gelu(x, approximate="tanh")
def _silu(x):
    return torch.nn.functional.silu(x)
def _sigmoid(x):
    return torch.sigmoid(x)
def _layernorm(x, axis=-1, eps=1e-5):
    m = x.mean(dim=axis, keepdim=True)
    v = x.var(dim=axis, keepdim=True, unbiased=False)
    return (x - m) / torch.sqrt(v + eps)
def _scatter_add(tgt, idx, src, axis=0):
    return tgt.clone().index_add_(axis, idx, src)
def _segment_sum(src, seg, n):
    out = torch.zeros((n,) + src.shape[1:], dtype=src.dtype)
    return out.index_add_(0, seg, src)
def _pad(x, pw):
    flat = tuple(v for pair in reversed(pw) for v in pair)
    return torch.nn.functional.pad(x, flat)

kernel_name = 'interleave_layers__ssm_conv_h3__dense_activation_mish__prompt'

def setup_inputs():

    _w = (lambda a: (lambda t: t.to(torch.bfloat16) if t.is_floating_point() else t)(torch.as_tensor(a)))
    def _A_setup():
        B, S, D = 1, 2048, 640
        ks = 4
        return {
            "q": _w((lambda *s: torch.randn(*s, dtype=torch.bfloat16) * 0.05)(B, S, D)),
            "k": _w((lambda *s: torch.randn(*s, dtype=torch.bfloat16) * 0.2)(B, S, D)),
            "v": _w((lambda *s: torch.randn(*s, dtype=torch.bfloat16) * 0.2)(B, S, D)),
            "shift_k": _w((lambda *s: torch.randn(*s, dtype=torch.bfloat16) * 0.2)(D, ks) * 0.5),
            "ssm_k": _w((lambda *s: torch.randn(*s, dtype=torch.bfloat16) * 0.4)(D, S) * (1.0 / S ** 0.5)),
            "ssm_D": _w((lambda *s: torch.randn(*s, dtype=torch.bfloat16) * 0.2)(D)),
        }
    _A_in = _A_setup()

    def _B_setup():
        B, S, D, F = 1, 2048, 640, 1728
        return {
            "x": _w((lambda *s: torch.randn(*s, dtype=torch.bfloat16) * 0.05)(B, S, D)),
            "w1": _w((lambda *s: torch.randn(*s, dtype=torch.bfloat16) * 0.8)(D, F) * (D ** -0.5)),
            "b1": _w((lambda *s: torch.randn(*s, dtype=torch.bfloat16) * 0.2)(F)),
            "w2": _w((lambda *s: torch.randn(*s, dtype=torch.bfloat16) * 0.1)(F, D) * (F ** -0.5)),
        }
    _B_in = _B_setup()

    return {
        "A_q": _A_in["q"],
        "A_k": _A_in["k"],
        "A_v": _A_in["v"],
        "A_shift_k": _A_in["shift_k"],
        "A_ssm_k": _A_in["ssm_k"],
        "A_ssm_D": _A_in["ssm_D"],
        "B_x": _B_in["x"],
        "B_w1": _B_in["w1"],
        "B_b1": _B_in["b1"],
        "B_w2": _B_in["w2"],
    }

def reference(A_q, A_k, A_v, A_shift_k, A_ssm_k, A_ssm_D, B_x, B_w1, B_b1, B_w2):
    def _A_ref(q, k, v, shift_k, ssm_k, ssm_D):
        B, S, D = q.shape
        ks = shift_k.shape[-1]
        k_pad = _pad(k, ((0, 0), (ks - 1, 0), (0, 0)))
        k_shift = torch.zeros_like(k)
        for t in range(ks):
            k_shift = k_shift + k_pad[:, t:t + S, :] * shift_k[:, ks - 1 - t][None, None, :]
        kv = k_shift * v
        L = 2 * S
        kv_pad = _pad(kv, ((0, 0), (0, S), (0, 0)))
        kf_pad = _pad(ssm_k, ((0, 0), (0, S)))
        kv_f = (lambda x, axes=(-2,-1): torch.fft.rfft2(x.float(), dim=axes))(kv_pad[:, None, :, :], axes=(1, 2))
        kf_f = (lambda x, axes=(-2,-1): torch.fft.rfft2(x.float(), dim=axes))(kf_pad[:, None, :], axes=(1, 2))
        kf_f = (lambda x, *d: x.permute(*d))(kf_f, 1, 2, 0)
        y_f = kv_f * kf_f[None, :, :, :]
        y = (lambda x, s, axes=(-2,-1): torch.fft.irfft2(x, s=s, dim=axes).to(torch.bfloat16))(y_f, (1, L), axes=(1, 2))[:, 0, :S, :]
        y = y + kv * ssm_D[None, None, :]
        return q * y

    def _B_ref(x, w1, b1, w2):
        h = torch.einsum("bsd,df->bsf", x, w1) + b1
        sp = torch.log1p(torch.exp(-torch.abs(h))) + torch.where(h > 0, h, torch.zeros_like(h))
        act = h * torch.tanh(sp)
        return torch.einsum("bsf,fd->bsd", act, w2)

    x = A_q
    x = x + _A_ref(_layernorm(x, axis=-1), A_k, A_v, A_shift_k, A_ssm_k, A_ssm_D)
    x = x + _B_ref(_layernorm(x, axis=-1), B_w1, B_b1, B_w2)
    x = x + _A_ref(_layernorm(x, axis=-1), A_k, A_v, A_shift_k, A_ssm_k, A_ssm_D)
    x = x + _B_ref(_layernorm(x, axis=-1), B_w1, B_b1, B_w2)
    return x
```

```cpp
#include <hip/hip_runtime.h>
#include <hip/hip_bf16.h>
#include <stdint.h>
typedef unsigned short u16;
constexpr int S=2048, D=640, F=1728;
__device__ __forceinline__ float b2f(u16 v){ return __uint_as_float(((unsigned)v)<<16); }
__device__ __forceinline__ u16 f2b(float f){ __hip_bfloat16 h=__float2bfloat16(f); return *(u16*)&h; }

__global__ void k_kv(const u16* k,const u16* v,const u16* sk,u16* kvT){
  int i=blockIdx.x*blockDim.x+threadIdx.x; if(i>=S*D) return;
  int s=i/D,d=i%D; float a=0;
  for(int j=0;j<4;j++){ if(s-j>=0) a+=b2f(k[(s-j)*D+d])*b2f(sk[d*4+j]); }
  kvT[d*S+s]=f2b(a*b2f(v[i]));
}
__global__ void k_conv(const u16* kvT,const u16* hk,const u16* sD,u16* y){
  __shared__ float u[S], h[S];
  int d=blockIdx.x;
  for(int i=threadIdx.x;i<S;i+=blockDim.x){u[i]=b2f(kvT[d*S+i]);h[i]=b2f(hk[d*S+i]);}
  __syncthreads();
  for(int s=threadIdx.x;s<S;s+=blockDim.x){ float a=0; for(int j=0;j<=s;j++) a+=u[j]*h[s-j]; y[s*D+d]=f2b(a+u[s]*b2f(sD[d])); }
}
__device__ float blksum(float v,float* red){
  for(int o=32;o;o>>=1) v+=__shfl_xor(v,o);
  __syncthreads(); if((threadIdx.x&63)==0) red[threadIdx.x>>6]=v; __syncthreads();
  float t=0; for(int i=0;i<blockDim.x/64;i++) t+=red[i]; return t;
}
__global__ void k_ln(const u16* x,const u16* y,u16* x1,u16* xn){
  __shared__ float red[16]; int s=blockIdx.x,d=threadIdx.x;
  float a=b2f(x[s*D+d]); float m=blksum(a,red)/D; float c=a-m; float var=blksum(c*c,red)/D;
  float b=a+c*rsqrtf(var+1e-5f)*b2f(y[s*D+d]);
  m=blksum(b,red)/D; c=b-m; var=blksum(c*c,red)/D;
  x1[s*D+d]=f2b(b); xn[s*D+d]=f2b(c*rsqrtf(var+1e-5f));
}
template<int K,int N,int MODE>
__global__ void k_gemm(const u16* A,const u16* B,const u16* aux,u16* C){
  __shared__ float As[16][17],Bs[16][17];
  int tx=threadIdx.x&15,ty=threadIdx.x>>4; int r=blockIdx.y*16+ty,c=blockIdx.x*16+tx; float a=0;
  for(int k0=0;k0<K;k0+=16){ As[ty][tx]=b2f(A[r*K+k0+tx]); Bs[ty][tx]=b2f(B[(k0+ty)*N+c]); __syncthreads();
    for(int k=0;k<16;k++) a+=As[ty][k]*Bs[k][tx]; __syncthreads(); }
  if(MODE==0){ float h=a+b2f(aux[c]); float e=__expf(fminf(h,30.f)); float n=e*(e+2.f); C[r*N+c]=f2b(h*n/(n+2.f)); }
  else C[r*N+c]=f2b(a+b2f(aux[r*N+c]));
}
extern "C" void kernel_launch(void* const* d_in, const int* in_sizes, int n_in, void* d_out, int out_size, void* d_ws, size_t ws_size, hipStream_t stream) {
  const u16 *q=(const u16*)d_in[0],*k=(const u16*)d_in[1],*v=(const u16*)d_in[2],*sk=(const u16*)d_in[3],*hk=(const u16*)d_in[4],*sD=(const u16*)d_in[5],*w1=(const u16*)d_in[7],*b1=(const u16*)d_in[8],*w2=(const u16*)d_in[9];
  u16* ws=(u16*)d_ws; u16 *kvT=ws,*y=ws+S*D,*x1=ws+2*S*D,*xn=ws+3*S*D,*x2=ws+4*S*D,*h=ws+5*S*D; u16* out=(u16*)d_out;
  k_kv<<<S*D/256,256,0,stream>>>(k,v,sk,kvT);
  k_conv<<<D,1024,0,stream>>>(kvT,hk,sD,y);
  k_ln<<<S,D,0,stream>>>(q,y,x1,xn);
  k_gemm<D,F,0><<<dim3(F/16,S/16),256,0,stream>>>(xn,w1,b1,h);
  k_gemm<F,D,1><<<dim3(D/16,S/16),256,0,stream>>>(h,w2,x1,x2);
  k_ln<<<S,D,0,stream>>>(x2,y,x1,xn);
  k_gemm<D,F,0><<<dim3(F/16,S/16),256,0,stream>>>(xn,w1,b1,h);
  k_gemm<F,D,1><<<dim3(D/16,S/16),256,0,stream>>>(h,w2,x1,out);
}
```

```cpp
#include <hip/hip_runtime.h>
#include <hip/hip_bf16.h>
#include <stdint.h>
typedef unsigned short u16;
constexpr int S=2048, D=640, F=1728;
__device__ __forceinline__ float b2f(u16 v){ return __uint_as_float(((unsigned)v)<<16); }
__device__ __forceinline__ u16 f2b(float f){ __hip_bfloat16 h=__float2bfloat16(f); return *(u16*)&h; }

__global__ void k_kv(const u16* k,const u16* v,const u16* sk,u16* kvT){
  int i=blockIdx.x*blockDim.x+threadIdx.x; if(i>=S*D) return;
  int s=i/D,d=i%D; float a=0;
  for(int j=0;j<4;j++){ if(s-j>=0) a+=b2f(k[(s-j)*D+d])*b2f(sk[d*4+j]); }
  kvT[d*S+s]=f2b(a*b2f(v[i]));
}
__global__ void k_conv(const u16* kvT,const u16* hk,const u16* sD,u16* y){
  __shared__ float u[S], h[S];
  int d=blockIdx.x;
  for(int i=threadIdx.x;i<S;i+=blockDim.x){u[i]=b2f(kvT[d*S+i]);h[i]=b2f(hk[d*S+i]);}
  __syncthreads();
  for(int s=threadIdx.x;s<S;s+=blockDim.x){ float a=0; for(int j=0;j<=s;j++) a+=u[j]*h[s-j]; y[s*D+d]=f2b(a+u[s]*b2f(sD[d])); }
}
__device__ float blksum(float v,float* red){
  for(int o=32;o;o>>=1) v+=__shfl_xor(v,o);
  __syncthreads(); if((threadIdx.x&63)==0) red[threadIdx.x>>6]=v; __syncthreads();
  float t=0; for(int i=0;i<blockDim.x/64;i++) t+=red[i]; return t;
}
__global__ void k_ln(const u16* x,const u16* y,u16* x1,u16* xn){
  __shared__ float red[16]; int s=blockIdx.x,d=threadIdx.x;
  float a=b2f(x[s*D+d]); float m=blksum(a,red)/D; float c=a-m; float var=blksum(c*c,red)/D;
  float b=a+c*rsqrtf(var+1e-5f)*b2f(y[s*D+d]);
  m=blksum(b,red)/D; c=b-m; var=blksum(c*c,red)/D;
  x1[s*D+d]=f2b(b); xn[s*D+d]=f2b(c*rsqrtf(var+1e-5f));
}
template<int K,int N,int MODE>
__global__ void k_gemm(const u16* A,const u16* B,const u16* aux,u16* C){
  __shared__ float As[16][17],Bs[16][17];
  int tx=threadIdx.x&15,ty=threadIdx.x>>4; int r=blockIdx.y*16+ty,c=blockIdx.x*16+tx; float a=0;
  for(int k0=0;k0<K;k0+=16){ As[ty][tx]=b2f(A[r*K+k0+tx]); Bs[ty][tx]=b2f(B[(k0+ty)*N+c]); __syncthreads();
    for(int k=0;k<16;k++) a+=As[ty][k]*Bs[k][tx]; __syncthreads(); }
  if(MODE==0){ float h=a+b2f(aux[c]); float e=__expf(fminf(h,30.f)); float n=e*(e+2.f); C[r*N+c]=f2b(h*n/(n+2.f)); }
  else C[r*N+c]=f2b(a+b2f(aux[r*N+c]));
}

#define R8(x) x x x x x x x x
#define R7(x) x x x x x x x
#define R64(x) R8(R8(x))
#define R512(x) R8(R64(x))
#define BODY asm volatile("v_fma_f32 %0, %2, %2, %2\n\tv_fma_f32 %1, %2, %2, %2":"=v"(a),"=v"(c):"v"(b));
__global__ __launch_bounds__(256,1) void k_x(float* p){ float a,c,b=threadIdx.x; for(int i=0;i<56;i++){ R64(BODY) asm volatile("":::"memory"); } if(a+c==1234.5f) p[0]=a; }
extern "C" void kernel_launch(void* const* d_in, const int* in_sizes, int n_in, void* d_out, int out_size, void* d_ws, size_t ws_size, hipStream_t stream) {
  const u16 *q=(const u16*)d_in[0],*k=(const u16*)d_in[1],*v=(const u16*)d_in[2],*sk=(const u16*)d_in[3],*hk=(const u16*)d_in[4],*sD=(const u16*)d_in[5],*w1=(const u16*)d_in[7],*b1=(const u16*)d_in[8],*w2=(const u16*)d_in[9];
  u16* ws=(u16*)d_ws; u16 *kvT=ws,*y=ws+S*D,*x1=ws+2*S*D,*xn=ws+3*S*D,*x2=ws+4*S*D,*h=ws+5*S*D; u16* out=(u16*)d_out;
  k_x<<<256,256,0,stream>>>((float*)d_ws+(64<<20)/4);
  k_kv<<<S*D/256,256,0,stream>>>(k,v,sk,kvT);
  k_conv<<<D,1024,0,stream>>>(kvT,hk,sD,y);
  k_ln<<<S,D,0,stream>>>(q,y,x1,xn);
  k_gemm<D,F,0><<<dim3(F/16,S/16),256,0,stream>>>(xn,w1,b1,h);
  k_gemm<F,D,1><<<dim3(D/16,S/16),256,0,stream>>>(h,w2,x1,x2);
  k_ln<<<S,D,0,stream>>>(x2,y,x1,xn);
  k_gemm<D,F,0><<<dim3(F/16,S/16),256,0,stream>>>(xn,w1,b1,h);
  k_gemm<F,D,1><<<dim3(D/16,S/16),256,0,stream>>>(h,w2,x1,out);
}
```

```cpp
#include <hip/hip_runtime.h>
#include <hip/hip_bf16.h>

__global__ __launch_bounds__(256, 1) void h3_mish_fused(const void* q, const void* k, const void* v, const void* sk, const void* hk,
    const void* sd, const void* w1, const void* b1, const void* w2, void* out, void* ws) {
  __shared__ __attribute__((aligned(16))) char lds[163840];
  asm volatile(
    "s_mov_b64 s[6:7], %0\n"
    "s_mov_b32 s8, %1\n"
    "v_mov_b32 v0, %2\n"
    "Lbegin_%=:\n"
    "s_load_dwordx16 s[32:47], s[6:7], 0\n"
    "s_load_dwordx4 s[48:51], s[6:7], 64\n"
    "s_load_dwordx2 s[52:53], s[6:7], 80\n"
    "v_lshrrev_b32 v4, 6, v0\n"
    "v_and_b32 v0, 63, v0\n"
    "v_lshlrev_b32 v1, 4, v0\n"
    "v_readfirstlane_b32 s9, v4\n"
    "v_lshlrev_b32 v2, 3, v0\n"
    "s_and_b32 s11, s8, 7\n"
    "s_lshr_b32 s10, s8, 3\n"
    "s_lshr_b32 s83, s10, 4\n"
    "s_lshl_b32 s54, s11, 1\n"
    "s_add_u32 s54, s54, s83\n"
    "s_and_b32 s55, s10, 15\n"
    "s_mul_i32 s56, s11, 20\n"
    "s_add_u32 s56, s56, s10\n"
    "s_lshr_b32 s57, s56, 4\n"
    "s_and_b32 s58, s56, 15\n"
    "s_mul_i32 s97, s9, 35840\n"
    "s_lshr_b32 s59, s11, 1\n"
    "s_lshl_b32 s59, s59, 2\n"
    "s_lshr_b32 s83, s10, 3\n"
    "s_add_u32 s59, s59, s83\n"
    "s_and_b32 s5, s11, 1\n"
    "s_lshl_b32 s5, s5, 3\n"
    "s_and_b32 s83, s10, 7\n"
    "s_add_u32 s5, s5, s83\n"
    "s_getpc_b64 s[28:29]\n"
    "Lhere_1_%=:\n"
    "s_sub_u32 s28, s28, Lhere_1_%=-Lbegin_%=\n"
    "s_subb_u32 s29, s29, 0\n"
    "s_and_b32 s29, s29, 0xffff\n"
    "s_mov_b32 s30, Lend_%=-Lbegin_%=\n"
    "s_mov_b32 s31, 0x20000\n"
    "s_and_b32 s99, s10, 31\n"
    "s_lshl_b32 s99, s99, 2\n"
    "s_add_u32 s99, s99, s9\n"
    "s_lshl_b32 s99, s99, 10\n"
    "v_add_u32 v228, s99, v1\n"
    "s_mov_b32 s99, 0\n"
    "buffer_load_dwordx4 v[224:227], v228, s[28:31], s99 offen\n"
    "s_waitcnt lgkmcnt(0)\n"
    "s_mov_b32 s12, s52\n"
    "s_and_b32 s13, s53, 0xffff\n"
    "s_mov_b32 s14, 35115008\n"
    "s_mov_b32 s15, 0x20000\n"
    "s_cmp_lt_u32 s10, 20\n"
    "s_cbranch_scc1 Lconv_%=\n"
    "Lprep_%=:\n"
    "s_sub_u32 s56, s10, 20\n"
    "s_lshl_b32 s56, s56, 3\n"
    "s_add_u32 s56, s56, s11\n"
    "s_cmp_lt_u32 s56, 32\n"
    "s_cbranch_scc1 Lpst_%=\n"
    "s_cmp_lt_u32 s56, 64\n"
    "s_cbranch_scc1 Lpw1_%=\n"
    "s_cmp_lt_u32 s56, 92\n"
    "s_cbranch_scc1 Lpw2_%=\n"
    "s_branch Lpidle_%=\n"
    "Lpw2_%=:\n"
    "s_mov_b32 s16, s48\n"
    "s_and_b32 s17, s49, 0xffff\n"
    "s_mov_b32 s18, 2211840\n"
    "s_mov_b32 s19, 0x20000\n"
    "s_sub_u32 s90, s56, 64\n"
    "s_lshl_b32 s91, s90, 1\n"
    "s_lshr_b32 s83, s9, 1\n"
    "s_add_u32 s91, s91, s83\n"
    "s_and_b32 s92, s9, 1\n"
    "s_mul_i32 s83, s91, 43691\n"
    "s_lshr_b32 s83, s83, 17\n"
    "s_mul_i32 s84, s83, 12\n"
    "s_lshl_b32 s85, s91, 5\n"
    "s_add_u32 s84, s84, s85\n"
    "s_sub_u32 s85, s91, 48\n"
    "s_mul_i32 s85, s85, 216\n"
    "s_cmp_ge_u32 s91, 48\n"
    "s_cselect_b32 s84, s85, s84\n"
    "s_cselect_b64 s[100:101], -1, 0\n"
    "v_lshrrev_b32 v10, 3, v0\n"
    "v_and_b32 v11, 7, v0\n"
    "s_mul_i32 s86, s92, 640\n"
    "v_lshl_add_u32 v12, v11, 4, s86\n"
    "v_lshrrev_b32 v17, 2, v10\n"
    "v_lshl_add_u32 v17, v17, 2, v10\n"
    "v_cndmask_b32 v17, v10, v17, s[100:101]\n"
    "v_add_u32 v17, s84, v17\n"
    "v_cmp_lt_u32 vcc, 3, v10\n"
    "s_nop 0\n"
    "s_and_b64 vcc, vcc, s[100:101]\n"
    "v_mov_b32 v18, 2147418112\n"
    "v_cndmask_b32 v18, 0, v18, vcc\n"
    "s_cmp_ge_u32 s91, 48\n"
    "s_movk_i32 s85, 0\n"
    "s_cselect_b32 s85, 96, s85\n"
    "v_add_u32 v16, s85, v17\n"
    "v_mul_u32_u24 v16, 1280, v16\n"
    "v_add_u32 v4, v16, v12\n"
    "buffer_load_dwordx4 v[20:23], v4, s[16:19], 0 offen offset:0\n"
    "buffer_load_dwordx4 v[24:27], v4, s[16:19], 0 offen offset:128\n"
    "buffer_load_dwordx4 v[28:31], v4, s[16:19], 0 offen offset:256\n"
    "buffer_load_dwordx4 v[32:35], v4, s[16:19], 0 offen offset:384\n"
    "buffer_load_dwordx4 v[36:39], v4, s[16:19], 0 offen offset:512\n"
    "s_cmp_ge_u32 s91, 48\n"
    "s_movk_i32 s85, 8\n"
    "s_cselect_b32 s85, 204, s85\n"
    "v_add_u32 v16, s85, v17\n"
    "v_mul_u32_u24 v16, 1280, v16\n"
    "v_add_u32 v5, v16, v12\n"
    "buffer_load_dwordx4 v[40:43], v5, s[16:19], 0 offen offset:0\n"
    "buffer_load_dwordx4 v[44:47], v5, s[16:19], 0 offen offset:128\n"
    "buffer_load_dwordx4 v[48:51], v5, s[16:19], 0 offen offset:256\n"
    "buffer_load_dwordx4 v[52:55], v5, s[16:19], 0 offen offset:384\n"
    "buffer_load_dwordx4 v[56:59], v5, s[16:19], 0 offen offset:512\n"
    "s_cmp_ge_u32 s91, 48\n"
    "s_movk_i32 s85, 16\n"
    "s_cselect_b32 s85, 100, s85\n"
    "v_add_u32 v16, s85, v17\n"
    "v_mul_u32_u24 v16, 1280, v16\n"
    "v_add_u32 v6, v16, v12\n"
    "v_add_u32 v6, v6, v18\n"
    "buffer_load_dwordx4 v[60:63], v6, s[16:19], 0 offen offset:0\n"
    "buffer_load_dwordx4 v[64:67], v6, s[16:19], 0 offen offset:128\n"
    "buffer_load_dwordx4 v[68:71], v6, s[16:19], 0 offen offset:256\n"
    "buffer_load_dwordx4 v[72:75], v6, s[16:19], 0 offen offset:384\n"
    "buffer_load_dwordx4 v[76:79], v6, s[16:19], 0 offen offset:512\n"
    "s_cmp_ge_u32 s91, 48\n"
    "s_movk_i32 s85, 24\n"
    "s_cselect_b32 s85, 208, s85\n"
    "v_add_u32 v16, s85, v17\n"
    "v_mul_u32_u24 v16, 1280, v16\n"
    "v_add_u32 v7, v16, v12\n"
    "v_add_u32 v7, v7, v18\n"
    "buffer_load_dwordx4 v[80:83], v7, s[16:19], 0 offen offset:0\n"
    "buffer_load_dwordx4 v[84:87], v7, s[16:19], 0 offen offset:128\n"
    "buffer_load_dwordx4 v[88:91], v7, s[16:19], 0 offen offset:256\n"
    "buffer_load_dwordx4 v[92:95], v7, s[16:19], 0 offen offset:384\n"
    "buffer_load_dwordx4 v[96:99], v7, s[16:19], 0 offen offset:512\n"
    "v_mov_b32 v12, 0\n"
    "buffer_load_dwordx2 v[14:15], v12, s[12:15], 0 offen sc1\n"
    "v_mul_u32_u24 v8, 672, v10\n"
    "v_lshl_add_u32 v8, v11, 4, v8\n"
    "v_add_u32 v8, s97, v8\n"
    "v_lshrrev_b32 v16, 2, v0\n"
    "v_mul_u32_u24 v9, 672, v16\n"
    "v_and_b32 v16, 3, v0\n"
    "v_lshl_add_u32 v9, v16, 3, v9\n"
    "v_add_u32 v9, s97, v9\n"
    "s_waitcnt vmcnt(20)\n"
    "ds_write_b128 v8, v[20:23] offset:0\n"
    "s_waitcnt vmcnt(19)\n"
    "ds_write_b128 v8, v[24:27] offset:128\n"
    "s_waitcnt vmcnt(18)\n"
    "ds_write_b128 v8, v[28:31] offset:256\n"
    "s_waitcnt vmcnt(17)\n"
    "ds_write_b128 v8, v[32:35] offset:384\n"
    "s_waitcnt vmcnt(16)\n"
    "ds_write_b128 v8, v[36:39] offset:512\n"
    "s_waitcnt vmcnt(15)\n"
    "ds_write_b128 v8, v[40:43] offset:5376\n"
    "s_waitcnt vmcnt(14)\n"
    "ds_write_b128 v8, v[44:47] offset:5504\n"
    "s_waitcnt vmcnt(13)\n"
    "ds_write_b128 v8, v[48:51] offset:5632\n"
    "s_waitcnt vmcnt(12)\n"
    "ds_write_b128 v8, v[52:55] offset:5760\n"
    "s_waitcnt vmcnt(11)\n"
    "ds_write_b128 v8, v[56:59] offset:5888\n"
    "s_waitcnt vmcnt(10)\n"
    "ds_write_b128 v8, v[60:63] offset:10752\n"
    "s_waitcnt vmcnt(9)\n"
    "ds_write_b128 v8, v[64:67] offset:10880\n"
    "s_waitcnt vmcnt(8)\n"
    "ds_write_b128 v8, v[68:71] offset:11008\n"
    "s_waitcnt vmcnt(7)\n"
    "ds_write_b128 v8, v[72:75] offset:11136\n"
    "s_waitcnt vmcnt(6)\n"
    "ds_write_b128 v8, v[76:79] offset:11264\n"
    "s_waitcnt vmcnt(5)\n"
    "ds_write_b128 v8, v[80:83] offset:16128\n"
    "s_waitcnt vmcnt(4)\n"
    "ds_write_b128 v8, v[84:87] offset:16256\n"
    "s_waitcnt vmcnt(3)\n"
    "ds_write_b128 v8, v[88:91] offset:16384\n"
    "s_waitcnt vmcnt(2)\n"
    "ds_write_b128 v8, v[92:95] offset:16512\n"
    "s_waitcnt vmcnt(1)\n"
    "ds_write_b128 v8, v[96:99] offset:16640\n"
    "s_mul_i32 s86, s92, 1146880\n"
    "s_mul_i32 s87, s91, 5120\n"
    "s_add_u32 s86, s86, s87\n"
    "s_add_u32 s86, s86, 9850880\n"
    "ds_read_b64_tr_b16 v[120:121], v9 offset:0\n"
    "ds_read_b64_tr_b16 v[122:123], v9 offset:10752\n"
    "ds_read_b64_tr_b16 v[124:125], v9 offset:32\n"
    "ds_read_b64_tr_b16 v[126:127], v9 offset:10784\n"
    "ds_read_b64_tr_b16 v[128:129], v9 offset:64\n"
    "ds_read_b64_tr_b16 v[130:131], v9 offset:10816\n"
    "ds_read_b64_tr_b16 v[132:133], v9 offset:96\n"
    "ds_read_b64_tr_b16 v[134:135], v9 offset:10848\n"
    "ds_read_b64_tr_b16 v[136:137], v9 offset:128\n"
    "ds_read_b64_tr_b16 v[138:139], v9 offset:10880\n"
    "ds_read_b64_tr_b16 v[140:141], v9 offset:160\n"
    "ds_read_b64_tr_b16 v[142:143], v9 offset:10912\n"
    "ds_read_b64_tr_b16 v[144:145], v9 offset:192\n"
    "ds_read_b64_tr_b16 v[146:147], v9 offset:10944\n"
    "ds_read_b64_tr_b16 v[148:149], v9 offset:224\n"
    "ds_read_b64_tr_b16 v[150:151], v9 offset:10976\n"
    "ds_read_b64_tr_b16 v[152:153], v9 offset:256\n"
    "ds_read_b64_tr_b16 v[154:155], v9 offset:11008\n"
    "ds_read_b64_tr_b16 v[156:157], v9 offset:288\n"
    "ds_read_b64_tr_b16 v[158:159], v9 offset:11040\n"
    "ds_read_b64_tr_b16 v[160:161], v9 offset:320\n"
    "ds_read_b64_tr_b16 v[162:163], v9 offset:11072\n"
    "ds_read_b64_tr_b16 v[164:165], v9 offset:352\n"
    "ds_read_b64_tr_b16 v[166:167], v9 offset:11104\n"
    "ds_read_b64_tr_b16 v[168:169], v9 offset:384\n"
    "ds_read_b64_tr_b16 v[170:171], v9 offset:11136\n"
    "ds_read_b64_tr_b16 v[172:173], v9 offset:416\n"
    "ds_read_b64_tr_b16 v[174:175], v9 offset:11168\n"
    "ds_read_b64_tr_b16 v[176:177], v9 offset:448\n"
    "ds_read_b64_tr_b16 v[178:179], v9 offset:11200\n"
    "ds_read_b64_tr_b16 v[180:181], v9 offset:480\n"
    "ds_read_b64_tr_b16 v[182:183], v9 offset:11232\n"
    "ds_read_b64_tr_b16 v[184:185], v9 offset:512\n"
    "ds_read_b64_tr_b16 v[186:187], v9 offset:11264\n"
    "ds_read_b64_tr_b16 v[188:189], v9 offset:544\n"
    "ds_read_b64_tr_b16 v[190:191], v9 offset:11296\n"
    "ds_read_b64_tr_b16 v[192:193], v9 offset:576\n"
    "ds_read_b64_tr_b16 v[194:195], v9 offset:11328\n"
    "ds_read_b64_tr_b16 v[196:197], v9 offset:608\n"
    "ds_read_b64_tr_b16 v[198:199], v9 offset:11360\n"
    "s_add_u32 s87, s86, 0\n"
    "s_waitcnt lgkmcnt(15)\n"
    "buffer_store_dwordx4 v[120:123], v1, s[12:15], s87 offen offset:0 sc1\n"
    "buffer_store_dwordx4 v[124:127], v1, s[12:15], s87 offen offset:1024 sc1\n"
    "buffer_store_dwordx4 v[128:131], v1, s[12:15], s87 offen offset:2048 sc1\n"
    "buffer_store_dwordx4 v[132:135], v1, s[12:15], s87 offen offset:3072 sc1\n"
    "s_add_u32 s87, s86, 4096\n"
    "buffer_store_dwordx4 v[136:139], v1, s[12:15], s87 offen offset:0 sc1\n"
    "s_add_u32 s87, s86, 286720\n"
    "buffer_store_dwordx4 v[140:143], v1, s[12:15], s87 offen offset:0 sc1\n"
    "buffer_store_dwordx4 v[144:147], v1, s[12:15], s87 offen offset:1024 sc1\n"
    "buffer_store_dwordx4 v[148:151], v1, s[12:15], s87 offen offset:2048 sc1\n"
    "buffer_store_dwordx4 v[152:155], v1, s[12:15], s87 offen offset:3072 sc1\n"
    "s_add_u32 s87, s86, 290816\n"
    "buffer_store_dwordx4 v[156:159], v1, s[12:15], s87 offen offset:0 sc1\n"
    "s_add_u32 s87, s86, 573440\n"
    "buffer_store_dwordx4 v[160:163], v1, s[12:15], s87 offen offset:0 sc1\n"
    "buffer_store_dwordx4 v[164:167], v1, s[12:15], s87 offen offset:1024 sc1\n"
    "s_waitcnt lgkmcnt(14)\n"
    "buffer_store_dwordx4 v[168:171], v1, s[12:15], s87 offen offset:2048 sc1\n"
    "s_waitcnt lgkmcnt(12)\n"
    "buffer_store_dwordx4 v[172:175], v1, s[12:15], s87 offen offset:3072 sc1\n"
    "s_add_u32 s87, s86, 577536\n"
    "s_waitcnt lgkmcnt(10)\n"
    "buffer_store_dwordx4 v[176:179], v1, s[12:15], s87 offen offset:0 sc1\n"
    "s_add_u32 s87, s86, 860160\n"
    "s_waitcnt lgkmcnt(8)\n"
    "buffer_store_dwordx4 v[180:183], v1, s[12:15], s87 offen offset:0 sc1\n"
    "s_waitcnt lgkmcnt(6)\n"
    "buffer_store_dwordx4 v[184:187], v1, s[12:15], s87 offen offset:1024 sc1\n"
    "s_waitcnt lgkmcnt(4)\n"
    "buffer_store_dwordx4 v[188:191], v1, s[12:15], s87 offen offset:2048 sc1\n"
    "s_waitcnt lgkmcnt(2)\n"
    "buffer_store_dwordx4 v[192:195], v1, s[12:15], s87 offen offset:3072 sc1\n"
    "s_add_u32 s87, s86, 864256\n"
    "s_waitcnt lgkmcnt(0)\n"
    "buffer_store_dwordx4 v[196:199], v1, s[12:15], s87 offen offset:0 sc1\n"
    "s_mov_b32 s83, 0x7f4a7c15\n"
    "v_mov_b32 v12, 0x9e3779b9\n"
    "s_waitcnt vmcnt(20)\n"
    "v_add_co_u32 v14, vcc, s83, v14\n"
    "s_nop 1\n"
    "v_addc_co_u32 v15, vcc, v15, v12, vcc\n"
    "v_readfirstlane_b32 s60, v14\n"
    "v_readfirstlane_b32 s61, v15\n"
    "s_add_u32 s62, s60, 16\n"
    "s_addc_u32 s63, s61, 0\n"
    "s_add_u32 s90, s90, 32\n"
    "s_lshl_b32 s83, s90, 7\n"
    "s_add_u32 s83, s83, 53248\n"
    "s_lshl_b32 s84, s9, 13\n"
    "s_add_u32 s83, s83, s84\n"
    "v_mov_b32 v212, s60\n"
    "v_mov_b32 v213, s61\n"
    "v_mov_b32 v214, s60\n"
    "v_mov_b32 v215, s61\n"
    "v_add_u32 v216, 0, v1\n"
    "s_waitcnt vmcnt(0) lgkmcnt(0)\n"
    "s_barrier\n"
    "s_mov_b64 exec, 0xff\n"
    "buffer_store_dwordx4 v[212:215], v216, s[12:15], s83 offen sc1\n"
    "s_mov_b64 exec, -1\n"
    "s_waitcnt vmcnt(0) lgkmcnt(0)\n"
    "s_branch Lptail_%=\n"
    "Lpw1_%=:\n"
    "s_mov_b32 s16, s44\n"
    "s_and_b32 s17, s45, 0xffff\n"
    "s_mov_b32 s18, 2211840\n"
    "s_mov_b32 s19, 0x20000\n"
    "s_sub_u32 s90, s56, 32\n"
    "s_lshr_b32 s91, s90, 1\n"
    "s_and_b32 s92, s90, 1\n"
    "s_mul_i32 s93, s9, 3\n"
    "s_cmp_eq_u32 s9, 3\n"
    "s_cselect_b32 s93, 8, s93\n"
    "s_mul_i32 s83, s92, 10\n"
    "s_add_u32 s93, s93, s83\n"
    "v_lshrrev_b32 v6, 4, v0\n"
    "v_and_b32 v7, 15, v0\n"
    "v_mul_u32_u24 v4, 3456, v6\n"
    "v_lshl_add_u32 v4, v7, 4, v4\n"
    "s_mul_i32 s83, s91, 216\n"
    "v_add_u32 v4, s83, v4\n"
    "v_mov_b32 v8, 2147418112\n"
    "v_cmp_lt_u32 vcc, 13, v7\n"
    "s_nop 1\n"
    "v_cndmask_b32 v4, v4, v8, vcc\n"
    "s_cmp_ge_u32 s9, 2\n"
    "s_cselect_b32 s84, 2147418112, 0\n"
    "v_or_b32 v13, s84, v4\n"
    "v_or_b32 v18, s84, v1\n"
    "v_cmp_eq_u32 s[100:101], 13, v7\n"
    "v_mul_u32_u24 v5, 224, v6\n"
    "v_lshl_add_u32 v5, v7, 4, v5\n"
    "v_add_u32 v5, s97, v5\n"
    "s_mul_i32 s85, s93, 110592\n"
    "buffer_load_dwordx4 v[20:23], v4, s[16:19], s85 offen\n"
    "s_add_u32 s85, s85, 13824\n"
    "buffer_load_dwordx4 v[24:27], v4, s[16:19], s85 offen\n"
    "s_add_u32 s85, s85, 13824\n"
    "buffer_load_dwordx4 v[28:31], v4, s[16:19], s85 offen\n"
    "s_add_u32 s85, s85, 13824\n"
    "buffer_load_dwordx4 v[32:35], v4, s[16:19], s85 offen\n"
    "s_add_u32 s85, s85, 13824\n"
    "buffer_load_dwordx4 v[36:39], v4, s[16:19], s85 offen\n"
    "s_add_u32 s85, s85, 13824\n"
    "buffer_load_dwordx4 v[40:43], v4, s[16:19], s85 offen\n"
    "s_add_u32 s85, s85, 13824\n"
    "buffer_load_dwordx4 v[44:47], v4, s[16:19], s85 offen\n"
    "s_add_u32 s85, s85, 13824\n"
    "buffer_load_dwordx4 v[48:51], v4, s[16:19], s85 offen\n"
    "s_add_u32 s85, s85, 13824\n"
    "buffer_load_dwordx4 v[52:55], v4, s[16:19], s85 offen\n"
    "s_add_u32 s85, s85, 13824\n"
    "buffer_load_dwordx4 v[56:59], v4, s[16:19], s85 offen\n"
    "s_add_u32 s85, s85, 13824\n"
    "buffer_load_dwordx4 v[60:63], v4, s[16:19], s85 offen\n"
    "s_add_u32 s85, s85, 13824\n"
    "buffer_load_dwordx4 v[64:67], v4, s[16:19], s85 offen\n"
    "s_add_u32 s85, s85, 13824\n"
    "buffer_load_dwordx4 v[68:71], v4, s[16:19], s85 offen\n"
    "s_add_u32 s85, s85, 13824\n"
    "buffer_load_dwordx4 v[72:75], v4, s[16:19], s85 offen\n"
    "s_add_u32 s85, s85, 13824\n"
    "buffer_load_dwordx4 v[76:79], v4, s[16:19], s85 offen\n"
    "s_add_u32 s85, s85, 13824\n"
    "buffer_load_dwordx4 v[80:83], v4, s[16:19], s85 offen\n"
    "s_add_u32 s85, s85, 13824\n"
    "buffer_load_dwordx4 v[84:87], v13, s[16:19], s85 offen\n"
    "s_add_u32 s85, s85, 13824\n"
    "buffer_load_dwordx4 v[88:91], v13, s[16:19], s85 offen\n"
    "s_add_u32 s85, s85, 13824\n"
    "buffer_load_dwordx4 v[92:95], v13, s[16:19], s85 offen\n"
    "s_add_u32 s85, s85, 13824\n"
    "buffer_load_dwordx4 v[96:99], v13, s[16:19], s85 offen\n"
    "s_add_u32 s85, s85, 13824\n"
    "buffer_load_dwordx4 v[100:103], v13, s[16:19], s85 offen\n"
    "s_add_u32 s85, s85, 13824\n"
    "buffer_load_dwordx4 v[104:107], v13, s[16:19], s85 offen\n"
    "s_add_u32 s85, s85, 13824\n"
    "buffer_load_dwordx4 v[108:111], v13, s[16:19], s85 offen\n"
    "s_add_u32 s85, s85, 13824\n"
    "buffer_load_dwordx4 v[112:115], v13, s[16:19], s85 offen\n"
    "s_add_u32 s85, s85, 13824\n"
    "v_mov_b32 v12, 0\n"
    "buffer_load_dwordx2 v[14:15], v12, s[12:15], 0 offen sc1\n"
    "s_mov_b32 s20, s46\n"
    "s_and_b32 s21, s47, 0xffff\n"
    "s_mov_b32 s22, 3456\n"
    "s_mov_b32 s23, 0x20000\n"
    "s_mul_i32 s83, s91, 216\n"
    "s_cmp_eq_u32 s92, 0\n"
    "s_cselect_b32 s83, s83, 2147418112\n"
    "v_subrev_u32 v11, 28, v0\n"
    "v_lshl_add_u32 v10, v11, 3, s83\n"
    "v_cmp_lt_u32 vcc, 26, v11\n"
    "v_mov_b32 v11, 2147418112\n"
    "s_nop 0\n"
    "v_cndmask_b32 v10, v10, v11, vcc\n"
    "buffer_load_dwordx2 v[10:11], v10, s[20:23], 0 offen\n"
    "v_mov_b32 v8, 0x3f80\n"
    "v_lshrrev_b32 v16, 2, v0\n"
    "v_mul_u32_u24 v9, 224, v16\n"
    "v_and_b32 v16, 3, v0\n"
    "v_lshl_add_u32 v9, v16, 3, v9\n"
    "v_add_u32 v9, s97, v9\n"
    "s_mov_b32 exec_lo, 0x3fff3fff\n"
    "s_mov_b32 exec_hi, 0x3fff3fff\n"
    "s_waitcnt vmcnt(25)\n"
    "v_cndmask_b32 v22, v22, v8, s[100:101]\n"
    "v_cndmask_b32 v23, v23, 0, s[100:101]\n"
    "ds_write_b128 v5, v[20:23] offset:0\n"
    "s_waitcnt vmcnt(24)\n"
    "v_cndmask_b32 v26, v26, v8, s[100:101]\n"
    "v_cndmask_b32 v27, v27, 0, s[100:101]\n"
    "ds_write_b128 v5, v[24:27] offset:896\n"
    "s_waitcnt vmcnt(23)\n"
    "v_cndmask_b32 v30, v30, v8, s[100:101]\n"
    "v_cndmask_b32 v31, v31, 0, s[100:101]\n"
    "ds_write_b128 v5, v[28:31] offset:1792\n"
    "s_waitcnt vmcnt(22)\n"
    "v_cndmask_b32 v34, v34, v8, s[100:101]\n"
    "v_cndmask_b32 v35, v35, 0, s[100:101]\n"
    "ds_write_b128 v5, v[32:35] offset:2688\n"
    "s_waitcnt vmcnt(21)\n"
    "v_cndmask_b32 v38, v38, v8, s[100:101]\n"
    "v_cndmask_b32 v39, v39, 0, s[100:101]\n"
    "ds_write_b128 v5, v[36:39] offset:3584\n"
    "s_waitcnt vmcnt(20)\n"
    "v_cndmask_b32 v42, v42, v8, s[100:101]\n"
    "v_cndmask_b32 v43, v43, 0, s[100:101]\n"
    "ds_write_b128 v5, v[40:43] offset:4480\n"
    "s_waitcnt vmcnt(19)\n"
    "v_cndmask_b32 v46, v46, v8, s[100:101]\n"
    "v_cndmask_b32 v47, v47, 0, s[100:101]\n"
    "ds_write_b128 v5, v[44:47] offset:5376\n"
    "s_waitcnt vmcnt(18)\n"
    "v_cndmask_b32 v50, v50, v8, s[100:101]\n"
    "v_cndmask_b32 v51, v51, 0, s[100:101]\n"
    "ds_write_b128 v5, v[48:51] offset:6272\n"
    "s_waitcnt vmcnt(17)\n"
    "v_cndmask_b32 v54, v54, v8, s[100:101]\n"
    "v_cndmask_b32 v55, v55, 0, s[100:101]\n"
    "ds_write_b128 v5, v[52:55] offset:7168\n"
    "s_waitcnt vmcnt(16)\n"
    "v_cndmask_b32 v58, v58, v8, s[100:101]\n"
    "v_cndmask_b32 v59, v59, 0, s[100:101]\n"
    "ds_write_b128 v5, v[56:59] offset:8064\n"
    "s_waitcnt vmcnt(15)\n"
    "v_cndmask_b32 v62, v62, v8, s[100:101]\n"
    "v_cndmask_b32 v63, v63, 0, s[100:101]\n"
    "ds_write_b128 v5, v[60:63] offset:8960\n"
    "s_waitcnt vmcnt(14)\n"
    "v_cndmask_b32 v66, v66, v8, s[100:101]\n"
    "v_cndmask_b32 v67, v67, 0, s[100:101]\n"
    "ds_write_b128 v5, v[64:67] offset:9856\n"
    "s_waitcnt vmcnt(13)\n"
    "v_cndmask_b32 v70, v70, v8, s[100:101]\n"
    "v_cndmask_b32 v71, v71, 0, s[100:101]\n"
    "ds_write_b128 v5, v[68:71] offset:10752\n"
    "s_waitcnt vmcnt(12)\n"
    "v_cndmask_b32 v74, v74, v8, s[100:101]\n"
    "v_cndmask_b32 v75, v75, 0, s[100:101]\n"
    "ds_write_b128 v5, v[72:75] offset:11648\n"
    "s_waitcnt vmcnt(11)\n"
    "v_cndmask_b32 v78, v78, v8, s[100:101]\n"
    "v_cndmask_b32 v79, v79, 0, s[100:101]\n"
    "ds_write_b128 v5, v[76:79] offset:12544\n"
    "s_waitcnt vmcnt(10)\n"
    "v_cndmask_b32 v82, v82, v8, s[100:101]\n"
    "v_cndmask_b32 v83, v83, 0, s[100:101]\n"
    "ds_write_b128 v5, v[80:83] offset:13440\n"
    "s_waitcnt vmcnt(9)\n"
    "v_cndmask_b32 v86, v86, v8, s[100:101]\n"
    "v_cndmask_b32 v87, v87, 0, s[100:101]\n"
    "ds_write_b128 v5, v[84:87] offset:14336\n"
    "s_waitcnt vmcnt(8)\n"
    "v_cndmask_b32 v90, v90, v8, s[100:101]\n"
    "v_cndmask_b32 v91, v91, 0, s[100:101]\n"
    "ds_write_b128 v5, v[88:91] offset:15232\n"
    "s_waitcnt vmcnt(7)\n"
    "v_cndmask_b32 v94, v94, v8, s[100:101]\n"
    "v_cndmask_b32 v95, v95, 0, s[100:101]\n"
    "ds_write_b128 v5, v[92:95] offset:16128\n"
    "s_waitcnt vmcnt(6)\n"
    "v_cndmask_b32 v98, v98, v8, s[100:101]\n"
    "v_cndmask_b32 v99, v99, 0, s[100:101]\n"
    "ds_write_b128 v5, v[96:99] offset:17024\n"
    "s_waitcnt vmcnt(5)\n"
    "v_cndmask_b32 v102, v102, v8, s[100:101]\n"
    "v_cndmask_b32 v103, v103, 0, s[100:101]\n"
    "ds_write_b128 v5, v[100:103] offset:17920\n"
    "s_waitcnt vmcnt(4)\n"
    "v_cndmask_b32 v106, v106, v8, s[100:101]\n"
    "v_cndmask_b32 v107, v107, 0, s[100:101]\n"
    "ds_write_b128 v5, v[104:107] offset:18816\n"
    "s_waitcnt vmcnt(3)\n"
    "v_cndmask_b32 v110, v110, v8, s[100:101]\n"
    "v_cndmask_b32 v111, v111, 0, s[100:101]\n"
    "ds_write_b128 v5, v[108:111] offset:19712\n"
    "s_waitcnt vmcnt(2)\n"
    "v_cndmask_b32 v114, v114, v8, s[100:101]\n"
    "v_cndmask_b32 v115, v115, 0, s[100:101]\n"
    "ds_write_b128 v5, v[112:115] offset:20608\n"
    "s_mov_b64 exec, -1\n"
    "v_mov_b32 v148, 0x3f803f80\n"
    "v_mov_b32 v149, 0x3f803f80\n"
    "v_mov_b32 v150, 0x3f803f80\n"
    "v_mov_b32 v151, 0x3f803f80\n"
    "s_mul_i32 s86, s91, 143360\n"
    "s_mul_i32 s87, s93, 7168\n"
    "s_add_u32 s86, s86, s87\n"
    "s_add_u32 s86, s86, 7524352\n"
    "v_lshrrev_b32 v19, 4, v0\n"
    "v_mul_u32_u24 v19, 896, v19\n"
    "s_add_u32 s83, s93, 0\n"
    "s_mul_i32 s84, s83, 52\n"
    "s_lshr_b32 s84, s84, 8\n"
    "s_mul_i32 s84, s84, 5\n"
    "s_sub_u32 s83, s83, s84\n"
    "s_cmp_eq_u32 s83, 2\n"
    "s_cselect_b32 s83, 1, 0\n"
    "s_movk_i32 s84, 3584\n"
    "s_cselect_b32 s84, 896, s84\n"
    "v_mad_u32_u24 v16, v19, s83, v9\n"
    "v_add_u32 v17, s84, v16\n"
    "ds_read_b64_tr_b16 v[152:153], v16 offset:0\n"
    "ds_read_b64_tr_b16 v[154:155], v17 offset:0\n"
    "ds_read_b64_tr_b16 v[156:157], v16 offset:32\n"
    "ds_read_b64_tr_b16 v[158:159], v17 offset:32\n"
    "ds_read_b64_tr_b16 v[160:161], v16 offset:64\n"
    "ds_read_b64_tr_b16 v[162:163], v17 offset:64\n"
    "ds_read_b64_tr_b16 v[164:165], v16 offset:96\n"
    "ds_read_b64_tr_b16 v[166:167], v17 offset:96\n"
    "ds_read_b64_tr_b16 v[168:169], v16 offset:128\n"
    "ds_read_b64_tr_b16 v[170:171], v17 offset:128\n"
    "ds_read_b64_tr_b16 v[172:173], v16 offset:160\n"
    "ds_read_b64_tr_b16 v[174:175], v17 offset:160\n"
    "ds_read_b64_tr_b16 v[176:177], v16 offset:192\n"
    "ds_read_b64_tr_b16 v[178:179], v17 offset:192\n"
    "s_add_u32 s87, s86, 0\n"
    "s_waitcnt lgkmcnt(12)\n"
    "buffer_store_dwordx4 v[152:155], v1, s[12:15], s87 offen offset:0 sc1\n"
    "v_mfma_f32_16x16x32_bf16 v[120:123], v[152:155], v[148:151], 0\n"
    "s_waitcnt lgkmcnt(10)\n"
    "buffer_store_dwordx4 v[156:159], v1, s[12:15], s87 offen offset:1024 sc1\n"
    "v_mfma_f32_16x16x32_bf16 v[124:127], v[156:159], v[148:151], 0\n"
    "s_waitcnt lgkmcnt(8)\n"
    "buffer_store_dwordx4 v[160:163], v1, s[12:15], s87 offen offset:2048 sc1\n"
    "v_mfma_f32_16x16x32_bf16 v[128:131], v[160:163], v[148:151], 0\n"
    "s_waitcnt lgkmcnt(6)\n"
    "buffer_store_dwordx4 v[164:167], v1, s[12:15], s87 offen offset:3072 sc1\n"
    "v_mfma_f32_16x16x32_bf16 v[132:135], v[164:167], v[148:151], 0\n"
    "s_add_u32 s87, s86, 4096\n"
    "s_waitcnt lgkmcnt(4)\n"
    "buffer_store_dwordx4 v[168:171], v1, s[12:15], s87 offen offset:0 sc1\n"
    "v_mfma_f32_16x16x32_bf16 v[136:139], v[168:171], v[148:151], 0\n"
    "s_waitcnt lgkmcnt(2)\n"
    "buffer_store_dwordx4 v[172:175], v1, s[12:15], s87 offen offset:1024 sc1\n"
    "v_mfma_f32_16x16x32_bf16 v[140:143], v[172:175], v[148:151], 0\n"
    "s_waitcnt lgkmcnt(0)\n"
    "buffer_store_dwordx4 v[176:179], v1, s[12:15], s87 offen offset:2048 sc1\n"
    "v_mfma_f32_16x16x32_bf16 v[144:147], v[176:179], v[148:151], 0\n"
    "s_add_u32 s83, s93, 1\n"
    "s_mul_i32 s84, s83, 52\n"
    "s_lshr_b32 s84, s84, 8\n"
    "s_mul_i32 s84, s84, 5\n"
    "s_sub_u32 s83, s83, s84\n"
    "s_cmp_eq_u32 s83, 2\n"
    "s_cselect_b32 s83, 1, 0\n"
    "s_movk_i32 s84, 3584\n"
    "s_cselect_b32 s84, 896, s84\n"
    "v_mad_u32_u24 v16, v19, s83, v9\n"
    "v_add_u32 v17, s84, v16\n"
    "ds_read_b64_tr_b16 v[180:181], v16 offset:7168\n"
    "ds_read_b64_tr_b16 v[182:183], v17 offset:7168\n"
    "ds_read_b64_tr_b16 v[184:185], v16 offset:7200\n"
    "ds_read_b64_tr_b16 v[186:187], v17 offset:7200\n"
    "ds_read_b64_tr_b16 v[188:189], v16 offset:7232\n"
    "ds_read_b64_tr_b16 v[190:191], v17 offset:7232\n"
    "ds_read_b64_tr_b16 v[192:193], v16 offset:7264\n"
    "ds_read_b64_tr_b16 v[194:195], v17 offset:7264\n"
    "ds_read_b64_tr_b16 v[196:197], v16 offset:7296\n"
    "ds_read_b64_tr_b16 v[198:199], v17 offset:7296\n"
    "ds_read_b64_tr_b16 v[200:201], v16 offset:7328\n"
    "ds_read_b64_tr_b16 v[202:203], v17 offset:7328\n"
    "ds_read_b64_tr_b16 v[204:205], v16 offset:7360\n"
    "ds_read_b64_tr_b16 v[206:207], v17 offset:7360\n"
    "s_add_u32 s87, s86, 7168\n"
    "s_waitcnt lgkmcnt(12)\n"
    "buffer_store_dwordx4 v[180:183], v1, s[12:15], s87 offen offset:0 sc1\n"
    "v_mfma_f32_16x16x32_bf16 v[120:123], v[180:183], v[148:151], v[120:123]\n"
    "s_waitcnt lgkmcnt(10)\n"
    "buffer_store_dwordx4 v[184:187], v1, s[12:15], s87 offen offset:1024 sc1\n"
    "v_mfma_f32_16x16x32_bf16 v[124:127], v[184:187], v[148:151], v[124:127]\n"
    "s_waitcnt lgkmcnt(8)\n"
    "buffer_store_dwordx4 v[188:191], v1, s[12:15], s87 offen offset:2048 sc1\n"
    "v_mfma_f32_16x16x32_bf16 v[128:131], v[188:191], v[148:151], v[128:131]\n"
    "s_waitcnt lgkmcnt(6)\n"
    "buffer_store_dwordx4 v[192:195], v1, s[12:15], s87 offen offset:3072 sc1\n"
    "v_mfma_f32_16x16x32_bf16 v[132:135], v[192:195], v[148:151], v[132:135]\n"
    "s_add_u32 s87, s86, 11264\n"
    "s_waitcnt lgkmcnt(4)\n"
    "buffer_store_dwordx4 v[196:199], v1, s[12:15], s87 offen offset:0 sc1\n"
    "v_mfma_f32_16x16x32_bf16 v[136:139], v[196:199], v[148:151], v[136:139]\n"
    "s_waitcnt lgkmcnt(2)\n"
    "buffer_store_dwordx4 v[200:203], v1, s[12:15], s87 offen offset:1024 sc1\n"
    "v_mfma_f32_16x16x32_bf16 v[140:143], v[200:203], v[148:151], v[140:143]\n"
    "s_waitcnt lgkmcnt(0)\n"
    "buffer_store_dwordx4 v[204:207], v1, s[12:15], s87 offen offset:2048 sc1\n"
    "v_mfma_f32_16x16x32_bf16 v[144:147], v[204:207], v[148:151], v[144:147]\n"
    "s_add_u32 s83, s93, 2\n"
    "s_mul_i32 s84, s83, 52\n"
    "s_lshr_b32 s84, s84, 8\n"
    "s_mul_i32 s84, s84, 5\n"
    "s_sub_u32 s83, s83, s84\n"
    "s_cmp_eq_u32 s83, 2\n"
    "s_cselect_b32 s83, 1, 0\n"
    "s_movk_i32 s84, 3584\n"
    "s_cselect_b32 s84, 896, s84\n"
    "v_mad_u32_u24 v16, v19, s83, v9\n"
    "v_add_u32 v17, s84, v16\n"
    "ds_read_b64_tr_b16 v[152:153], v16 offset:14336\n"
    "ds_read_b64_tr_b16 v[154:155], v17 offset:14336\n"
    "ds_read_b64_tr_b16 v[156:157], v16 offset:14368\n"
    "ds_read_b64_tr_b16 v[158:159], v17 offset:14368\n"
    "ds_read_b64_tr_b16 v[160:161], v16 offset:14400\n"
    "ds_read_b64_tr_b16 v[162:163], v17 offset:14400\n"
    "ds_read_b64_tr_b16 v[164:165], v16 offset:14432\n"
    "ds_read_b64_tr_b16 v[166:167], v17 offset:14432\n"
    "ds_read_b64_tr_b16 v[168:169], v16 offset:14464\n"
    "ds_read_b64_tr_b16 v[170:171], v17 offset:14464\n"
    "ds_read_b64_tr_b16 v[172:173], v16 offset:14496\n"
    "ds_read_b64_tr_b16 v[174:175], v17 offset:14496\n"
    "ds_read_b64_tr_b16 v[176:177], v16 offset:14528\n"
    "ds_read_b64_tr_b16 v[178:179], v17 offset:14528\n"
    "s_add_u32 s87, s86, 14336\n"
    "s_waitcnt lgkmcnt(12)\n"
    "buffer_store_dwordx4 v[152:155], v18, s[12:15], s87 offen offset:0 sc1\n"
    "v_mfma_f32_16x16x32_bf16 v[120:123], v[152:155], v[148:151], v[120:123]\n"
    "s_waitcnt lgkmcnt(10)\n"
    "buffer_store_dwordx4 v[156:159], v18, s[12:15], s87 offen offset:1024 sc1\n"
    "v_mfma_f32_16x16x32_bf16 v[124:127], v[156:159], v[148:151], v[124:127]\n"
    "s_waitcnt lgkmcnt(8)\n"
    "buffer_store_dwordx4 v[160:163], v18, s[12:15], s87 offen offset:2048 sc1\n"
    "v_mfma_f32_16x16x32_bf16 v[128:131], v[160:163], v[148:151], v[128:131]\n"
    "s_waitcnt lgkmcnt(6)\n"
    "buffer_store_dwordx4 v[164:167], v18, s[12:15], s87 offen offset:3072 sc1\n"
    "v_mfma_f32_16x16x32_bf16 v[132:135], v[164:167], v[148:151], v[132:135]\n"
    "s_add_u32 s87, s86, 18432\n"
    "s_waitcnt lgkmcnt(4)\n"
    "buffer_store_dwordx4 v[168:171], v18, s[12:15], s87 offen offset:0 sc1\n"
    "v_mfma_f32_16x16x32_bf16 v[136:139], v[168:171], v[148:151], v[136:139]\n"
    "s_waitcnt lgkmcnt(2)\n"
    "buffer_store_dwordx4 v[172:175], v18, s[12:15], s87 offen offset:1024 sc1\n"
    "v_mfma_f32_16x16x32_bf16 v[140:143], v[172:175], v[148:151], v[140:143]\n"
    "s_waitcnt lgkmcnt(0)\n"
    "buffer_store_dwordx4 v[176:179], v18, s[12:15], s87 offen offset:2048 sc1\n"
    "v_mfma_f32_16x16x32_bf16 v[144:147], v[176:179], v[148:151], v[144:147]\n"
    "v_lshrrev_b32 v6, 4, v0\n"
    "v_lshlrev_b32 v6, 4, v6\n"
    "s_mul_i32 s83, s9, 448\n"
    "s_add_u32 s83, s83, 143360\n"
    "v_add_u32 v6, s83, v6\n"
    "ds_write_b128 v6, v[120:123] offset:0\n"
    "ds_write_b128 v6, v[124:127] offset:64\n"
    "ds_write_b128 v6, v[128:131] offset:128\n"
    "ds_write_b128 v6, v[132:135] offset:192\n"
    "ds_write_b128 v6, v[136:139] offset:256\n"
    "ds_write_b128 v6, v[140:143] offset:320\n"
    "ds_write_b128 v6, v[144:147] offset:384\n"
    "s_waitcnt lgkmcnt(0)\n"
    "s_barrier\n"
    "v_min_u32 v6, 27, v0\n"
    "v_lshlrev_b32 v6, 4, v6\n"
    "v_add_u32 v7, 143360, v6\n"
    "ds_read_b128 v[20:23], v7 offset:0\n"
    "ds_read_b128 v[24:27], v7 offset:448\n"
    "ds_read_b128 v[28:31], v7 offset:896\n"
    "ds_read_b128 v[32:35], v7 offset:1344\n"
    "s_mov_b32 s85, 0x3fb8aa3b\n"
    "s_waitcnt vmcnt(21)\n"
    "v_lshlrev_b32 v36, 16, v10\n"
    "v_and_b32 v37, 0xffff0000, v10\n"
    "v_lshlrev_b32 v38, 16, v11\n"
    "v_and_b32 v39, 0xffff0000, v11\n"
    "v_cmp_gt_u32 vcc, 28, v0\n"
    "s_lshl_b32 s83, s90, 10\n"
    "s_add_u32 s83, s83, 9818112\n"
    "s_waitcnt lgkmcnt(2)\n"
    "v_add_f32 v20, v20, v24\n"
    "s_waitcnt lgkmcnt(0)\n"
    "v_add_f32 v28, v28, v32\n"
    "v_add_f32 v20, v20, v28\n"
    "v_mul_f32 v36, s85, v36\n"
    "v_cndmask_b32 v20, v36, v20, vcc\n"
    "v_add_f32 v21, v21, v25\n"
    "v_add_f32 v29, v29, v33\n"
    "v_add_f32 v21, v21, v29\n"
    "v_mul_f32 v37, s85, v37\n"
    "v_cndmask_b32 v21, v37, v21, vcc\n"
    "v_add_f32 v22, v22, v26\n"
    "v_add_f32 v30, v30, v34\n"
    "v_add_f32 v22, v22, v30\n"
    "v_mul_f32 v38, s85, v38\n"
    "v_cndmask_b32 v22, v38, v22, vcc\n"
    "v_add_f32 v23, v23, v27\n"
    "v_add_f32 v31, v31, v35\n"
    "v_add_f32 v23, v23, v31\n"
    "v_mul_f32 v39, s85, v39\n"
    "v_cndmask_b32 v23, v39, v23, vcc\n"
    "buffer_store_dwordx4 v[20:23], v1, s[12:15], s83 offen sc1\n"
    "s_mov_b32 s83, 0x7f4a7c15\n"
    "v_mov_b32 v12, 0x9e3779b9\n"
    "v_add_co_u32 v14, vcc, s83, v14\n"
    "s_nop 1\n"
    "v_addc_co_u32 v15, vcc, v15, v12, vcc\n"
    "v_readfirstlane_b32 s60, v14\n"
    "v_readfirstlane_b32 s61, v15\n"
    "s_add_u32 s62, s60, 16\n"
    "s_addc_u32 s63, s61, 0\n"
    "s_lshl_b32 s83, s90, 7\n"
    "s_add_u32 s83, s83, 53248\n"
    "s_lshl_b32 s84, s9, 13\n"
    "s_add_u32 s83, s83, s84\n"
    "v_mov_b32 v212, s60\n"
    "v_mov_b32 v213, s61\n"
    "v_mov_b32 v214, s60\n"
    "v_mov_b32 v215, s61\n"
    "v_add_u32 v216, 0, v1\n"
    "s_waitcnt vmcnt(0) lgkmcnt(0)\n"
    "s_barrier\n"
    "s_mov_b64 exec, 0xff\n"
    "buffer_store_dwordx4 v[212:215], v216, s[12:15], s83 offen sc1\n"
    "s_mov_b64 exec, -1\n"
    "s_waitcnt vmcnt(0) lgkmcnt(0)\n"
    "s_branch Lptail_%=\n"
    "Lpst_%=:\n"
    "s_sleep 4\n"
    "s_mov_b32 s16, s32\n"
    "s_and_b32 s17, s33, 0xffff\n"
    "s_mov_b32 s18, 2621440\n"
    "s_mov_b32 s19, 0x20000\n"
    "v_lshrrev_b32 v6, 4, v0\n"
    "s_lshl_b32 s83, s56, 6\n"
    "s_lshl_b32 s84, s9, 4\n"
    "s_add_u32 s83, s83, s84\n"
    "v_add_u32 v6, s83, v6\n"
    "v_mul_u32_u24 v4, 1280, v6\n"
    "v_and_b32 v7, 15, v0\n"
    "v_lshl_add_u32 v4, v7, 4, v4\n"
    "s_movk_i32 s85, 0\n"
    "buffer_load_dwordx4 v[16:19], v4, s[16:19], s85 offen offset:0\n"
    "buffer_load_dwordx4 v[20:23], v4, s[16:19], s85 offen offset:256\n"
    "buffer_load_dwordx4 v[24:27], v4, s[16:19], s85 offen offset:512\n"
    "buffer_load_dwordx4 v[28:31], v4, s[16:19], s85 offen offset:768\n"
    "buffer_load_dwordx4 v[32:35], v4, s[16:19], s85 offen offset:1024\n"
    "s_movk_i32 s85, 5120\n"
    "buffer_load_dwordx4 v[36:39], v4, s[16:19], s85 offen offset:0\n"
    "buffer_load_dwordx4 v[40:43], v4, s[16:19], s85 offen offset:256\n"
    "buffer_load_dwordx4 v[44:47], v4, s[16:19], s85 offen offset:512\n"
    "buffer_load_dwordx4 v[48:51], v4, s[16:19], s85 offen offset:768\n"
    "buffer_load_dwordx4 v[52:55], v4, s[16:19], s85 offen offset:1024\n"
    "s_movk_i32 s85, 10240\n"
    "buffer_load_dwordx4 v[56:59], v4, s[16:19], s85 offen offset:0\n"
    "buffer_load_dwordx4 v[60:63], v4, s[16:19], s85 offen offset:256\n"
    "buffer_load_dwordx4 v[64:67], v4, s[16:19], s85 offen offset:512\n"
    "buffer_load_dwordx4 v[68:71], v4, s[16:19], s85 offen offset:768\n"
    "buffer_load_dwordx4 v[72:75], v4, s[16:19], s85 offen offset:1024\n"
    "s_movk_i32 s85, 15360\n"
    "buffer_load_dwordx4 v[76:79], v4, s[16:19], s85 offen offset:0\n"
    "buffer_load_dwordx4 v[80:83], v4, s[16:19], s85 offen offset:256\n"
    "buffer_load_dwordx4 v[84:87], v4, s[16:19], s85 offen offset:512\n"
    "buffer_load_dwordx4 v[88:91], v4, s[16:19], s85 offen offset:768\n"
    "buffer_load_dwordx4 v[92:95], v4, s[16:19], s85 offen offset:1024\n"
    "v_mov_b32 v12, 0\n"
    "buffer_load_dwordx2 v[14:15], v12, s[12:15], 0 offen sc1\n"
    "v_mov_b32 v8, 0x3f803f80\n"
    "s_waitcnt vmcnt(20)\n"
    "v_dot2_f32_bf16 v100, v16, v8, 0\n"
    "v_dot2_f32_bf16 v104, v16, v16, 0\n"
    "v_dot2_f32_bf16 v100, v17, v8, v100\n"
    "v_dot2_f32_bf16 v104, v17, v17, v104\n"
    "v_dot2_f32_bf16 v100, v18, v8, v100\n"
    "v_dot2_f32_bf16 v104, v18, v18, v104\n"
    "v_dot2_f32_bf16 v100, v19, v8, v100\n"
    "v_dot2_f32_bf16 v104, v19, v19, v104\n"
    "s_waitcnt vmcnt(19)\n"
    "v_dot2_f32_bf16 v100, v20, v8, v100\n"
    "v_dot2_f32_bf16 v104, v20, v20, v104\n"
    "v_dot2_f32_bf16 v100, v21, v8, v100\n"
    "v_dot2_f32_bf16 v104, v21, v21, v104\n"
    "v_dot2_f32_bf16 v100, v22, v8, v100\n"
    "v_dot2_f32_bf16 v104, v22, v22, v104\n"
    "v_dot2_f32_bf16 v100, v23, v8, v100\n"
    "v_dot2_f32_bf16 v104, v23, v23, v104\n"
    "s_waitcnt vmcnt(18)\n"
    "v_dot2_f32_bf16 v100, v24, v8, v100\n"
    "v_dot2_f32_bf16 v104, v24, v24, v104\n"
    "v_dot2_f32_bf16 v100, v25, v8, v100\n"
    "v_dot2_f32_bf16 v104, v25, v25, v104\n"
    "v_dot2_f32_bf16 v100, v26, v8, v100\n"
    "v_dot2_f32_bf16 v104, v26, v26, v104\n"
    "v_dot2_f32_bf16 v100, v27, v8, v100\n"
    "v_dot2_f32_bf16 v104, v27, v27, v104\n"
    "s_waitcnt vmcnt(17)\n"
    "v_dot2_f32_bf16 v100, v28, v8, v100\n"
    "v_dot2_f32_bf16 v104, v28, v28, v104\n"
    "v_dot2_f32_bf16 v100, v29, v8, v100\n"
    "v_dot2_f32_bf16 v104, v29, v29, v104\n"
    "v_dot2_f32_bf16 v100, v30, v8, v100\n"
    "v_dot2_f32_bf16 v104, v30, v30, v104\n"
    "v_dot2_f32_bf16 v100, v31, v8, v100\n"
    "v_dot2_f32_bf16 v104, v31, v31, v104\n"
    "s_waitcnt vmcnt(16)\n"
    "v_dot2_f32_bf16 v100, v32, v8, v100\n"
    "v_dot2_f32_bf16 v104, v32, v32, v104\n"
    "v_dot2_f32_bf16 v100, v33, v8, v100\n"
    "v_dot2_f32_bf16 v104, v33, v33, v104\n"
    "v_dot2_f32_bf16 v100, v34, v8, v100\n"
    "v_dot2_f32_bf16 v104, v34, v34, v104\n"
    "v_dot2_f32_bf16 v100, v35, v8, v100\n"
    "v_dot2_f32_bf16 v104, v35, v35, v104\n"
    "s_waitcnt vmcnt(15)\n"
    "v_dot2_f32_bf16 v101, v36, v8, 0\n"
    "v_dot2_f32_bf16 v105, v36, v36, 0\n"
    "v_dot2_f32_bf16 v101, v37, v8, v101\n"
    "v_dot2_f32_bf16 v105, v37, v37, v105\n"
    "v_dot2_f32_bf16 v101, v38, v8, v101\n"
    "v_dot2_f32_bf16 v105, v38, v38, v105\n"
    "v_dot2_f32_bf16 v101, v39, v8, v101\n"
    "v_dot2_f32_bf16 v105, v39, v39, v105\n"
    "s_waitcnt vmcnt(14)\n"
    "v_dot2_f32_bf16 v101, v40, v8, v101\n"
    "v_dot2_f32_bf16 v105, v40, v40, v105\n"
    "v_dot2_f32_bf16 v101, v41, v8, v101\n"
    "v_dot2_f32_bf16 v105, v41, v41, v105\n"
    "v_dot2_f32_bf16 v101, v42, v8, v101\n"
    "v_dot2_f32_bf16 v105, v42, v42, v105\n"
    "v_dot2_f32_bf16 v101, v43, v8, v101\n"
    "v_dot2_f32_bf16 v105, v43, v43, v105\n"
    "s_waitcnt vmcnt(13)\n"
    "v_dot2_f32_bf16 v101, v44, v8, v101\n"
    "v_dot2_f32_bf16 v105, v44, v44, v105\n"
    "v_dot2_f32_bf16 v101, v45, v8, v101\n"
    "v_dot2_f32_bf16 v105, v45, v45, v105\n"
    "v_dot2_f32_bf16 v101, v46, v8, v101\n"
    "v_dot2_f32_bf16 v105, v46, v46, v105\n"
    "v_dot2_f32_bf16 v101, v47, v8, v101\n"
    "v_dot2_f32_bf16 v105, v47, v47, v105\n"
    "s_waitcnt vmcnt(12)\n"
    "v_dot2_f32_bf16 v101, v48, v8, v101\n"
    "v_dot2_f32_bf16 v105, v48, v48, v105\n"
    "v_dot2_f32_bf16 v101, v49, v8, v101\n"
    "v_dot2_f32_bf16 v105, v49, v49, v105\n"
    "v_dot2_f32_bf16 v101, v50, v8, v101\n"
    "v_dot2_f32_bf16 v105, v50, v50, v105\n"
    "v_dot2_f32_bf16 v101, v51, v8, v101\n"
    "v_dot2_f32_bf16 v105, v51, v51, v105\n"
    "s_waitcnt vmcnt(11)\n"
    "v_dot2_f32_bf16 v101, v52, v8, v101\n"
    "v_dot2_f32_bf16 v105, v52, v52, v105\n"
    "v_dot2_f32_bf16 v101, v53, v8, v101\n"
    "v_dot2_f32_bf16 v105, v53, v53, v105\n"
    "v_dot2_f32_bf16 v101, v54, v8, v101\n"
    "v_dot2_f32_bf16 v105, v54, v54, v105\n"
    "v_dot2_f32_bf16 v101, v55, v8, v101\n"
    "v_dot2_f32_bf16 v105, v55, v55, v105\n"
    "s_waitcnt vmcnt(10)\n"
    "v_dot2_f32_bf16 v102, v56, v8, 0\n"
    "v_dot2_f32_bf16 v106, v56, v56, 0\n"
    "v_dot2_f32_bf16 v102, v57, v8, v102\n"
    "v_dot2_f32_bf16 v106, v57, v57, v106\n"
    "v_dot2_f32_bf16 v102, v58, v8, v102\n"
    "v_dot2_f32_bf16 v106, v58, v58, v106\n"
    "v_dot2_f32_bf16 v102, v59, v8, v102\n"
    "v_dot2_f32_bf16 v106, v59, v59, v106\n"
    "s_waitcnt vmcnt(9)\n"
    "v_dot2_f32_bf16 v102, v60, v8, v102\n"
    "v_dot2_f32_bf16 v106, v60, v60, v106\n"
    "v_dot2_f32_bf16 v102, v61, v8, v102\n"
    "v_dot2_f32_bf16 v106, v61, v61, v106\n"
    "v_dot2_f32_bf16 v102, v62, v8, v102\n"
    "v_dot2_f32_bf16 v106, v62, v62, v106\n"
    "v_dot2_f32_bf16 v102, v63, v8, v102\n"
    "v_dot2_f32_bf16 v106, v63, v63, v106\n"
    "s_waitcnt vmcnt(8)\n"
    "v_dot2_f32_bf16 v102, v64, v8, v102\n"
    "v_dot2_f32_bf16 v106, v64, v64, v106\n"
    "v_dot2_f32_bf16 v102, v65, v8, v102\n"
    "v_dot2_f32_bf16 v106, v65, v65, v106\n"
    "v_dot2_f32_bf16 v102, v66, v8, v102\n"
    "v_dot2_f32_bf16 v106, v66, v66, v106\n"
    "v_dot2_f32_bf16 v102, v67, v8, v102\n"
    "v_dot2_f32_bf16 v106, v67, v67, v106\n"
    "s_waitcnt vmcnt(7)\n"
    "v_dot2_f32_bf16 v102, v68, v8, v102\n"
    "v_dot2_f32_bf16 v106, v68, v68, v106\n"
    "v_dot2_f32_bf16 v102, v69, v8, v102\n"
    "v_dot2_f32_bf16 v106, v69, v69, v106\n"
    "v_dot2_f32_bf16 v102, v70, v8, v102\n"
    "v_dot2_f32_bf16 v106, v70, v70, v106\n"
    "v_dot2_f32_bf16 v102, v71, v8, v102\n"
    "v_dot2_f32_bf16 v106, v71, v71, v106\n"
    "s_waitcnt vmcnt(6)\n"
    "v_dot2_f32_bf16 v102, v72, v8, v102\n"
    "v_dot2_f32_bf16 v106, v72, v72, v106\n"
    "v_dot2_f32_bf16 v102, v73, v8, v102\n"
    "v_dot2_f32_bf16 v106, v73, v73, v106\n"
    "v_dot2_f32_bf16 v102, v74, v8, v102\n"
    "v_dot2_f32_bf16 v106, v74, v74, v106\n"
    "v_dot2_f32_bf16 v102, v75, v8, v102\n"
    "v_dot2_f32_bf16 v106, v75, v75, v106\n"
    "s_waitcnt vmcnt(5)\n"
    "v_dot2_f32_bf16 v103, v76, v8, 0\n"
    "v_dot2_f32_bf16 v107, v76, v76, 0\n"
    "v_dot2_f32_bf16 v103, v77, v8, v103\n"
    "v_dot2_f32_bf16 v107, v77, v77, v107\n"
    "v_dot2_f32_bf16 v103, v78, v8, v103\n"
    "v_dot2_f32_bf16 v107, v78, v78, v107\n"
    "v_dot2_f32_bf16 v103, v79, v8, v103\n"
    "v_dot2_f32_bf16 v107, v79, v79, v107\n"
    "s_waitcnt vmcnt(4)\n"
    "v_dot2_f32_bf16 v103, v80, v8, v103\n"
    "v_dot2_f32_bf16 v107, v80, v80, v107\n"
    "v_dot2_f32_bf16 v103, v81, v8, v103\n"
    "v_dot2_f32_bf16 v107, v81, v81, v107\n"
    "v_dot2_f32_bf16 v103, v82, v8, v103\n"
    "v_dot2_f32_bf16 v107, v82, v82, v107\n"
    "v_dot2_f32_bf16 v103, v83, v8, v103\n"
    "v_dot2_f32_bf16 v107, v83, v83, v107\n"
    "s_waitcnt vmcnt(3)\n"
    "v_dot2_f32_bf16 v103, v84, v8, v103\n"
    "v_dot2_f32_bf16 v107, v84, v84, v107\n"
    "v_dot2_f32_bf16 v103, v85, v8, v103\n"
    "v_dot2_f32_bf16 v107, v85, v85, v107\n"
    "v_dot2_f32_bf16 v103, v86, v8, v103\n"
    "v_dot2_f32_bf16 v107, v86, v86, v107\n"
    "v_dot2_f32_bf16 v103, v87, v8, v103\n"
    "v_dot2_f32_bf16 v107, v87, v87, v107\n"
    "s_waitcnt vmcnt(2)\n"
    "v_dot2_f32_bf16 v103, v88, v8, v103\n"
    "v_dot2_f32_bf16 v107, v88, v88, v107\n"
    "v_dot2_f32_bf16 v103, v89, v8, v103\n"
    "v_dot2_f32_bf16 v107, v89, v89, v107\n"
    "v_dot2_f32_bf16 v103, v90, v8, v103\n"
    "v_dot2_f32_bf16 v107, v90, v90, v107\n"
    "v_dot2_f32_bf16 v103, v91, v8, v103\n"
    "v_dot2_f32_bf16 v107, v91, v91, v107\n"
    "s_waitcnt vmcnt(1)\n"
    "v_dot2_f32_bf16 v103, v92, v8, v103\n"
    "v_dot2_f32_bf16 v107, v92, v92, v107\n"
    "v_dot2_f32_bf16 v103, v93, v8, v103\n"
    "v_dot2_f32_bf16 v107, v93, v93, v107\n"
    "v_dot2_f32_bf16 v103, v94, v8, v103\n"
    "v_dot2_f32_bf16 v107, v94, v94, v107\n"
    "v_dot2_f32_bf16 v103, v95, v8, v103\n"
    "v_dot2_f32_bf16 v107, v95, v95, v107\n"
    "v_add_f32_dpp v100, v100, v100 row_ror:8 row_mask:0xf bank_mask:0xf\n"
    "v_add_f32_dpp v101, v101, v101 row_ror:8 row_mask:0xf bank_mask:0xf\n"
    "v_add_f32_dpp v102, v102, v102 row_ror:8 row_mask:0xf bank_mask:0xf\n"
    "v_add_f32_dpp v103, v103, v103 row_ror:8 row_mask:0xf bank_mask:0xf\n"
    "v_add_f32_dpp v104, v104, v104 row_ror:8 row_mask:0xf bank_mask:0xf\n"
    "v_add_f32_dpp v105, v105, v105 row_ror:8 row_mask:0xf bank_mask:0xf\n"
    "v_add_f32_dpp v106, v106, v106 row_ror:8 row_mask:0xf bank_mask:0xf\n"
    "v_add_f32_dpp v107, v107, v107 row_ror:8 row_mask:0xf bank_mask:0xf\n"
    "v_add_f32_dpp v100, v100, v100 row_ror:4 row_mask:0xf bank_mask:0xf\n"
    "v_add_f32_dpp v101, v101, v101 row_ror:4 row_mask:0xf bank_mask:0xf\n"
    "v_add_f32_dpp v102, v102, v102 row_ror:4 row_mask:0xf bank_mask:0xf\n"
    "v_add_f32_dpp v103, v103, v103 row_ror:4 row_mask:0xf bank_mask:0xf\n"
    "v_add_f32_dpp v104, v104, v104 row_ror:4 row_mask:0xf bank_mask:0xf\n"
    "v_add_f32_dpp v105, v105, v105 row_ror:4 row_mask:0xf bank_mask:0xf\n"
    "v_add_f32_dpp v106, v106, v106 row_ror:4 row_mask:0xf bank_mask:0xf\n"
    "v_add_f32_dpp v107, v107, v107 row_ror:4 row_mask:0xf bank_mask:0xf\n"
    "v_add_f32_dpp v100, v100, v100 row_ror:2 row_mask:0xf bank_mask:0xf\n"
    "v_add_f32_dpp v101, v101, v101 row_ror:2 row_mask:0xf bank_mask:0xf\n"
    "v_add_f32_dpp v102, v102, v102 row_ror:2 row_mask:0xf bank_mask:0xf\n"
    "v_add_f32_dpp v103, v103, v103 row_ror:2 row_mask:0xf bank_mask:0xf\n"
    "v_add_f32_dpp v104, v104, v104 row_ror:2 row_mask:0xf bank_mask:0xf\n"
    "v_add_f32_dpp v105, v105, v105 row_ror:2 row_mask:0xf bank_mask:0xf\n"
    "v_add_f32_dpp v106, v106, v106 row_ror:2 row_mask:0xf bank_mask:0xf\n"
    "v_add_f32_dpp v107, v107, v107 row_ror:2 row_mask:0xf bank_mask:0xf\n"
    "v_add_f32_dpp v100, v100, v100 row_ror:1 row_mask:0xf bank_mask:0xf\n"
    "v_add_f32_dpp v101, v101, v101 row_ror:1 row_mask:0xf bank_mask:0xf\n"
    "v_add_f32_dpp v102, v102, v102 row_ror:1 row_mask:0xf bank_mask:0xf\n"
    "v_add_f32_dpp v103, v103, v103 row_ror:1 row_mask:0xf bank_mask:0xf\n"
    "v_add_f32_dpp v104, v104, v104 row_ror:1 row_mask:0xf bank_mask:0xf\n"
    "v_add_f32_dpp v105, v105, v105 row_ror:1 row_mask:0xf bank_mask:0xf\n"
    "v_add_f32_dpp v106, v106, v106 row_ror:1 row_mask:0xf bank_mask:0xf\n"
    "v_add_f32_dpp v107, v107, v107 row_ror:1 row_mask:0xf bank_mask:0xf\n"
    "s_mov_b32 s85, 0x3acccccd\n"
    "v_mov_b32 v8, 0x3727c5ac\n"
    "v_mul_f32 v100, s85, v100\n"
    "v_mul_f32 v116, v100, v100\n"
    "v_fma_f32 v116, v104, s85, -v116\n"
    "v_max_f32 v116, 0, v116\n"
    "v_add_f32 v116, v8, v116\n"
    "v_rsq_f32 v116, v116\n"
    "s_nop 0\n"
    "v_mul_f32 v117, -v100, v116\n"
    "v_mul_f32 v101, s85, v101\n"
    "v_mul_f32 v118, v101, v101\n"
    "v_fma_f32 v118, v105, s85, -v118\n"
    "v_max_f32 v118, 0, v118\n"
    "v_add_f32 v118, v8, v118\n"
    "v_rsq_f32 v118, v118\n"
    "s_nop 0\n"
    "v_mul_f32 v119, -v101, v118\n"
    "v_mul_f32 v102, s85, v102\n"
    "v_mul_f32 v120, v102, v102\n"
    "v_fma_f32 v120, v106, s85, -v120\n"
    "v_max_f32 v120, 0, v120\n"
    "v_add_f32 v120, v8, v120\n"
    "v_rsq_f32 v120, v120\n"
    "s_nop 0\n"
    "v_mul_f32 v121, -v102, v120\n"
    "v_mul_f32 v103, s85, v103\n"
    "v_mul_f32 v122, v103, v103\n"
    "v_fma_f32 v122, v107, s85, -v122\n"
    "v_max_f32 v122, 0, v122\n"
    "v_add_f32 v122, v8, v122\n"
    "v_rsq_f32 v122, v122\n"
    "s_nop 0\n"
    "v_mul_f32 v123, -v103, v122\n"
    "s_and_b32 s85, s9, 1\n"
    "s_lshl_b32 s85, s85, 8\n"
    "s_lshr_b32 s86, s9, 1\n"
    "s_lshl_b32 s86, s86, 3\n"
    "s_add_u32 s85, s85, s86\n"
    "v_lshrrev_b32 v6, 4, v0\n"
    "v_lshlrev_b32 v6, 2, v6\n"
    "v_and_b32 v8, 7, v0\n"
    "v_lshl_add_u32 v9, v7, 10, s85\n"
    "v_add_u32 v108, 0, v6\n"
    "v_xor_b32 v108, v108, v8\n"
    "v_lshl_add_u32 v108, v108, 4, v9\n"
    "v_add_u32 v109, 1, v6\n"
    "v_xor_b32 v109, v109, v8\n"
    "v_lshl_add_u32 v109, v109, 4, v9\n"
    "v_add_u32 v110, 2, v6\n"
    "v_xor_b32 v110, v110, v8\n"
    "v_lshl_add_u32 v110, v110, 4, v9\n"
    "v_add_u32 v111, 3, v6\n"
    "v_xor_b32 v111, v111, v8\n"
    "v_lshl_add_u32 v111, v111, 4, v9\n"
    "ds_write_b64 v108, v[16:17] offset:0\n"
    "ds_write_b64 v108, v[18:19] offset:512\n"
    "ds_write_b64 v109, v[36:37] offset:0\n"
    "ds_write_b64 v109, v[38:39] offset:512\n"
    "ds_write_b64 v110, v[56:57] offset:0\n"
    "ds_write_b64 v110, v[58:59] offset:512\n"
    "ds_write_b64 v111, v[76:77] offset:0\n"
    "ds_write_b64 v111, v[78:79] offset:512\n"
    "ds_write_b64 v108, v[20:21] offset:16384\n"
    "ds_write_b64 v108, v[22:23] offset:16896\n"
    "ds_write_b64 v109, v[40:41] offset:16384\n"
    "ds_write_b64 v109, v[42:43] offset:16896\n"
    "ds_write_b64 v110, v[60:61] offset:16384\n"
    "ds_write_b64 v110, v[62:63] offset:16896\n"
    "ds_write_b64 v111, v[80:81] offset:16384\n"
    "ds_write_b64 v111, v[82:83] offset:16896\n"
    "ds_write_b64 v108, v[24:25] offset:32768\n"
    "ds_write_b64 v108, v[26:27] offset:33280\n"
    "ds_write_b64 v109, v[44:45] offset:32768\n"
    "ds_write_b64 v109, v[46:47] offset:33280\n"
    "ds_write_b64 v110, v[64:65] offset:32768\n"
    "ds_write_b64 v110, v[66:67] offset:33280\n"
    "ds_write_b64 v111, v[84:85] offset:32768\n"
    "ds_write_b64 v111, v[86:87] offset:33280\n"
    "ds_write_b64 v108, v[28:29] offset:49152\n"
    "ds_write_b64 v108, v[30:31] offset:49664\n"
    "ds_write_b64 v109, v[48:49] offset:49152\n"
    "ds_write_b64 v109, v[50:51] offset:49664\n"
    "ds_write_b64 v110, v[68:69] offset:49152\n"
    "ds_write_b64 v110, v[70:71] offset:49664\n"
    "ds_write_b64 v111, v[88:89] offset:49152\n"
    "ds_write_b64 v111, v[90:91] offset:49664\n"
    "v_add_u32 v108, 65536, v108\n"
    "ds_write_b64 v108, v[32:33] offset:0\n"
    "ds_write_b64 v108, v[34:35] offset:512\n"
    "v_add_u32 v109, 65536, v109\n"
    "ds_write_b64 v109, v[52:53] offset:0\n"
    "ds_write_b64 v109, v[54:55] offset:512\n"
    "v_add_u32 v110, 65536, v110\n"
    "ds_write_b64 v110, v[72:73] offset:0\n"
    "ds_write_b64 v110, v[74:75] offset:512\n"
    "v_add_u32 v111, 65536, v111\n"
    "ds_write_b64 v111, v[92:93] offset:0\n"
    "ds_write_b64 v111, v[94:95] offset:512\n"
    "s_mov_b32 exec_lo, 0x00010001\n"
    "s_mov_b32 exec_hi, 0x00010001\n"
    "ds_write_b64 v108, v[116:117] offset:16384\n"
    "ds_write_b64 v109, v[118:119] offset:16384\n"
    "ds_write_b64 v110, v[120:121] offset:16384\n"
    "ds_write_b64 v111, v[122:123] offset:16384\n"
    "s_mov_b64 exec, -1\n"
    "v_and_b32 v6, 15, v0\n"
    "v_lshrrev_b32 v7, 4, v0\n"
    "s_mul_i32 s85, s9, 20480\n"
    "v_lshl_add_u32 v7, v7, 8, s85\n"
    "v_lshrrev_b32 v9, 5, v0\n"
    "v_lshlrev_b32 v9, 14, v9\n"
    "v_and_b32 v8, 31, v0\n"
    "v_lshl_add_u32 v9, v8, 4, v9\n"
    "s_mul_i32 s86, s9, 655360\n"
    "s_lshl_b32 s87, s56, 9\n"
    "s_add_u32 s86, s86, s87\n"
    "s_add_u32 s86, s86, 2789376\n"
    "s_lshl_b32 s88, s9, 2\n"
    "s_waitcnt lgkmcnt(0)\n"
    "s_barrier\n"
    "s_add_u32 s85, s88, 0\n"
    "s_and_b32 s85, s85, 7\n"
    "v_xor_b32 v8, s85, v6\n"
    "v_lshl_add_u32 v8, v8, 4, v7\n"
    "ds_read_b128 v[16:19], v8 offset:0\n"
    "s_add_u32 s85, s88, 1\n"
    "s_and_b32 s85, s85, 7\n"
    "v_xor_b32 v8, s85, v6\n"
    "v_lshl_add_u32 v8, v8, 4, v7\n"
    "ds_read_b128 v[20:23], v8 offset:1024\n"
    "s_add_u32 s85, s88, 2\n"
    "s_and_b32 s85, s85, 7\n"
    "v_xor_b32 v8, s85, v6\n"
    "v_lshl_add_u32 v8, v8, 4, v7\n"
    "ds_read_b128 v[24:27], v8 offset:2048\n"
    "s_add_u32 s85, s88, 3\n"
    "s_and_b32 s85, s85, 7\n"
    "v_xor_b32 v8, s85, v6\n"
    "v_lshl_add_u32 v8, v8, 4, v7\n"
    "ds_read_b128 v[28:31], v8 offset:3072\n"
    "s_add_u32 s85, s88, 4\n"
    "s_and_b32 s85, s85, 7\n"
    "v_xor_b32 v8, s85, v6\n"
    "v_lshl_add_u32 v8, v8, 4, v7\n"
    "ds_read_b128 v[32:35], v8 offset:4096\n"
    "s_add_u32 s85, s88, 5\n"
    "s_and_b32 s85, s85, 7\n"
    "v_xor_b32 v8, s85, v6\n"
    "v_lshl_add_u32 v8, v8, 4, v7\n"
    "ds_read_b128 v[36:39], v8 offset:5120\n"
    "s_add_u32 s85, s88, 6\n"
    "s_and_b32 s85, s85, 7\n"
    "v_xor_b32 v8, s85, v6\n"
    "v_lshl_add_u32 v8, v8, 4, v7\n"
    "ds_read_b128 v[40:43], v8 offset:6144\n"
    "s_add_u32 s85, s88, 7\n"
    "s_and_b32 s85, s85, 7\n"
    "v_xor_b32 v8, s85, v6\n"
    "v_lshl_add_u32 v8, v8, 4, v7\n"
    "ds_read_b128 v[44:47], v8 offset:7168\n"
    "s_add_u32 s85, s88, 8\n"
    "s_and_b32 s85, s85, 7\n"
    "v_xor_b32 v8, s85, v6\n"
    "v_lshl_add_u32 v8, v8, 4, v7\n"
    "ds_read_b128 v[48:51], v8 offset:8192\n"
    "s_add_u32 s85, s88, 9\n"
    "s_and_b32 s85, s85, 7\n"
    "v_xor_b32 v8, s85, v6\n"
    "v_lshl_add_u32 v8, v8, 4, v7\n"
    "ds_read_b128 v[52:55], v8 offset:9216\n"
    "s_add_u32 s85, s88, 10\n"
    "s_and_b32 s85, s85, 7\n"
    "v_xor_b32 v8, s85, v6\n"
    "v_lshl_add_u32 v8, v8, 4, v7\n"
    "ds_read_b128 v[56:59], v8 offset:10240\n"
    "s_add_u32 s85, s88, 11\n"
    "s_and_b32 s85, s85, 7\n"
    "v_xor_b32 v8, s85, v6\n"
    "v_lshl_add_u32 v8, v8, 4, v7\n"
    "ds_read_b128 v[60:63], v8 offset:11264\n"
    "s_add_u32 s85, s88, 12\n"
    "s_and_b32 s85, s85, 7\n"
    "v_xor_b32 v8, s85, v6\n"
    "v_lshl_add_u32 v8, v8, 4, v7\n"
    "ds_read_b128 v[64:67], v8 offset:12288\n"
    "s_add_u32 s85, s88, 13\n"
    "s_and_b32 s85, s85, 7\n"
    "v_xor_b32 v8, s85, v6\n"
    "v_lshl_add_u32 v8, v8, 4, v7\n"
    "ds_read_b128 v[68:71], v8 offset:13312\n"
    "s_add_u32 s85, s88, 14\n"
    "s_and_b32 s85, s85, 7\n"
    "v_xor_b32 v8, s85, v6\n"
    "v_lshl_add_u32 v8, v8, 4, v7\n"
    "ds_read_b128 v[72:75], v8 offset:14336\n"
    "s_add_u32 s85, s88, 15\n"
    "s_and_b32 s85, s85, 7\n"
    "v_xor_b32 v8, s85, v6\n"
    "v_lshl_add_u32 v8, v8, 4, v7\n"
    "ds_read_b128 v[76:79], v8 offset:15360\n"
    "s_add_u32 s85, s88, 16\n"
    "s_and_b32 s85, s85, 7\n"
    "v_xor_b32 v8, s85, v6\n"
    "v_lshl_add_u32 v8, v8, 4, v7\n"
    "ds_read_b128 v[80:83], v8 offset:16384\n"
    "s_add_u32 s85, s88, 17\n"
    "s_and_b32 s85, s85, 7\n"
    "v_xor_b32 v8, s85, v6\n"
    "v_lshl_add_u32 v8, v8, 4, v7\n"
    "ds_read_b128 v[84:87], v8 offset:17408\n"
    "s_add_u32 s85, s88, 18\n"
    "s_and_b32 s85, s85, 7\n"
    "v_xor_b32 v8, s85, v6\n"
    "v_lshl_add_u32 v8, v8, 4, v7\n"
    "ds_read_b128 v[88:91], v8 offset:18432\n"
    "s_add_u32 s85, s88, 19\n"
    "s_and_b32 s85, s85, 7\n"
    "v_xor_b32 v8, s85, v6\n"
    "v_lshl_add_u32 v8, v8, 4, v7\n"
    "ds_read_b128 v[92:95], v8 offset:19456\n"
    "v_add_u32 v8, 81920, v1\n"
    "ds_read_b128 v[112:115], v8\n"
    "s_waitcnt lgkmcnt(15)\n"
    "buffer_store_dwordx4 v[16:19], v9, s[12:15], s86 offen sc1\n"
    "s_add_u32 s86, s86, 32768\n"
    "buffer_store_dwordx4 v[20:23], v9, s[12:15], s86 offen sc1\n"
    "s_add_u32 s86, s86, 32768\n"
    "buffer_store_dwordx4 v[24:27], v9, s[12:15], s86 offen sc1\n"
    "s_add_u32 s86, s86, 32768\n"
    "buffer_store_dwordx4 v[28:31], v9, s[12:15], s86 offen sc1\n"
    "s_add_u32 s86, s86, 32768\n"
    "buffer_store_dwordx4 v[32:35], v9, s[12:15], s86 offen sc1\n"
    "s_add_u32 s86, s86, 32768\n"
    "buffer_store_dwordx4 v[36:39], v9, s[12:15], s86 offen sc1\n"
    "s_add_u32 s86, s86, 32768\n"
    "s_waitcnt lgkmcnt(14)\n"
    "buffer_store_dwordx4 v[40:43], v9, s[12:15], s86 offen sc1\n"
    "s_add_u32 s86, s86, 32768\n"
    "s_waitcnt lgkmcnt(13)\n"
    "buffer_store_dwordx4 v[44:47], v9, s[12:15], s86 offen sc1\n"
    "s_add_u32 s86, s86, 32768\n"
    "s_waitcnt lgkmcnt(12)\n"
    "buffer_store_dwordx4 v[48:51], v9, s[12:15], s86 offen sc1\n"
    "s_add_u32 s86, s86, 32768\n"
    "s_waitcnt lgkmcnt(11)\n"
    "buffer_store_dwordx4 v[52:55], v9, s[12:15], s86 offen sc1\n"
    "s_add_u32 s86, s86, 32768\n"
    "s_waitcnt lgkmcnt(10)\n"
    "buffer_store_dwordx4 v[56:59], v9, s[12:15], s86 offen sc1\n"
    "s_add_u32 s86, s86, 32768\n"
    "s_waitcnt lgkmcnt(9)\n"
    "buffer_store_dwordx4 v[60:63], v9, s[12:15], s86 offen sc1\n"
    "s_add_u32 s86, s86, 32768\n"
    "s_waitcnt lgkmcnt(8)\n"
    "buffer_store_dwordx4 v[64:67], v9, s[12:15], s86 offen sc1\n"
    "s_add_u32 s86, s86, 32768\n"
    "s_waitcnt lgkmcnt(7)\n"
    "buffer_store_dwordx4 v[68:71], v9, s[12:15], s86 offen sc1\n"
    "s_add_u32 s86, s86, 32768\n"
    "s_waitcnt lgkmcnt(6)\n"
    "buffer_store_dwordx4 v[72:75], v9, s[12:15], s86 offen sc1\n"
    "s_add_u32 s86, s86, 32768\n"
    "s_waitcnt lgkmcnt(5)\n"
    "buffer_store_dwordx4 v[76:79], v9, s[12:15], s86 offen sc1\n"
    "s_add_u32 s86, s86, 32768\n"
    "s_waitcnt lgkmcnt(4)\n"
    "buffer_store_dwordx4 v[80:83], v9, s[12:15], s86 offen sc1\n"
    "s_add_u32 s86, s86, 32768\n"
    "s_waitcnt lgkmcnt(3)\n"
    "buffer_store_dwordx4 v[84:87], v9, s[12:15], s86 offen sc1\n"
    "s_add_u32 s86, s86, 32768\n"
    "s_waitcnt lgkmcnt(2)\n"
    "buffer_store_dwordx4 v[88:91], v9, s[12:15], s86 offen sc1\n"
    "s_add_u32 s86, s86, 32768\n"
    "s_waitcnt lgkmcnt(1)\n"
    "buffer_store_dwordx4 v[92:95], v9, s[12:15], s86 offen sc1\n"
    "s_add_u32 s86, s86, 32768\n"
    "s_add_u32 s87, s87, 7507968\n"
    "s_mov_b32 exec_hi, 0\n"
    "s_waitcnt lgkmcnt(0)\n"
    "buffer_store_dwordx4 v[112:115], v1, s[12:15], s87 offen sc1\n"
    "s_mov_b64 exec, -1\n"
    "s_mov_b32 s83, 0x7f4a7c15\n"
    "v_mov_b32 v12, 0x9e3779b9\n"
    "s_waitcnt vmcnt(21)\n"
    "v_add_co_u32 v14, vcc, s83, v14\n"
    "s_nop 1\n"
    "v_addc_co_u32 v15, vcc, v15, v12, vcc\n"
    "v_readfirstlane_b32 s60, v14\n"
    "v_readfirstlane_b32 s61, v15\n"
    "s_add_u32 s62, s60, 16\n"
    "s_addc_u32 s63, s61, 0\n"
    "s_lshl_b32 s83, s56, 7\n"
    "s_add_u32 s83, s83, 49152\n"
    "s_cmp_eq_u32 s9, 0\n"
    "s_cselect_b32 s84, 0, 2147418112\n"
    "v_mov_b32 v212, s60\n"
    "v_mov_b32 v213, s61\n"
    "v_mov_b32 v214, s60\n"
    "v_mov_b32 v215, s61\n"
    "v_add_u32 v216, s84, v1\n"
    "s_waitcnt vmcnt(0) lgkmcnt(0)\n"
    "s_barrier\n"
    "s_mov_b64 exec, 0xff\n"
    "buffer_store_dwordx4 v[212:215], v216, s[12:15], s83 offen sc1\n"
    "s_mov_b64 exec, -1\n"
    "s_waitcnt vmcnt(0) lgkmcnt(0)\n"
    "s_branch Lptail_%=\n"
    "Lpidle_%=:\n"
    "s_sub_u32 s83, s56, 92\n"
    "s_lshl_b32 s83, s83, 2\n"
    "s_add_u32 s83, s83, s9\n"
    "s_lshl_b32 s83, s83, 13\n"
    "s_add_u32 s83, s83, 8192\n"
    "v_lshlrev_b32 v16, 7, v0\n"
    "buffer_load_dword v17, v16, s[12:15], s83 offen sc1\n"
    "v_mov_b32 v12, 0\n"
    "buffer_load_dwordx2 v[14:15], v12, s[12:15], 0 offen sc1\n"
    "s_mov_b32 s83, 0x7f4a7c15\n"
    "v_mov_b32 v12, 0x9e3779b9\n"
    "s_waitcnt vmcnt(0)\n"
    "v_add_co_u32 v14, vcc, s83, v14\n"
    "s_nop 1\n"
    "v_addc_co_u32 v15, vcc, v15, v12, vcc\n"
    "v_readfirstlane_b32 s60, v14\n"
    "v_readfirstlane_b32 s61, v15\n"
    "s_add_u32 s62, s60, 16\n"
    "s_addc_u32 s63, s61, 0\n"
    "s_sub_u32 s90, s56, 32\n"
    "s_lshl_b32 s83, s90, 7\n"
    "s_add_u32 s83, s83, 53248\n"
    "s_lshl_b32 s84, s9, 13\n"
    "s_add_u32 s83, s83, s84\n"
    "v_mov_b32 v212, s60\n"
    "v_mov_b32 v213, s61\n"
    "v_mov_b32 v214, s60\n"
    "v_mov_b32 v215, s61\n"
    "v_add_u32 v216, 0, v1\n"
    "s_waitcnt vmcnt(0) lgkmcnt(0)\n"
    "s_barrier\n"
    "s_mov_b64 exec, 0xff\n"
    "buffer_store_dwordx4 v[212:215], v216, s[12:15], s83 offen sc1\n"
    "s_mov_b64 exec, -1\n"
    "s_waitcnt vmcnt(0) lgkmcnt(0)\n"
    "Lptail_%=:\n"
    "s_mul_i32 s83, s9, 14\n"
    "s_and_b32 s85, s55, 7\n"
    "s_mul_i32 s85, s85, 14\n"
    "s_lshr_b32 s85, s85, 3\n"
    "s_add_u32 s86, s85, s83\n"
    "s_lshl_b32 s64, s86, 10\n"
    "s_add_u32 s85, s85, 1\n"
    "s_cmp_ge_u32 s85, 14\n"
    "s_cselect_b32 s85, 0, s85\n"
    "s_add_u32 s86, s85, s83\n"
    "s_lshl_b32 s65, s86, 10\n"
    "s_add_u32 s85, s85, 1\n"
    "s_cmp_ge_u32 s85, 14\n"
    "s_cselect_b32 s85, 0, s85\n"
    "s_add_u32 s86, s85, s83\n"
    "s_lshl_b32 s66, s86, 10\n"
    "s_add_u32 s85, s85, 1\n"
    "s_cmp_ge_u32 s85, 14\n"
    "s_cselect_b32 s85, 0, s85\n"
    "s_add_u32 s86, s85, s83\n"
    "s_lshl_b32 s67, s86, 10\n"
    "s_add_u32 s85, s85, 1\n"
    "s_cmp_ge_u32 s85, 14\n"
    "s_cselect_b32 s85, 0, s85\n"
    "s_add_u32 s86, s85, s83\n"
    "s_lshl_b32 s68, s86, 10\n"
    "s_add_u32 s85, s85, 1\n"
    "s_cmp_ge_u32 s85, 14\n"
    "s_cselect_b32 s85, 0, s85\n"
    "s_add_u32 s86, s85, s83\n"
    "s_lshl_b32 s69, s86, 10\n"
    "s_add_u32 s85, s85, 1\n"
    "s_cmp_ge_u32 s85, 14\n"
    "s_cselect_b32 s85, 0, s85\n"
    "s_add_u32 s86, s85, s83\n"
    "s_lshl_b32 s70, s86, 10\n"
    "s_add_u32 s85, s85, 1\n"
    "s_cmp_ge_u32 s85, 14\n"
    "s_cselect_b32 s85, 0, s85\n"
    "s_add_u32 s86, s85, s83\n"
    "s_lshl_b32 s71, s86, 10\n"
    "s_add_u32 s85, s85, 1\n"
    "s_cmp_ge_u32 s85, 14\n"
    "s_cselect_b32 s85, 0, s85\n"
    "s_add_u32 s86, s85, s83\n"
    "s_lshl_b32 s72, s86, 10\n"
    "s_add_u32 s85, s85, 1\n"
    "s_cmp_ge_u32 s85, 14\n"
    "s_cselect_b32 s85, 0, s85\n"
    "s_add_u32 s86, s85, s83\n"
    "s_lshl_b32 s73, s86, 10\n"
    "s_add_u32 s85, s85, 1\n"
    "s_cmp_ge_u32 s85, 14\n"
    "s_cselect_b32 s85, 0, s85\n"
    "s_add_u32 s86, s85, s83\n"
    "s_lshl_b32 s74, s86, 10\n"
    "s_add_u32 s85, s85, 1\n"
    "s_cmp_ge_u32 s85, 14\n"
    "s_cselect_b32 s85, 0, s85\n"
    "s_add_u32 s86, s85, s83\n"
    "s_lshl_b32 s75, s86, 10\n"
    "s_add_u32 s85, s85, 1\n"
    "s_cmp_ge_u32 s85, 14\n"
    "s_cselect_b32 s85, 0, s85\n"
    "s_add_u32 s86, s85, s83\n"
    "s_lshl_b32 s76, s86, 10\n"
    "s_add_u32 s85, s85, 1\n"
    "s_cmp_ge_u32 s85, 14\n"
    "s_cselect_b32 s85, 0, s85\n"
    "s_add_u32 s86, s85, s83\n"
    "s_lshl_b32 s77, s86, 10\n"
    "s_mul_i32 s85, s5, 52\n"
    "s_lshr_b32 s85, s85, 8\n"
    "s_mul_i32 s85, s85, 5\n"
    "s_sub_u32 s85, s5, s85\n"
    "s_lshl_b32 s78, s85, 12\n"
    "s_add_u32 s85, s85, 1\n"
    "s_cmp_ge_u32 s85, 5\n"
    "s_cselect_b32 s85, 0, s85\n"
    "s_lshl_b32 s79, s85, 12\n"
    "s_add_u32 s85, s85, 1\n"
    "s_cmp_ge_u32 s85, 5\n"
    "s_cselect_b32 s85, 0, s85\n"
    "s_lshl_b32 s80, s85, 12\n"
    "s_add_u32 s85, s85, 1\n"
    "s_cmp_ge_u32 s85, 5\n"
    "s_cselect_b32 s85, 0, s85\n"
    "s_lshl_b32 s81, s85, 12\n"
    "s_add_u32 s85, s85, 1\n"
    "s_cmp_ge_u32 s85, 5\n"
    "s_cselect_b32 s85, 0, s85\n"
    "s_lshl_b32 s82, s85, 12\n"
    "v_min_u32 v11, 63, v0\n"
    "v_lshlrev_b32 v11, 7, v11\n"
    "s_lshr_b32 s90, s11, 1\n"
    "s_lshl_b32 s90, s90, 13\n"
    "s_add_u32 s90, s90, 53248\n"
    "s_cmp_lg_u32 s9, 0\n"
    "s_cbranch_scc1 Lh3skip_2_%=\n"
    "buffer_load_dwordx2 v[16:17], v11, s[12:15], s90 offen offset:0 sc1\n"
    "s_waitcnt vmcnt(0)\n"
    "v_cmp_ne_u64 vcc, s[60:61], v[16:17]\n"
    "s_cbranch_vccnz Lslow_3_%=\n"
    "Lback_4_%=:\n"
    "Lh3skip_2_%=:\n"
    "s_barrier\n"
    "s_and_b32 s88, s55, 7\n"
    "s_mul_i32 s88, s88, 286720\n"
    "s_add_u32 s88, s88, 9850880\n"
    "v_add_u32 v228, 4096, v1\n"
    "s_mul_i32 s89, s64, 5\n"
    "s_add_u32 s89, s89, s88\n"
    "buffer_load_dwordx4 a[0:3], v1, s[12:15], s89 offen offset:0 sc1\n"
    "buffer_load_dwordx4 a[4:7], v1, s[12:15], s89 offen offset:1024 sc1\n"
    "buffer_load_dwordx4 a[8:11], v1, s[12:15], s89 offen offset:2048 sc1\n"
    "buffer_load_dwordx4 a[12:15], v1, s[12:15], s89 offen offset:3072 sc1\n"
    "buffer_load_dwordx4 a[16:19], v228, s[12:15], s89 offen offset:0 sc1\n"
    "s_mul_i32 s89, s65, 5\n"
    "s_add_u32 s89, s89, s88\n"
    "buffer_load_dwordx4 a[20:23], v1, s[12:15], s89 offen offset:0 sc1\n"
    "buffer_load_dwordx4 a[24:27], v1, s[12:15], s89 offen offset:1024 sc1\n"
    "buffer_load_dwordx4 a[28:31], v1, s[12:15], s89 offen offset:2048 sc1\n"
    "buffer_load_dwordx4 a[32:35], v1, s[12:15], s89 offen offset:3072 sc1\n"
    "buffer_load_dwordx4 a[36:39], v228, s[12:15], s89 offen offset:0 sc1\n"
    "s_mul_i32 s89, s66, 5\n"
    "s_add_u32 s89, s89, s88\n"
    "buffer_load_dwordx4 a[40:43], v1, s[12:15], s89 offen offset:0 sc1\n"
    "buffer_load_dwordx4 a[44:47], v1, s[12:15], s89 offen offset:1024 sc1\n"
    "buffer_load_dwordx4 a[48:51], v1, s[12:15], s89 offen offset:2048 sc1\n"
    "buffer_load_dwordx4 a[52:55], v1, s[12:15], s89 offen offset:3072 sc1\n"
    "buffer_load_dwordx4 a[56:59], v228, s[12:15], s89 offen offset:0 sc1\n"
    "s_mul_i32 s89, s67, 5\n"
    "s_add_u32 s89, s89, s88\n"
    "buffer_load_dwordx4 a[60:63], v1, s[12:15], s89 offen offset:0 sc1\n"
    "buffer_load_dwordx4 a[64:67], v1, s[12:15], s89 offen offset:1024 sc1\n"
    "buffer_load_dwordx4 a[68:71], v1, s[12:15], s89 offen offset:2048 sc1\n"
    "buffer_load_dwordx4 a[72:75], v1, s[12:15], s89 offen offset:3072 sc1\n"
    "buffer_load_dwordx4 a[76:79], v228, s[12:15], s89 offen offset:0 sc1\n"
    "s_mul_i32 s89, s68, 5\n"
    "s_add_u32 s89, s89, s88\n"
    "buffer_load_dwordx4 a[80:83], v1, s[12:15], s89 offen offset:0 sc1\n"
    "buffer_load_dwordx4 a[84:87], v1, s[12:15], s89 offen offset:1024 sc1\n"
    "buffer_load_dwordx4 a[88:91], v1, s[12:15], s89 offen offset:2048 sc1\n"
    "buffer_load_dwordx4 a[92:95], v1, s[12:15], s89 offen offset:3072 sc1\n"
    "buffer_load_dwordx4 a[96:99], v228, s[12:15], s89 offen offset:0 sc1\n"
    "s_mul_i32 s86, s5, 143360\n"
    "s_mul_i32 s87, s9, 7168\n"
    "s_add_u32 s86, s86, s87\n"
    "s_add_u32 s86, s86, 7524352\n"
    "s_mul_i32 s87, s79, 7\n"
    "s_add_u32 s87, s87, s86\n"
    "s_add_u32 m0, s97, 0\n"
    "s_nop 0\n"
    "buffer_load_dwordx4 v1, s[12:15], s87 offen offset:0 sc1 lds\n"
    "buffer_load_dwordx4 v1, s[12:15], s87 offen offset:1024 sc1 lds\n"
    "buffer_load_dwordx4 v1, s[12:15], s87 offen offset:2048 sc1 lds\n"
    "buffer_load_dwordx4 v1, s[12:15], s87 offen offset:3072 sc1 lds\n"
    "s_add_u32 s87, s87, 4096\n"
    "s_add_u32 m0, s97, 4096\n"
    "s_nop 0\n"
    "buffer_load_dwordx4 v1, s[12:15], s87 offen offset:0 sc1 lds\n"
    "buffer_load_dwordx4 v1, s[12:15], s87 offen offset:1024 sc1 lds\n"
    "buffer_load_dwordx4 v1, s[12:15], s87 offen offset:2048 sc1 lds\n"
    "s_mul_i32 s87, s80, 7\n"
    "s_add_u32 s87, s87, s86\n"
    "s_add_u32 m0, s97, 7168\n"
    "s_nop 0\n"
    "buffer_load_dwordx4 v1, s[12:15], s87 offen offset:0 sc1 lds\n"
    "buffer_load_dwordx4 v1, s[12:15], s87 offen offset:1024 sc1 lds\n"
    "buffer_load_dwordx4 v1, s[12:15], s87 offen offset:2048 sc1 lds\n"
    "buffer_load_dwordx4 v1, s[12:15], s87 offen offset:3072 sc1 lds\n"
    "s_add_u32 s87, s87, 4096\n"
    "s_add_u32 m0, s97, 11264\n"
    "s_nop 0\n"
    "buffer_load_dwordx4 v1, s[12:15], s87 offen offset:0 sc1 lds\n"
    "buffer_load_dwordx4 v1, s[12:15], s87 offen offset:1024 sc1 lds\n"
    "buffer_load_dwordx4 v1, s[12:15], s87 offen offset:2048 sc1 lds\n"
    "s_mul_i32 s87, s81, 7\n"
    "s_add_u32 s87, s87, s86\n"
    "s_add_u32 m0, s97, 14336\n"
    "s_nop 0\n"
    "buffer_load_dwordx4 v1, s[12:15], s87 offen offset:0 sc1 lds\n"
    "buffer_load_dwordx4 v1, s[12:15], s87 offen offset:1024 sc1 lds\n"
    "buffer_load_dwordx4 v1, s[12:15], s87 offen offset:2048 sc1 lds\n"
    "buffer_load_dwordx4 v1, s[12:15], s87 offen offset:3072 sc1 lds\n"
    "s_add_u32 s87, s87, 4096\n"
    "s_add_u32 m0, s97, 18432\n"
    "s_nop 0\n"
    "buffer_load_dwordx4 v1, s[12:15], s87 offen offset:0 sc1 lds\n"
    "buffer_load_dwordx4 v1, s[12:15], s87 offen offset:1024 sc1 lds\n"
    "buffer_load_dwordx4 v1, s[12:15], s87 offen offset:2048 sc1 lds\n"
    "s_mul_i32 s87, s82, 7\n"
    "s_add_u32 s87, s87, s86\n"
    "s_add_u32 m0, s97, 21504\n"
    "s_nop 0\n"
    "buffer_load_dwordx4 v1, s[12:15], s87 offen offset:0 sc1 lds\n"
    "buffer_load_dwordx4 v1, s[12:15], s87 offen offset:1024 sc1 lds\n"
    "buffer_load_dwordx4 v1, s[12:15], s87 offen offset:2048 sc1 lds\n"
    "buffer_load_dwordx4 v1, s[12:15], s87 offen offset:3072 sc1 lds\n"
    "s_add_u32 s87, s87, 4096\n"
    "s_add_u32 m0, s97, 25600\n"
    "s_nop 0\n"
    "buffer_load_dwordx4 v1, s[12:15], s87 offen offset:0 sc1 lds\n"
    "buffer_load_dwordx4 v1, s[12:15], s87 offen offset:1024 sc1 lds\n"
    "buffer_load_dwordx4 v1, s[12:15], s87 offen offset:2048 sc1 lds\n"
    "s_mul_i32 s87, s78, 7\n"
    "s_add_u32 s87, s87, s86\n"
    "s_add_u32 m0, s97, 28672\n"
    "s_nop 0\n"
    "buffer_load_dwordx4 v1, s[12:15], s87 offen offset:0 sc1 lds\n"
    "buffer_load_dwordx4 v1, s[12:15], s87 offen offset:1024 sc1 lds\n"
    "buffer_load_dwordx4 v1, s[12:15], s87 offen offset:2048 sc1 lds\n"
    "buffer_load_dwordx4 v1, s[12:15], s87 offen offset:3072 sc1 lds\n"
    "s_add_u32 s87, s87, 4096\n"
    "s_add_u32 m0, s97, 32768\n"
    "s_nop 0\n"
    "buffer_load_dwordx4 v1, s[12:15], s87 offen offset:0 sc1 lds\n"
    "buffer_load_dwordx4 v1, s[12:15], s87 offen offset:1024 sc1 lds\n"
    "buffer_load_dwordx4 v1, s[12:15], s87 offen offset:2048 sc1 lds\n"
    "s_mov_b32 s28, s50\n"
    "s_and_b32 s29, s51, 0xffff\n"
    "s_mov_b32 s30, 2621440\n"
    "s_mov_b32 s31, 0x20000\n"
    "s_mov_b32 s32, 0x22222222\n"
    "s_mov_b32 s33, 0x22222222\n"
    "s_mov_b32 s34, 0x44444444\n"
    "s_mov_b32 s35, 0x44444444\n"
    "s_mov_b32 s36, 0x88888888\n"
    "s_mov_b32 s37, 0x88888888\n"
    "s_mov_b32 s100, 0\n"
    "s_mov_b32 s101, 0xffff0000\n"
    "s_mov_b32 s38, 0x3f3504f3\n"
    "s_mov_b32 s39, 0x3acccccd\n"
    "s_mov_b32 s40, 0x3fb8aa3b\n"
    "s_mov_b32 s41, 0x3727c5ac\n"
    "s_mov_b32 s42, 0x3f317218\n"
    "s_mov_b32 s43, 0x3f317218\n"
    "s_mov_b32 s44, 0x3f3504f3\n"
    "s_mov_b32 s45, 0x3f3504f3\n"
    "s_mov_b32 s46, 0x3f000000\n"
    "s_mov_b32 s47, 0x3f000000\n"
    "s_mov_b32 s48, 12144640\n"
    "s_mov_b32 s49, 20008960\n"
    "s_mov_b32 s52, 0\n"
    "s_lshl_b32 s83, s59, 3\n"
    "s_lshr_b32 s84, s9, 1\n"
    "s_lshl_b32 s84, s84, 2\n"
    "s_add_u32 s84, s84, s83\n"
    "s_and_b32 s85, s9, 1\n"
    "s_add_u32 s56, s84, s85\n"
    "s_mul_i32 s50, s56, 384\n"
    "s_add_u32 s50, s50, 102400\n"
    "s_lshl_b32 s83, s54, 3\n"
    "s_lshr_b32 s84, s55, 3\n"
    "s_lshl_b32 s84, s84, 2\n"
    "s_add_u32 s57, s84, s83\n"
    "s_mul_i32 s83, s57, 384\n"
    "s_add_u32 s58, s83, 102400\n"
    "s_lshl_b32 s22, s57, 10\n"
    "s_add_u32 s22, s22, 34689024\n"
    "s_mov_b32 s23, s22\n"
    "s_and_b32 s84, s55, 7\n"
    "s_lshl_b32 s84, s84, 7\n"
    "s_add_u32 s23, s23, s84\n"
    "s_mul_i32 s53, s57, 20480\n"
    "s_and_b32 s83, s55, 1\n"
    "s_min_u32 s84, s9, 2\n"
    "s_lshl_b32 s85, s84, 1\n"
    "s_add_u32 s19, s85, s83\n"
    "s_add_u32 s20, s19, 1\n"
    "s_cmp_ge_u32 s19, 5\n"
    "s_cselect_b32 s19, 0, s19\n"
    "s_cmp_ge_u32 s9, 2\n"
    "s_cselect_b32 s20, s19, s20\n"
    "s_cselect_b32 s17, 0, 1.0\n"
    "s_cmp_eq_u32 s9, 3\n"
    "s_cselect_b32 s16, 0, 1.0\n"
    "s_and_b32 s85, s55, 7\n"
    "s_mul_i32 s85, s85, 5\n"
    "s_add_u32 s19, s19, s85\n"
    "s_add_u32 s20, s20, s85\n"
    "s_lshr_b32 s18, s19, 1\n"
    "s_lshl_b32 s18, s18, 10\n"
    "s_and_b32 s83, s55, 1\n"
    "s_lshl_b32 s83, s83, 9\n"
    "s_cmp_ge_u32 s9, 2\n"
    "s_cselect_b32 s21, s83, 0\n"
    "s_lshl_b32 s83, s5, 11\n"
    "s_add_u32 s83, s83, 9818112\n"
    "v_min_u32 v16, 55, v0\n"
    "v_lshlrev_b32 v24, 4, v16\n"
    "buffer_load_dwordx4 v[208:211], v24, s[12:15], s83 offen sc1\n"
    "buffer_load_dwordx4 v[212:215], v24, s[12:15], s83 offen offset:1024 sc1\n"
    "v_cmp_lt_u32 vcc, 27, v16\n"
    "v_subrev_u32 v24, 28, v16\n"
    "s_nop 0\n"
    "v_cndmask_b32 v16, v16, v24, vcc\n"
    "v_cndmask_b32 v24, 0, 16, vcc\n"
    "v_lshrrev_b32 v25, 2, v16\n"
    "v_mul_u32_u24 v25, 7168, v25\n"
    "v_and_b32 v16, 3, v16\n"
    "v_lshl_add_u32 v25, v16, 8, v25\n"
    "v_add_u32 v25, v25, v24\n"
    "v_add_u32 v216, 6352, v25\n"
    "v_add_u32 v4, 35840, v1\n"
    "v_add_u32 v5, 71680, v1\n"
    "s_lshr_b32 s83, s56, 1\n"
    "s_and_b32 s84, s9, 1\n"
    "s_add_u32 s83, s83, s84\n"
    "s_mul_i32 s83, s83, 40960\n"
    "s_add_u32 s83, s83, 12144640\n"
    "v_add_u32 v14, s83, v1\n"
    "v_add_u32 v15, 4096, v14\n"
    "v_add_u32 v6, 107520, v1\n"
    "v_lshrrev_b32 v17, 4, v0\n"
    "v_and_b32 v18, 15, v0\n"
    "v_lshlrev_b32 v112, 8, v17\n"
    "v_add_u32 v112, 6352, v112\n"
    "v_lshrrev_b32 v19, 2, v18\n"
    "v_lshl_add_u32 v113, v19, 4, v18\n"
    "v_lshlrev_b32 v113, 2, v113\n"
    "v_lshlrev_b32 v114, 2, v18\n"
    "v_add_u32 v114, 192, v114\n"
    "s_lshl_b32 s84, s5, 1\n"
    "v_lshrrev_b32 v19, 1, v17\n"
    "v_add_u32 v19, s84, v19\n"
    "v_lshlrev_b32 v115, 8, v19\n"
    "v_lshl_add_u32 v115, v18, 4, v115\n"
    "v_add_u32 v115, 49152, v115\n"
    "v_and_b32 v17, 1, v17\n"
    "v_mul_u32_u24 v17, 32767, v17\n"
    "v_lshl_add_u32 v115, v17, 16, v115\n"
    "v_mov_b32 v123, 1\n"
    "s_waitcnt vmcnt(0) lgkmcnt(0)\n"
    "s_cmp_lg_u32 s9, 0\n"
    "s_cbranch_scc1 Lh4idle_7_%=\n"
    "s_lshr_b32 s83, s11, 1\n"
    "s_lshl_b32 s83, s83, 12\n"
    "s_lshl_b32 s84, s11, 8\n"
    "s_add_u32 s83, s83, s84\n"
    "s_add_u32 s83, s83, 86016\n"
    "v_mov_b32 v12, s83\n"
    "s_mov_b32 s96, 0x1000\n"
    "Lidle_8_%=:\n"
    "buffer_load_dword v16, v12, s[12:15], 0 offen offset:0 sc1\n"
    "s_waitcnt vmcnt(0)\n"
    "v_subrev_u32 v16, s60, v16\n"
    "v_add_u32 v16, -1, v16\n"
    "v_cmp_gt_u32 vcc, 16, v16\n"
    "s_cbranch_vccnz Lidledone_9_%=\n"
    "s_sleep 40\n"
    "s_sub_u32 s96, s96, 1\n"
    "s_cbranch_scc0 Lidle_8_%=\n"
    "Lidledone_9_%=:\n"
    "Lh4idle_7_%=:\n"
    "s_waitcnt vmcnt(0) lgkmcnt(0)\n"
    "s_branch Ljoin_%=\n"
    "Lconv_%=:\n"
    "s_cmp_ge_u32 s10, 20\n"
    "s_cbranch_scc1 Lprep_%=\n"
    "s_mov_b32 s16, s34\n"
    "s_and_b32 s17, s35, 0xffff\n"
    "s_mov_b32 s18, 2621440\n"
    "s_mov_b32 s19, 0x20000\n"
    "v_and_b32 v6, 7, v0\n"
    "v_lshrrev_b32 v7, 3, v0\n"
    "s_lshl_b32 s83, s58, 2\n"
    "s_add_u32 s93, s83, s9\n"
    "s_lshl_b32 s83, s93, 3\n"
    "v_add_u32 v8, s83, v7\n"
    "v_mul_u32_u24 v4, 5120, v8\n"
    "s_lshl_b32 s83, s57, 7\n"
    "v_lshl_add_u32 v4, v6, 4, v4\n"
    "v_add_u32 v4, s83, v4\n"
    "v_subrev_u32 v5, 3840, v4\n"
    "v_mov_b32 v9, 2147418112\n"
    "v_cmp_eq_u32 vcc, 0, v8\n"
    "s_nop 1\n"
    "v_cndmask_b32 v5, v5, v9, vcc\n"
    "buffer_load_dwordx4 v[16:19], v5, s[16:19], 0 offen offset:0\n"
    "buffer_load_dwordx4 v[20:23], v5, s[16:19], 0 offen offset:1280\n"
    "buffer_load_dwordx4 v[24:27], v5, s[16:19], 0 offen offset:2560\n"
    "buffer_load_dwordx4 v[28:31], v4, s[16:19], 0 offen offset:0\n"
    "buffer_load_dwordx4 v[32:35], v4, s[16:19], 0 offen offset:1280\n"
    "buffer_load_dwordx4 v[36:39], v4, s[16:19], 0 offen offset:2560\n"
    "buffer_load_dwordx4 v[40:43], v4, s[16:19], 0 offen offset:3840\n"
    "s_mov_b32 s28, s38\n"
    "s_and_b32 s29, s39, 0xffff\n"
    "s_mov_b32 s30, 5120\n"
    "s_mov_b32 s31, 0x20000\n"
    "s_lshl_b32 s83, s57, 9\n"
    "v_lshl_add_u32 v9, v6, 6, s83\n"
    "buffer_load_dwordx4 v[76:79], v9, s[28:31], 0 offen offset:0\n"
    "buffer_load_dwordx4 v[80:83], v9, s[28:31], 0 offen offset:16\n"
    "buffer_load_dwordx4 v[84:87], v9, s[28:31], 0 offen offset:32\n"
    "buffer_load_dwordx4 v[88:91], v9, s[28:31], 0 offen offset:48\n"
    "s_mov_b32 s20, s36\n"
    "s_and_b32 s21, s37, 0xffff\n"
    "s_mov_b32 s22, 2621440\n"
    "s_mov_b32 s23, 0x20000\n"
    "buffer_load_dwordx4 v[44:47], v4, s[20:23], 0 offen offset:0\n"
    "buffer_load_dwordx4 v[48:51], v4, s[20:23], 0 offen offset:1280\n"
    "buffer_load_dwordx4 v[52:55], v4, s[20:23], 0 offen offset:2560\n"
    "buffer_load_dwordx4 v[56:59], v4, s[20:23], 0 offen offset:3840\n"
    "v_mov_b32 v12, 0\n"
    "buffer_load_dwordx2 v[14:15], v12, s[12:15], 0 offen sc1\n"
    "s_lshl_b32 s83, s56, 2\n"
    "s_add_u32 s83, s83, s9\n"
    "s_lshl_b32 s85, s83, 12\n"
    "s_add_u32 s16, s40, s85\n"
    "s_addc_u32 s17, s41, 0\n"
    "s_and_b32 s17, s17, 0xffff\n"
    "s_movk_i32 s18, 4096\n"
    "s_lshl_b32 s83, s83, 1\n"
    "v_mov_b32 v11, s83\n"
    "v_mov_b32 v10, 2147418112\n"
    "v_cmp_eq_u32 vcc, 1, v0\n"
    "s_nop 1\n"
    "v_cndmask_b32 v10, v10, 0, vcc\n"
    "s_mov_b32 s20, s42\n"
    "s_and_b32 s21, s43, 0xffff\n"
    "s_mov_b32 s22, 1280\n"
    "s_mov_b32 s23, 0x20000\n"
    "s_mul_i32 s83, s9, 14\n"
    "s_and_b32 s85, s55, 7\n"
    "s_mul_i32 s85, s85, 14\n"
    "s_lshr_b32 s85, s85, 3\n"
    "s_add_u32 s86, s85, s83\n"
    "s_lshl_b32 s64, s86, 10\n"
    "s_add_u32 s85, s85, 1\n"
    "s_cmp_ge_u32 s85, 14\n"
    "s_cselect_b32 s85, 0, s85\n"
    "s_add_u32 s86, s85, s83\n"
    "s_lshl_b32 s65, s86, 10\n"
    "s_add_u32 s85, s85, 1\n"
    "s_cmp_ge_u32 s85, 14\n"
    "s_cselect_b32 s85, 0, s85\n"
    "s_add_u32 s86, s85, s83\n"
    "s_lshl_b32 s66, s86, 10\n"
    "s_add_u32 s85, s85, 1\n"
    "s_cmp_ge_u32 s85, 14\n"
    "s_cselect_b32 s85, 0, s85\n"
    "s_add_u32 s86, s85, s83\n"
    "s_lshl_b32 s67, s86, 10\n"
    "s_add_u32 s85, s85, 1\n"
    "s_cmp_ge_u32 s85, 14\n"
    "s_cselect_b32 s85, 0, s85\n"
    "s_add_u32 s86, s85, s83\n"
    "s_lshl_b32 s68, s86, 10\n"
    "s_add_u32 s85, s85, 1\n"
    "s_cmp_ge_u32 s85, 14\n"
    "s_cselect_b32 s85, 0, s85\n"
    "s_add_u32 s86, s85, s83\n"
    "s_lshl_b32 s69, s86, 10\n"
    "s_add_u32 s85, s85, 1\n"
    "s_cmp_ge_u32 s85, 14\n"
    "s_cselect_b32 s85, 0, s85\n"
    "s_add_u32 s86, s85, s83\n"
    "s_lshl_b32 s70, s86, 10\n"
    "s_add_u32 s85, s85, 1\n"
    "s_cmp_ge_u32 s85, 14\n"
    "s_cselect_b32 s85, 0, s85\n"
    "s_add_u32 s86, s85, s83\n"
    "s_lshl_b32 s71, s86, 10\n"
    "s_add_u32 s85, s85, 1\n"
    "s_cmp_ge_u32 s85, 14\n"
    "s_cselect_b32 s85, 0, s85\n"
    "s_add_u32 s86, s85, s83\n"
    "s_lshl_b32 s72, s86, 10\n"
    "s_add_u32 s85, s85, 1\n"
    "s_cmp_ge_u32 s85, 14\n"
    "s_cselect_b32 s85, 0, s85\n"
    "s_add_u32 s86, s85, s83\n"
    "s_lshl_b32 s73, s86, 10\n"
    "s_add_u32 s85, s85, 1\n"
    "s_cmp_ge_u32 s85, 14\n"
    "s_cselect_b32 s85, 0, s85\n"
    "s_add_u32 s86, s85, s83\n"
    "s_lshl_b32 s74, s86, 10\n"
    "s_add_u32 s85, s85, 1\n"
    "s_cmp_ge_u32 s85, 14\n"
    "s_cselect_b32 s85, 0, s85\n"
    "s_add_u32 s86, s85, s83\n"
    "s_lshl_b32 s75, s86, 10\n"
    "s_add_u32 s85, s85, 1\n"
    "s_cmp_ge_u32 s85, 14\n"
    "s_cselect_b32 s85, 0, s85\n"
    "s_add_u32 s86, s85, s83\n"
    "s_lshl_b32 s76, s86, 10\n"
    "s_add_u32 s85, s85, 1\n"
    "s_cmp_ge_u32 s85, 14\n"
    "s_cselect_b32 s85, 0, s85\n"
    "s_add_u32 s86, s85, s83\n"
    "s_lshl_b32 s77, s86, 10\n"
    "s_mul_i32 s85, s5, 52\n"
    "s_lshr_b32 s85, s85, 8\n"
    "s_mul_i32 s85, s85, 5\n"
    "s_sub_u32 s85, s5, s85\n"
    "s_lshl_b32 s78, s85, 12\n"
    "s_add_u32 s85, s85, 1\n"
    "s_cmp_ge_u32 s85, 5\n"
    "s_cselect_b32 s85, 0, s85\n"
    "s_lshl_b32 s79, s85, 12\n"
    "s_add_u32 s85, s85, 1\n"
    "s_cmp_ge_u32 s85, 5\n"
    "s_cselect_b32 s85, 0, s85\n"
    "s_lshl_b32 s80, s85, 12\n"
    "s_add_u32 s85, s85, 1\n"
    "s_cmp_ge_u32 s85, 5\n"
    "s_cselect_b32 s85, 0, s85\n"
    "s_lshl_b32 s81, s85, 12\n"
    "s_add_u32 s85, s85, 1\n"
    "s_cmp_ge_u32 s85, 5\n"
    "s_cselect_b32 s85, 0, s85\n"
    "s_lshl_b32 s82, s85, 12\n"
    "v_lshrrev_b32 v212, 1, v6\n"
    "v_mul_u32_u24 v208, 35840, v212\n"
    "v_and_b32 v213, 1, v212\n"
    "v_lshl_add_u32 v208, v213, 6, v208\n"
    "v_lshrrev_b32 v213, 1, v212\n"
    "v_lshl_add_u32 v208, v213, 4, v208\n"
    "v_and_b32 v213, 1, v6\n"
    "v_mul_u32_u24 v213, 2080, v213\n"
    "v_add_u32 v208, v208, v213\n"
    "v_add_u32 v208, 21504, v208\n"
    "v_lshrrev_b32 v213, 1, v7\n"
    "v_lshl_add_u32 v208, v213, 4, v208\n"
    "s_lshl_b32 s83, s9, 6\n"
    "s_add_u32 s83, s83, 8\n"
    "v_add_u32 v208, s83, v208\n"
    "v_and_b32 v213, 1, v7\n"
    "v_lshlrev_b32 v213, 3, v213\n"
    "v_sub_u32 v208, v208, v213\n"
    "s_and_b32 s83, s9, 1\n"
    "s_lshl_b32 s83, s83, 6\n"
    "s_lshr_b32 s84, s9, 1\n"
    "s_lshl_b32 s84, s84, 4\n"
    "s_add_u32 s83, s83, s84\n"
    "s_add_u32 s83, s83, s97\n"
    "s_add_u32 s83, s83, 21504\n"
    "v_add_u32 v210, s83, v1\n"
    "v_lshrrev_b32 v213, 4, v0\n"
    "v_lshlrev_b32 v211, 12, v213\n"
    "v_and_b32 v213, 15, v0\n"
    "v_lshl_add_u32 v211, v213, 4, v211\n"
    "s_lshl_b32 s90, s57, 2\n"
    "s_add_u32 s90, s90, s9\n"
    "s_lshl_b32 s90, s90, 16\n"
    "s_lshl_b32 s83, s58, 8\n"
    "s_add_u32 s90, s90, s83\n"
    "s_add_u32 s90, s90, 167936\n"
    "s_mov_b32 s85, 0x05040100\n"
    "s_mov_b32 s86, 0x07060302\n"
    "s_waitcnt vmcnt(14)\n"
    "v_perm_b32 v124, v16, v20, s85\n"
    "s_waitcnt vmcnt(13)\n"
    "v_perm_b32 v125, v20, v24, s85\n"
    "s_waitcnt vmcnt(12)\n"
    "v_perm_b32 v126, v24, v28, s85\n"
    "s_waitcnt vmcnt(11)\n"
    "v_perm_b32 v127, v28, v32, s85\n"
    "buffer_load_dwordx4 v[92:95], v1, s[16:19], 0 offen offset:0\n"
    "s_waitcnt vmcnt(11)\n"
    "v_perm_b32 v128, v32, v36, s85\n"
    "s_waitcnt vmcnt(10)\n"
    "v_perm_b32 v129, v36, v40, s85\n"
    "s_waitcnt vmcnt(9)\n"
    "v_dot2_f32_bf16 v132, v124, v77, 0\n"
    "v_dot2_f32_bf16 v133, v125, v77, 0\n"
    "v_dot2_f32_bf16 v134, v126, v77, 0\n"
    "v_dot2_f32_bf16 v135, v127, v77, 0\n"
    "v_dot2_f32_bf16 v132, v126, v76, v132\n"
    "v_dot2_f32_bf16 v133, v127, v76, v133\n"
    "v_dot2_f32_bf16 v134, v128, v76, v134\n"
    "v_dot2_f32_bf16 v135, v129, v76, v135\n"
    "s_waitcnt vmcnt(5)\n"
    "v_lshlrev_b32 v136, 16, v44\n"
    "s_waitcnt vmcnt(4)\n"
    "v_lshlrev_b32 v137, 16, v48\n"
    "s_waitcnt vmcnt(3)\n"
    "v_lshlrev_b32 v138, 16, v52\n"
    "s_waitcnt vmcnt(2)\n"
    "v_lshlrev_b32 v139, 16, v56\n"
    "v_mul_f32 v136, v136, v132\n"
    "v_mul_f32 v137, v137, v133\n"
    "buffer_load_dwordx2 v[96:97], v1, s[16:19], 0 offen offset:16\n"
    "v_mul_f32 v138, v138, v134\n"
    "v_mul_f32 v139, v139, v135\n"
    "v_cvt_pk_bf16_f32 v140, v139, v138\n"
    "v_cvt_pk_bf16_f32 v141, v137, v136\n"
    "ds_write_b64 v208, v[140:141] offset:0\n"
    "v_perm_b32 v144, v16, v20, s86\n"
    "v_perm_b32 v145, v20, v24, s86\n"
    "v_perm_b32 v146, v24, v28, s86\n"
    "v_perm_b32 v147, v28, v32, s86\n"
    "v_perm_b32 v148, v32, v36, s86\n"
    "v_perm_b32 v149, v36, v40, s86\n"
    "v_dot2_f32_bf16 v152, v144, v79, 0\n"
    "v_dot2_f32_bf16 v153, v145, v79, 0\n"
    "v_dot2_f32_bf16 v154, v146, v79, 0\n"
    "v_dot2_f32_bf16 v155, v147, v79, 0\n"
    "v_dot2_f32_bf16 v152, v146, v78, v152\n"
    "buffer_load_dwordx4 v[98:101], v1, s[16:19], 0 offen offset:1024\n"
    "v_dot2_f32_bf16 v153, v147, v78, v153\n"
    "v_dot2_f32_bf16 v154, v148, v78, v154\n"
    "v_dot2_f32_bf16 v155, v149, v78, v155\n"
    "v_and_b32 v156, 0xffff0000, v44\n"
    "v_and_b32 v157, 0xffff0000, v48\n"
    "v_and_b32 v158, 0xffff0000, v52\n"
    "v_and_b32 v159, 0xffff0000, v56\n"
    "v_mul_f32 v156, v156, v152\n"
    "v_mul_f32 v157, v157, v153\n"
    "v_mul_f32 v158, v158, v154\n"
    "v_mul_f32 v159, v159, v155\n"
    "v_cvt_pk_bf16_f32 v160, v159, v158\n"
    "v_cvt_pk_bf16_f32 v161, v157, v156\n"
    "ds_write_b64 v208, v[160:161] offset:256\n"
    "v_perm_b32 v124, v17, v21, s85\n"
    "v_perm_b32 v125, v21, v25, s85\n"
    "buffer_load_dwordx2 v[102:103], v1, s[16:19], 0 offen offset:1040\n"
    "v_perm_b32 v126, v25, v29, s85\n"
    "v_perm_b32 v127, v29, v33, s85\n"
    "v_perm_b32 v128, v33, v37, s85\n"
    "v_perm_b32 v129, v37, v41, s85\n"
    "v_dot2_f32_bf16 v132, v124, v81, 0\n"
    "v_dot2_f32_bf16 v133, v125, v81, 0\n"
    "v_dot2_f32_bf16 v134, v126, v81, 0\n"
    "v_dot2_f32_bf16 v135, v127, v81, 0\n"
    "v_dot2_f32_bf16 v132, v126, v80, v132\n"
    "v_dot2_f32_bf16 v133, v127, v80, v133\n"
    "v_dot2_f32_bf16 v134, v128, v80, v134\n"
    "v_dot2_f32_bf16 v135, v129, v80, v135\n"
    "v_lshlrev_b32 v136, 16, v45\n"
    "v_lshlrev_b32 v137, 16, v49\n"
    "v_lshlrev_b32 v138, 16, v53\n"
    "v_lshlrev_b32 v139, 16, v57\n"
    "buffer_load_dwordx4 v[104:107], v1, s[16:19], 0 offen offset:2048\n"
    "v_mul_f32 v136, v136, v132\n"
    "v_mul_f32 v137, v137, v133\n"
    "v_mul_f32 v138, v138, v134\n"
    "v_mul_f32 v139, v139, v135\n"
    "v_cvt_pk_bf16_f32 v140, v139, v138\n"
    "v_cvt_pk_bf16_f32 v141, v137, v136\n"
    "ds_write_b64 v208, v[140:141] offset:512\n"
    "v_perm_b32 v144, v17, v21, s86\n"
    "v_perm_b32 v145, v21, v25, s86\n"
    "v_perm_b32 v146, v25, v29, s86\n"
    "v_perm_b32 v147, v29, v33, s86\n"
    "v_perm_b32 v148, v33, v37, s86\n"
    "v_perm_b32 v149, v37, v41, s86\n"
    "v_dot2_f32_bf16 v152, v144, v83, 0\n"
    "v_dot2_f32_bf16 v153, v145, v83, 0\n"
    "v_dot2_f32_bf16 v154, v146, v83, 0\n"
    "buffer_load_dwordx2 v[108:109], v1, s[16:19], 0 offen offset:2064\n"
    "v_dot2_f32_bf16 v155, v147, v83, 0\n"
    "v_dot2_f32_bf16 v152, v146, v82, v152\n"
    "v_dot2_f32_bf16 v153, v147, v82, v153\n"
    "v_dot2_f32_bf16 v154, v148, v82, v154\n"
    "v_dot2_f32_bf16 v155, v149, v82, v155\n"
    "v_and_b32 v156, 0xffff0000, v45\n"
    "v_and_b32 v157, 0xffff0000, v49\n"
    "v_and_b32 v158, 0xffff0000, v53\n"
    "v_and_b32 v159, 0xffff0000, v57\n"
    "v_mul_f32 v156, v156, v152\n"
    "v_mul_f32 v157, v157, v153\n"
    "v_mul_f32 v158, v158, v154\n"
    "v_mul_f32 v159, v159, v155\n"
    "v_cvt_pk_bf16_f32 v160, v159, v158\n"
    "v_cvt_pk_bf16_f32 v161, v157, v156\n"
    "ds_write_b64 v208, v[160:161] offset:768\n"
    "buffer_load_dwordx4 v[110:113], v1, s[16:19], 0 offen offset:3072\n"
    "v_perm_b32 v124, v18, v22, s85\n"
    "v_perm_b32 v125, v22, v26, s85\n"
    "v_perm_b32 v126, v26, v30, s85\n"
    "v_perm_b32 v127, v30, v34, s85\n"
    "v_perm_b32 v128, v34, v38, s85\n"
    "v_perm_b32 v129, v38, v42, s85\n"
    "v_dot2_f32_bf16 v132, v124, v85, 0\n"
    "v_dot2_f32_bf16 v133, v125, v85, 0\n"
    "v_dot2_f32_bf16 v134, v126, v85, 0\n"
    "v_dot2_f32_bf16 v135, v127, v85, 0\n"
    "v_dot2_f32_bf16 v132, v126, v84, v132\n"
    "v_dot2_f32_bf16 v133, v127, v84, v133\n"
    "v_dot2_f32_bf16 v134, v128, v84, v134\n"
    "v_dot2_f32_bf16 v135, v129, v84, v135\n"
    "v_lshlrev_b32 v136, 16, v46\n"
    "v_lshlrev_b32 v137, 16, v50\n"
    "buffer_load_dwordx2 v[114:115], v1, s[16:19], 0 offen offset:3088\n"
    "v_lshlrev_b32 v138, 16, v54\n"
    "v_lshlrev_b32 v139, 16, v58\n"
    "v_mul_f32 v136, v136, v132\n"
    "v_mul_f32 v137, v137, v133\n"
    "v_mul_f32 v138, v138, v134\n"
    "v_mul_f32 v139, v139, v135\n"
    "v_cvt_pk_bf16_f32 v140, v139, v138\n"
    "v_cvt_pk_bf16_f32 v141, v137, v136\n"
    "ds_write_b64 v208, v[140:141] offset:1040\n"
    "v_perm_b32 v144, v18, v22, s86\n"
    "v_perm_b32 v145, v22, v26, s86\n"
    "v_perm_b32 v146, v26, v30, s86\n"
    "v_perm_b32 v147, v30, v34, s86\n"
    "v_perm_b32 v148, v34, v38, s86\n"
    "v_perm_b32 v149, v38, v42, s86\n"
    "v_dot2_f32_bf16 v152, v144, v87, 0\n"
    "buffer_load_dwordx2 v[120:121], v10, s[16:19], 0 offen\n"
    "v_dot2_f32_bf16 v153, v145, v87, 0\n"
    "v_dot2_f32_bf16 v154, v146, v87, 0\n"
    "v_dot2_f32_bf16 v155, v147, v87, 0\n"
    "v_dot2_f32_bf16 v152, v146, v86, v152\n"
    "buffer_load_ushort v11, v11, s[20:23], 0 offen\n"
    "v_dot2_f32_bf16 v153, v147, v86, v153\n"
    "v_dot2_f32_bf16 v154, v148, v86, v154\n"
    "v_dot2_f32_bf16 v155, v149, v86, v155\n"
    "v_and_b32 v156, 0xffff0000, v46\n"
    "v_and_b32 v157, 0xffff0000, v50\n"
    "v_and_b32 v158, 0xffff0000, v54\n"
    "v_and_b32 v159, 0xffff0000, v58\n"
    "v_mul_f32 v156, v156, v152\n"
    "v_mul_f32 v157, v157, v153\n"
    "v_mul_f32 v158, v158, v154\n"
    "v_mul_f32 v159, v159, v155\n"
    "v_cvt_pk_bf16_f32 v160, v159, v158\n"
    "v_cvt_pk_bf16_f32 v161, v157, v156\n"
    "ds_write_b64 v208, v[160:161] offset:1296\n"
    "v_perm_b32 v124, v19, v23, s85\n"
    "v_perm_b32 v125, v23, v27, s85\n"
    "v_perm_b32 v126, v27, v31, s85\n"
    "v_perm_b32 v127, v31, v35, s85\n"
    "v_perm_b32 v128, v35, v39, s85\n"
    "v_perm_b32 v129, v39, v43, s85\n"
    "v_dot2_f32_bf16 v132, v124, v89, 0\n"
    "v_dot2_f32_bf16 v133, v125, v89, 0\n"
    "v_dot2_f32_bf16 v134, v126, v89, 0\n"
    "v_dot2_f32_bf16 v135, v127, v89, 0\n"
    "v_dot2_f32_bf16 v132, v126, v88, v132\n"
    "v_dot2_f32_bf16 v133, v127, v88, v133\n"
    "v_dot2_f32_bf16 v134, v128, v88, v134\n"
    "v_dot2_f32_bf16 v135, v129, v88, v135\n"
    "v_lshlrev_b32 v136, 16, v47\n"
    "v_lshlrev_b32 v137, 16, v51\n"
    "v_lshlrev_b32 v138, 16, v55\n"
    "v_lshlrev_b32 v139, 16, v59\n"
    "v_mul_f32 v136, v136, v132\n"
    "v_mul_f32 v137, v137, v133\n"
    "v_mul_f32 v138, v138, v134\n"
    "v_mul_f32 v139, v139, v135\n"
    "v_cvt_pk_bf16_f32 v140, v139, v138\n"
    "v_cvt_pk_bf16_f32 v141, v137, v136\n"
    "ds_write_b64 v208, v[140:141] offset:1552\n"
    "v_perm_b32 v144, v19, v23, s86\n"
    "v_perm_b32 v145, v23, v27, s86\n"
    "v_perm_b32 v146, v27, v31, s86\n"
    "v_perm_b32 v147, v31, v35, s86\n"
    "v_perm_b32 v148, v35, v39, s86\n"
    "v_perm_b32 v149, v39, v43, s86\n"
    "v_dot2_f32_bf16 v152, v144, v91, 0\n"
    "v_dot2_f32_bf16 v153, v145, v91, 0\n"
    "v_dot2_f32_bf16 v154, v146, v91, 0\n"
    "v_dot2_f32_bf16 v155, v147, v91, 0\n"
    "v_dot2_f32_bf16 v152, v146, v90, v152\n"
    "v_dot2_f32_bf16 v153, v147, v90, v153\n"
    "v_dot2_f32_bf16 v154, v148, v90, v154\n"
    "v_dot2_f32_bf16 v155, v149, v90, v155\n"
    "v_and_b32 v156, 0xffff0000, v47\n"
    "v_and_b32 v157, 0xffff0000, v51\n"
    "v_and_b32 v158, 0xffff0000, v55\n"
    "v_and_b32 v159, 0xffff0000, v59\n"
    "v_mul_f32 v156, v156, v152\n"
    "v_mul_f32 v157, v157, v153\n"
    "v_mul_f32 v158, v158, v154\n"
    "v_mul_f32 v159, v159, v155\n"
    "v_cvt_pk_bf16_f32 v160, v159, v158\n"
    "v_cvt_pk_bf16_f32 v161, v157, v156\n"
    "ds_write_b64 v208, v[160:161] offset:1808\n"
    "s_waitcnt lgkmcnt(0)\n"
    "s_barrier\n"
    "ds_read_b128 v[16:19], v210 offset:0\n"
    "ds_read_b128 v[20:23], v210 offset:1040\n"
    "ds_read_b128 v[24:27], v210 offset:2080\n"
    "ds_read_b128 v[28:31], v210 offset:3120\n"
    "s_mov_b32 s83, 0x7f4a7c15\n"
    "v_mov_b32 v12, 0x9e3779b9\n"
    "s_waitcnt vmcnt(10)\n"
    "v_add_co_u32 v14, vcc, s83, v14\n"
    "s_nop 1\n"
    "v_addc_co_u32 v15, vcc, v15, v12, vcc\n"
    "v_readfirstlane_b32 s60, v14\n"
    "v_readfirstlane_b32 s61, v15\n"
    "s_add_u32 s62, s60, 16\n"
    "s_addc_u32 s63, s61, 0\n"
    "s_waitcnt lgkmcnt(3)\n"
    "buffer_store_dwordx4 v[16:19], v211, s[12:15], s90 offen sc1\n"
    "s_add_u32 s90, s90, 16384\n"
    "s_waitcnt lgkmcnt(2)\n"
    "buffer_store_dwordx4 v[20:23], v211, s[12:15], s90 offen sc1\n"
    "s_add_u32 s90, s90, 16384\n"
    "s_waitcnt lgkmcnt(1)\n"
    "buffer_store_dwordx4 v[24:27], v211, s[12:15], s90 offen sc1\n"
    "s_add_u32 s90, s90, 16384\n"
    "s_waitcnt lgkmcnt(0)\n"
    "buffer_store_dwordx4 v[28:31], v211, s[12:15], s90 offen sc1\n"
    "s_add_u32 s90, s90, 16384\n"
    "s_lshr_b32 s83, s58, 1\n"
    "s_lshl_b32 s83, s83, 12\n"
    "s_and_b32 s84, s58, 1\n"
    "s_lshl_b32 s84, s84, 7\n"
    "s_add_u32 s83, s83, s84\n"
    "s_lshl_b32 s84, s57, 8\n"
    "s_add_u32 s83, s83, s84\n"
    "s_add_u32 s83, s83, 86016\n"
    "s_cmp_lt_u32 s58, 8\n"
    "s_cselect_b32 s84, 0, 2147418112\n"
    "s_cmp_eq_u32 s9, 0\n"
    "s_cselect_b32 s84, s84, 2147418112\n"
    "s_cmp_eq_u32 s9, 1\n"
    "s_cselect_b32 s92, 0, 2147418112\n"
    "v_mov_b32 v212, s60\n"
    "v_mov_b32 v213, s61\n"
    "v_mov_b32 v214, s60\n"
    "v_mov_b32 v215, s61\n"
    "v_add_u32 v216, s84, v1\n"
    "s_mov_b64 exec, 0xff\n"
    "buffer_store_dwordx4 v[212:215], v216, s[12:15], s83 offen sc1\n"
    "s_mov_b64 exec, -1\n"
    "s_waitcnt vmcnt(5)\n"
    "v_lshlrev_b32 v11, 16, v11\n"
    "v_add_u32 v219, s97, v1\n"
    "v_readfirstlane_b32 s98, v11\n"
    "v_alignbit_b32 v124, v93, v92, 16\n"
    "v_mov_b32 v128, v93\n"
    "v_alignbit_b32 v132, v94, v93, 16\n"
    "v_alignbit_b32 v125, v94, v93, 16\n"
    "v_mov_b32 v129, v94\n"
    "v_alignbit_b32 v133, v95, v94, 16\n"
    "v_alignbit_b32 v126, v95, v94, 16\n"
    "v_mov_b32 v130, v95\n"
    "v_alignbit_b32 v134, v96, v95, 16\n"
    "v_alignbit_b32 v127, v96, v95, 16\n"
    "v_mov_b32 v131, v96\n"
    "v_alignbit_b32 v135, v97, v96, 16\n"
    "ds_write_b128 v219, v[92:95] offset:32\n"
    "ds_write_b128 v219, v[124:127] offset:4192\n"
    "ds_write_b128 v219, v[128:131] offset:8352\n"
    "ds_write_b128 v219, v[132:135] offset:12512\n"
    "v_alignbit_b32 v136, v99, v98, 16\n"
    "v_mov_b32 v140, v99\n"
    "v_alignbit_b32 v144, v100, v99, 16\n"
    "v_alignbit_b32 v137, v100, v99, 16\n"
    "v_mov_b32 v141, v100\n"
    "v_alignbit_b32 v145, v101, v100, 16\n"
    "v_alignbit_b32 v138, v101, v100, 16\n"
    "v_mov_b32 v142, v101\n"
    "v_alignbit_b32 v146, v102, v101, 16\n"
    "v_alignbit_b32 v139, v102, v101, 16\n"
    "v_mov_b32 v143, v102\n"
    "v_alignbit_b32 v147, v103, v102, 16\n"
    "ds_write_b128 v219, v[98:101] offset:1056\n"
    "ds_write_b128 v219, v[136:139] offset:5216\n"
    "ds_write_b128 v219, v[140:143] offset:9376\n"
    "ds_write_b128 v219, v[144:147] offset:13536\n"
    "s_lshl_b32 s83, s56, 7\n"
    "s_add_u32 s93, s83, 8192\n"
    "v_mov_b32 v212, s60\n"
    "v_mov_b32 v213, s61\n"
    "v_mov_b32 v214, s60\n"
    "v_mov_b32 v215, s61\n"
    "v_add_u32 v216, s92, v1\n"
    "s_waitcnt vmcnt(0)\n"
    "s_barrier\n"
    "s_mov_b64 exec, 0xff\n"
    "buffer_store_dwordx4 v[212:215], v216, s[12:15], s93 offen sc1\n"
    "s_mov_b64 exec, -1\n"
    "v_alignbit_b32 v124, v105, v104, 16\n"
    "v_mov_b32 v128, v105\n"
    "v_alignbit_b32 v132, v106, v105, 16\n"
    "v_alignbit_b32 v125, v106, v105, 16\n"
    "v_mov_b32 v129, v106\n"
    "v_alignbit_b32 v133, v107, v106, 16\n"
    "v_alignbit_b32 v126, v107, v106, 16\n"
    "v_mov_b32 v130, v107\n"
    "v_alignbit_b32 v134, v108, v107, 16\n"
    "v_alignbit_b32 v127, v108, v107, 16\n"
    "v_mov_b32 v131, v108\n"
    "v_alignbit_b32 v135, v109, v108, 16\n"
    "ds_write_b128 v219, v[104:107] offset:2080\n"
    "ds_write_b128 v219, v[124:127] offset:6240\n"
    "ds_write_b128 v219, v[128:131] offset:10400\n"
    "ds_write_b128 v219, v[132:135] offset:14560\n"
    "v_alignbit_b32 v136, v111, v110, 16\n"
    "v_mov_b32 v140, v111\n"
    "v_alignbit_b32 v144, v112, v111, 16\n"
    "v_alignbit_b32 v137, v112, v111, 16\n"
    "v_mov_b32 v141, v112\n"
    "v_alignbit_b32 v145, v113, v112, 16\n"
    "v_alignbit_b32 v138, v113, v112, 16\n"
    "v_mov_b32 v142, v113\n"
    "v_alignbit_b32 v146, v114, v113, 16\n"
    "v_alignbit_b32 v139, v114, v113, 16\n"
    "v_mov_b32 v143, v114\n"
    "v_alignbit_b32 v147, v115, v114, 16\n"
    "ds_write_b128 v219, v[110:113] offset:3104\n"
    "ds_write_b128 v219, v[136:139] offset:7264\n"
    "ds_write_b128 v219, v[140:143] offset:11424\n"
    "ds_write_b128 v219, v[144:147] offset:15584\n"
    "v_mov_b32 v116, 0\n"
    "v_mov_b32 v117, 0\n"
    "v_mov_b32 v118, 0\n"
    "v_mov_b32 v119, 0\n"
    "v_mov_b32 v12, 4128\n"
    "v_cmp_eq_u32 vcc, 2, v0\n"
    "s_nop 1\n"
    "v_cndmask_b32 v12, v1, v12, vcc\n"
    "v_add_u32 v219, s97, v12\n"
    "s_mov_b64 exec, 7\n"
    "v_alignbit_b32 v124, v117, v116, 16\n"
    "v_mov_b32 v128, v117\n"
    "v_alignbit_b32 v132, v118, v117, 16\n"
    "v_alignbit_b32 v125, v118, v117, 16\n"
    "v_mov_b32 v129, v118\n"
    "v_alignbit_b32 v133, v119, v118, 16\n"
    "v_alignbit_b32 v126, v119, v118, 16\n"
    "v_mov_b32 v130, v119\n"
    "v_alignbit_b32 v134, v120, v119, 16\n"
    "v_alignbit_b32 v127, v120, v119, 16\n"
    "v_mov_b32 v131, v120\n"
    "v_alignbit_b32 v135, v121, v120, 16\n"
    "ds_write_b128 v219, v[116:119] offset:0\n"
    "ds_write_b128 v219, v[124:127] offset:4160\n"
    "ds_write_b128 v219, v[128:131] offset:8320\n"
    "ds_write_b128 v219, v[132:135] offset:12480\n"
    "s_mov_b64 exec, -1\n"
    "v_and_b32 v48, 15, v0\n"
    "v_lshrrev_b32 v49, 4, v0\n"
    "v_add_u32 v50, 1, v48\n"
    "v_and_b32 v51, 3, v50\n"
    "v_mul_u32_u24 v4, 4160, v51\n"
    "v_and_b32 v50, 28, v50\n"
    "v_lshl_add_u32 v4, v50, 1, v4\n"
    "v_lshl_add_u32 v4, v49, 4, v4\n"
    "v_add_u32 v4, s97, v4\n"
    "v_lshlrev_b32 v5, 1, v48\n"
    "v_sub_u32 v5, v5, v49\n"
    "v_lshlrev_b32 v5, 4, v5\n"
    "s_add_u32 s83, s97, 16720\n"
    "v_add_u32 v5, s83, v5\n"
    "v_lshrrev_b32 v50, 1, v49\n"
    "v_lshl_add_u32 v50, v48, 1, v50\n"
    "v_and_b32 v51, 1, v49\n"
    "v_lshlrev_b32 v51, 3, v51\n"
    "v_lshlrev_b32 v6, 4, v50\n"
    "v_sub_u32 v6, v6, v51\n"
    "s_add_u32 s83, s97, 17160\n"
    "v_add_u32 v6, s83, v6\n"
    "v_lshlrev_b32 v7, 5, v48\n"
    "v_lshl_add_u32 v7, v49, 3, v7\n"
    "s_mul_i32 s83, s9, 4128\n"
    "s_add_u32 s83, s83, 143360\n"
    "v_add_u32 v7, s83, v7\n"
    "v_bfe_u32 v51, v0, 4, 1\n"
    "v_lshrrev_b32 v50, 5, v0\n"
    "v_lshl_add_u32 v51, v51, 1, v50\n"
    "v_lshrrev_b32 v50, 2, v48\n"
    "v_mul_u32_u24 v8, 4128, v50\n"
    "v_lshl_add_u32 v8, v51, 6, v8\n"
    "v_and_b32 v52, 3, v0\n"
    "v_lshl_add_u32 v8, v52, 3, v8\n"
    "s_lshl_b32 s83, s9, 10\n"
    "s_add_u32 s83, s83, 143360\n"
    "v_add_u32 v8, s83, v8\n"
    "v_lshl_add_u32 v52, v52, 2, v50\n"
    "v_lshlrev_b32 v229, 4, v52\n"
    "v_lshl_add_u32 v229, v51, 8, v229\n"
    "s_lshl_b32 s83, s9, 4\n"
    "v_add_u32 v50, s83, v51\n"
    "v_mul_u32_u24 v9, 40960, v50\n"
    "v_lshl_add_u32 v9, v48, 4, v9\n"
    "v_lshrrev_b32 v231, 5, v0\n"
    "v_lshlrev_b32 v231, 5, v231\n"
    "v_sub_u32 v231, v8, v231\n"
    "v_lshlrev_b32 v10, 7, v48\n"
    "s_lshl_b32 s83, s57, 11\n"
    "s_add_u32 s83, s83, 8192\n"
    "v_add_u32 v10, s83, v10\n"
    "v_mov_b32 v16, 0\n"
    "v_mov_b32 v17, 0\n"
    "v_add_u32 v219, s97, v2\n"
    "ds_write_b64 v219, v[16:17] offset:16640\n"
    "s_lshl_b32 s83, s56, 14\n"
    "s_lshl_b32 s84, s9, 12\n"
    "s_add_u32 s83, s83, s84\n"
    "s_add_u32 s85, s83, 167936\n"
    "s_add_u32 s4, s83, 2789376\n"
    "v_lshlrev_b32 v11, 7, v0\n"
    "s_lshr_b32 s90, s11, 1\n"
    "s_lshl_b32 s90, s90, 13\n"
    "s_add_u32 s90, s90, 53248\n"
    "v_and_b32 v12, 31, v0\n"
    "v_lshlrev_b32 v12, 7, v12\n"
    "v_add_u32 v230, 4096, v12\n"
    "s_mov_b32 s91, 49152\n"
    "s_add_u32 s83, s97, 17152\n"
    "v_add_u32 v219, s83, v1\n"
    "s_mov_b32 m0, s83\n"
    "ds_read_b64 v[48:49], v4 offset:0\n"
    "ds_read_b64 v[50:51], v4 offset:8\n"
    "ds_read_b64 v[52:53], v4 offset:512\n"
    "ds_read_b64 v[54:55], v4 offset:520\n"
    "ds_read_b64 v[56:57], v4 offset:1024\n"
    "ds_read_b64 v[58:59], v4 offset:1032\n"
    "ds_read_b64 v[60:61], v4 offset:1536\n"
    "ds_read_b64 v[62:63], v4 offset:1544\n"
    "ds_read_b64 v[64:65], v4 offset:2048\n"
    "ds_read_b64 v[66:67], v4 offset:2056\n"
    "ds_read_b64 v[68:69], v4 offset:2560\n"
    "ds_read_b64 v[70:71], v4 offset:2568\n"
    "ds_read_b64 v[72:73], v4 offset:3072\n"
    "ds_read_b64 v[74:75], v4 offset:3080\n"
    "ds_read_b64 v[76:77], v4 offset:3584\n"
    "ds_read_b64 v[78:79], v4 offset:3592\n"
    "s_cmp_lg_u32 s9, 0\n"
    "s_cbranch_scc1 Lh1skip_10_%=\n"
    "buffer_load_dwordx2 v[16:17], v10, s[12:15], 0 offen offset:0 sc1\n"
    "s_waitcnt vmcnt(0)\n"
    "v_cmp_ne_u64 vcc, s[60:61], v[16:17]\n"
    "s_cbranch_vccnz Lslow_11_%=\n"
    "Lback_12_%=:\n"
    "Lh1skip_10_%=:\n"
    "s_barrier\n"
    "buffer_load_dwordx4 v1, s[12:15], s85 offen offset:0 sc1 lds\n"
    "buffer_load_dwordx4 v1, s[12:15], s85 offen offset:1024 sc1 lds\n"
    "buffer_load_dwordx4 v1, s[12:15], s85 offen offset:2048 sc1 lds\n"
    "buffer_load_dwordx4 v1, s[12:15], s85 offen offset:3072 sc1 lds\n"
    "s_waitcnt vmcnt(0)\n"
    "ds_read_b128 v[80:83], v5 offset:448\n"
    "ds_read_b128 v[84:87], v5 offset:960\n"
    "ds_read_b128 v[88:91], v5 offset:1472\n"
    "ds_read_b128 v[92:95], v5 offset:1984\n"
    "ds_read_b128 v[96:99], v5 offset:2496\n"
    "ds_read_b128 v[100:103], v5 offset:3008\n"
    "ds_read_b128 v[104:107], v5 offset:3520\n"
    "ds_read_b128 v[108:111], v5 offset:4032\n"
    "s_waitcnt lgkmcnt(7)\n"
    "v_mfma_f32_16x16x32_bf16 v[16:19], v[48:51], v[80:83], 0\n"
    "ds_read_b128 v[144:147], v5 offset:384\n"
    "v_mov_b32 v216, 0\n"
    "s_waitcnt lgkmcnt(7)\n"
    "v_mfma_f32_16x16x32_bf16 v[20:23], v[48:51], v[84:87], 0\n"
    "ds_read_b64 v[112:113], v4 offset:64\n"
    "v_mov_b32 v217, 0\n"
    "s_waitcnt lgkmcnt(7)\n"
    "v_mfma_f32_16x16x32_bf16 v[24:27], v[48:51], v[88:91], 0\n"
    "ds_read_b64 v[114:115], v4 offset:72\n"
    "v_mov_b32 v218, 0\n"
    "s_waitcnt lgkmcnt(7)\n"
    "v_mfma_f32_16x16x32_bf16 v[28:31], v[48:51], v[92:95], 0\n"
    "ds_read_b128 v[148:151], v5 offset:896\n"
    "v_mov_b32 v219, 0\n"
    "s_waitcnt lgkmcnt(7)\n"
    "v_mfma_f32_16x16x32_bf16 v[32:35], v[48:51], v[96:99], 0\n"
    "ds_read_b64 v[116:117], v4 offset:576\n"
    "s_mul_i32 s84, s56, 384\n"
    "s_waitcnt lgkmcnt(7)\n"
    "v_mfma_f32_16x16x32_bf16 v[36:39], v[48:51], v[100:103], 0\n"
    "ds_read_b64 v[118:119], v4 offset:584\n"
    "s_add_u32 s84, s84, 102400\n"
    "s_waitcnt lgkmcnt(7)\n"
    "v_mfma_f32_16x16x32_bf16 v[40:43], v[48:51], v[104:107], 0\n"
    "ds_read_b128 v[152:155], v5 offset:1408\n"
    "s_mov_b64 exec, 0xffffff\n"
    "buffer_store_dwordx4 v[216:219], v1, s[12:15], s84 offen sc1\n"
    "s_mov_b64 exec, -1\n"
    "s_waitcnt lgkmcnt(7)\n"
    "v_mfma_f32_16x16x32_bf16 v[44:47], v[48:51], v[108:111], 0\n"
    "ds_read_b64 v[120:121], v4 offset:1088\n"
    "v_mov_b32 v220, -1\n"
    "v_mfma_f32_16x16x32_bf16 v[20:23], v[52:55], v[80:83], v[20:23]\n"
    "ds_read_b64 v[122:123], v4 offset:1096\n"
    "v_mov_b32 v221, -1\n"
    "v_mfma_f32_16x16x32_bf16 v[24:27], v[52:55], v[84:87], v[24:27]\n"
    "ds_read_b128 v[156:159], v5 offset:1920\n"
    "v_mov_b32 v222, -1\n"
    "v_mfma_f32_16x16x32_bf16 v[28:31], v[52:55], v[88:91], v[28:31]\n"
    "ds_read_b64 v[124:125], v4 offset:1600\n"
    "v_mov_b32 v223, -1\n"
    "v_mfma_f32_16x16x32_bf16 v[32:35], v[52:55], v[92:95], v[32:35]\n"
    "ds_read_b64 v[126:127], v4 offset:1608\n"
    "s_lshl_b32 s84, s56, 2\n"
    "v_mfma_f32_16x16x32_bf16 v[36:39], v[52:55], v[96:99], v[36:39]\n"
    "ds_read_b128 v[160:163], v5 offset:2432\n"
    "s_add_u32 s84, s84, s9\n"
    "v_mfma_f32_16x16x32_bf16 v[40:43], v[52:55], v[100:103], v[40:43]\n"
    "ds_read_b64 v[128:129], v4 offset:2112\n"
    "s_lshl_b32 s84, s84, 8\n"
    "v_mfma_f32_16x16x32_bf16 v[44:47], v[52:55], v[104:107], v[44:47]\n"
    "ds_read_b64 v[130:131], v4 offset:2120\n"
    "s_add_u32 s84, s84, 34689024\n"
    "v_mfma_f32_16x16x32_bf16 v[24:27], v[56:59], v[80:83], v[24:27]\n"
    "ds_read_b128 v[164:167], v5 offset:2944\n"
    "s_mov_b64 exec, 0xffff\n"
    "buffer_store_dwordx4 v[220:223], v1, s[12:15], s84 offen sc1\n"
    "s_mov_b64 exec, -1\n"
    "v_mfma_f32_16x16x32_bf16 v[28:31], v[56:59], v[84:87], v[28:31]\n"
    "ds_read_b64 v[132:133], v4 offset:2624\n"
    "v_mfma_f32_16x16x32_bf16 v[32:35], v[56:59], v[88:91], v[32:35]\n"
    "ds_read_b64 v[134:135], v4 offset:2632\n"
    "v_mfma_f32_16x16x32_bf16 v[36:39], v[56:59], v[92:95], v[36:39]\n"
    "ds_read_b128 v[168:171], v5 offset:3456\n"
    "v_mfma_f32_16x16x32_bf16 v[40:43], v[56:59], v[96:99], v[40:43]\n"
    "ds_read_b64 v[136:137], v4 offset:3136\n"
    "v_mfma_f32_16x16x32_bf16 v[44:47], v[56:59], v[100:103], v[44:47]\n"
    "ds_read_b64 v[138:139], v4 offset:3144\n"
    "v_mfma_f32_16x16x32_bf16 v[28:31], v[60:63], v[80:83], v[28:31]\n"
    "ds_read_b128 v[172:175], v5 offset:3968\n"
    "v_mfma_f32_16x16x32_bf16 v[32:35], v[60:63], v[84:87], v[32:35]\n"
    "ds_read_b64 v[140:141], v4 offset:3648\n"
    "v_mfma_f32_16x16x32_bf16 v[36:39], v[60:63], v[88:91], v[36:39]\n"
    "ds_read_b64 v[142:143], v4 offset:3656\n"
    "v_mfma_f32_16x16x32_bf16 v[40:43], v[60:63], v[92:95], v[40:43]\n"
    "v_mfma_f32_16x16x32_bf16 v[44:47], v[60:63], v[96:99], v[44:47]\n"
    "v_mfma_f32_16x16x32_bf16 v[32:35], v[64:67], v[80:83], v[32:35]\n"
    "v_mfma_f32_16x16x32_bf16 v[36:39], v[64:67], v[84:87], v[36:39]\n"
    "v_mfma_f32_16x16x32_bf16 v[40:43], v[64:67], v[88:91], v[40:43]\n"
    "v_mfma_f32_16x16x32_bf16 v[44:47], v[64:67], v[92:95], v[44:47]\n"
    "buffer_load_dwordx2 v[208:209], v230, s[12:15], s90 offen offset:0 sc1\n"
    "v_mfma_f32_16x16x32_bf16 v[36:39], v[68:71], v[80:83], v[36:39]\n"
    "v_mfma_f32_16x16x32_bf16 v[40:43], v[68:71], v[84:87], v[40:43]\n"
    "v_mfma_f32_16x16x32_bf16 v[44:47], v[68:71], v[88:91], v[44:47]\n"
    "v_mfma_f32_16x16x32_bf16 v[40:43], v[72:75], v[80:83], v[40:43]\n"
    "v_mfma_f32_16x16x32_bf16 v[44:47], v[72:75], v[84:87], v[44:47]\n"
    "v_mfma_f32_16x16x32_bf16 v[44:47], v[76:79], v[80:83], v[44:47]\n"
    "s_waitcnt lgkmcnt(0)\n"
    "v_mfma_f32_16x16x32_bf16 v[16:19], v[112:115], v[144:147], v[16:19]\n"
    "ds_read_b128 v[80:83], v5 offset:320\n"
    "v_mfma_f32_16x16x32_bf16 v[20:23], v[112:115], v[148:151], v[20:23]\n"
    "ds_read_b64 v[48:49], v4 offset:128\n"
    "v_mfma_f32_16x16x32_bf16 v[24:27], v[112:115], v[152:155], v[24:27]\n"
    "ds_read_b64 v[50:51], v4 offset:136\n"
    "v_mfma_f32_16x16x32_bf16 v[28:31], v[112:115], v[156:159], v[28:31]\n"
    "ds_read_b128 v[84:87], v5 offset:832\n"
    "v_mfma_f32_16x16x32_bf16 v[32:35], v[112:115], v[160:163], v[32:35]\n"
    "ds_read_b64 v[52:53], v4 offset:640\n"
    "v_mfma_f32_16x16x32_bf16 v[36:39], v[112:115], v[164:167], v[36:39]\n"
    "ds_read_b64 v[54:55], v4 offset:648\n"
    "v_mfma_f32_16x16x32_bf16 v[40:43], v[112:115], v[168:171], v[40:43]\n"
    "ds_read_b128 v[88:91], v5 offset:1344\n"
    "v_mfma_f32_16x16x32_bf16 v[44:47], v[112:115], v[172:175], v[44:47]\n"
    "ds_read_b64 v[56:57], v4 offset:1152\n"
    "v_mfma_f32_16x16x32_bf16 v[20:23], v[116:119], v[144:147], v[20:23]\n"
    "ds_read_b64 v[58:59], v4 offset:1160\n"
    "v_mfma_f32_16x16x32_bf16 v[24:27], v[116:119], v[148:151], v[24:27]\n"
    "ds_read_b128 v[92:95], v5 offset:1856\n"
    "v_mfma_f32_16x16x32_bf16 v[28:31], v[116:119], v[152:155], v[28:31]\n"
    "ds_read_b64 v[60:61], v4 offset:1664\n"
    "v_mfma_f32_16x16x32_bf16 v[32:35], v[116:119], v[156:159], v[32:35]\n"
    "ds_read_b64 v[62:63], v4 offset:1672\n"
    "v_mfma_f32_16x16x32_bf16 v[36:39], v[116:119], v[160:163], v[36:39]\n"
    "ds_read_b128 v[96:99], v5 offset:2368\n"
    "v_mfma_f32_16x16x32_bf16 v[40:43], v[116:119], v[164:167], v[40:43]\n"
    "ds_read_b64 v[64:65], v4 offset:2176\n"
    "v_mfma_f32_16x16x32_bf16 v[44:47], v[116:119], v[168:171], v[44:47]\n"
    "ds_read_b64 v[66:67], v4 offset:2184\n"
    "v_mfma_f32_16x16x32_bf16 v[24:27], v[120:123], v[144:147], v[24:27]\n"
    "ds_read_b128 v[100:103], v5 offset:2880\n"
    "v_mfma_f32_16x16x32_bf16 v[28:31], v[120:123], v[148:151], v[28:31]\n"
    "ds_read_b64 v[68:69], v4 offset:2688\n"
    "v_mfma_f32_16x16x32_bf16 v[32:35], v[120:123], v[152:155], v[32:35]\n"
    "ds_read_b64 v[70:71], v4 offset:2696\n"
    "v_mfma_f32_16x16x32_bf16 v[36:39], v[120:123], v[156:159], v[36:39]\n"
    "ds_read_b128 v[104:107], v5 offset:3392\n"
    "v_mfma_f32_16x16x32_bf16 v[40:43], v[120:123], v[160:163], v[40:43]\n"
    "ds_read_b64 v[72:73], v4 offset:3200\n"
    "v_mfma_f32_16x16x32_bf16 v[44:47], v[120:123], v[164:167], v[44:47]\n"
    "ds_read_b64 v[74:75], v4 offset:3208\n"
    "v_mfma_f32_16x16x32_bf16 v[28:31], v[124:127], v[144:147], v[28:31]\n"
    "ds_read_b128 v[108:111], v5 offset:3904\n"
    "v_mfma_f32_16x16x32_bf16 v[32:35], v[124:127], v[148:151], v[32:35]\n"
    "ds_read_b64 v[76:77], v4 offset:3712\n"
    "v_mfma_f32_16x16x32_bf16 v[36:39], v[124:127], v[152:155], v[36:39]\n"
    "ds_read_b64 v[78:79], v4 offset:3720\n"
    "v_mfma_f32_16x16x32_bf16 v[40:43], v[124:127], v[156:159], v[40:43]\n"
    "v_mfma_f32_16x16x32_bf16 v[44:47], v[124:127], v[160:163], v[44:47]\n"
    "v_mfma_f32_16x16x32_bf16 v[32:35], v[128:131], v[144:147], v[32:35]\n"
    "v_mfma_f32_16x16x32_bf16 v[36:39], v[128:131], v[148:151], v[36:39]\n"
    "v_mfma_f32_16x16x32_bf16 v[40:43], v[128:131], v[152:155], v[40:43]\n"
    "v_mfma_f32_16x16x32_bf16 v[44:47], v[128:131], v[156:159], v[44:47]\n"
    "v_mfma_f32_16x16x32_bf16 v[36:39], v[132:135], v[144:147], v[36:39]\n"
    "v_mfma_f32_16x16x32_bf16 v[40:43], v[132:135], v[148:151], v[40:43]\n"
    "v_mfma_f32_16x16x32_bf16 v[44:47], v[132:135], v[152:155], v[44:47]\n"
    "v_mfma_f32_16x16x32_bf16 v[40:43], v[136:139], v[144:147], v[40:43]\n"
    "v_mfma_f32_16x16x32_bf16 v[44:47], v[136:139], v[148:151], v[44:47]\n"
    "v_mfma_f32_16x16x32_bf16 v[44:47], v[140:143], v[144:147], v[44:47]\n"
    "s_waitcnt lgkmcnt(0)\n"
    "v_mfma_f32_16x16x32_bf16 v[16:19], v[48:51], v[80:83], v[16:19]\n"
    "ds_read_b128 v[144:147], v5 offset:256\n"
    "v_mfma_f32_16x16x32_bf16 v[20:23], v[48:51], v[84:87], v[20:23]\n"
    "ds_read_b64 v[112:113], v4 offset:192\n"
    "v_mfma_f32_16x16x32_bf16 v[24:27], v[48:51], v[88:91], v[24:27]\n"
    "ds_read_b64 v[114:115], v4 offset:200\n"
    "v_mfma_f32_16x16x32_bf16 v[28:31], v[48:51], v[92:95], v[28:31]\n"
    "ds_read_b128 v[148:151], v5 offset:768\n"
    "v_mfma_f32_16x16x32_bf16 v[32:35], v[48:51], v[96:99], v[32:35]\n"
    "ds_read_b64 v[116:117], v4 offset:704\n"
    "v_mfma_f32_16x16x32_bf16 v[36:39], v[48:51], v[100:103], v[36:39]\n"
    "ds_read_b64 v[118:119], v4 offset:712\n"
    "v_mfma_f32_16x16x32_bf16 v[40:43], v[48:51], v[104:107], v[40:43]\n"
    "ds_read_b128 v[152:155], v5 offset:1280\n"
    "v_mfma_f32_16x16x32_bf16 v[44:47], v[48:51], v[108:111], v[44:47]\n"
    "ds_read_b64 v[120:121], v4 offset:1216\n"
    "v_mfma_f32_16x16x32_bf16 v[20:23], v[52:55], v[80:83], v[20:23]\n"
    "ds_read_b64 v[122:123], v4 offset:1224\n"
    "v_mfma_f32_16x16x32_bf16 v[24:27], v[52:55], v[84:87], v[24:27]\n"
    "ds_read_b128 v[156:159], v5 offset:1792\n"
    "v_mfma_f32_16x16x32_bf16 v[28:31], v[52:55], v[88:91], v[28:31]\n"
    "ds_read_b64 v[124:125], v4 offset:1728\n"
    "v_mfma_f32_16x16x32_bf16 v[32:35], v[52:55], v[92:95], v[32:35]\n"
    "ds_read_b64 v[126:127], v4 offset:1736\n"
    "v_mfma_f32_16x16x32_bf16 v[36:39], v[52:55], v[96:99], v[36:39]\n"
    "ds_read_b128 v[160:163], v5 offset:2304\n"
    "s_waitcnt vmcnt(0)\n"
    "v_cmp_ne_u64 vcc, s[60:61], v[208:209]\n"
    "s_cbranch_vccnz Lslow_15_%=\n"
    "Lback_16_%=:\n"
    "v_mfma_f32_16x16x32_bf16 v[40:43], v[52:55], v[100:103], v[40:43]\n"
    "ds_read_b64 v[128:129], v4 offset:2240\n"
    "s_and_b32 s88, s55, 7\n"
    "v_mfma_f32_16x16x32_bf16 v[44:47], v[52:55], v[104:107], v[44:47]\n"
    "ds_read_b64 v[130:131], v4 offset:2248\n"
    "s_mul_i32 s88, s88, 286720\n"
    "v_mfma_f32_16x16x32_bf16 v[24:27], v[56:59], v[80:83], v[24:27]\n"
    "ds_read_b128 v[164:167], v5 offset:2816\n"
    "s_add_u32 s88, s88, 9850880\n"
    "v_mfma_f32_16x16x32_bf16 v[28:31], v[56:59], v[84:87], v[28:31]\n"
    "ds_read_b64 v[132:133], v4 offset:2752\n"
    "v_add_u32 v228, 4096, v1\n"
    "v_mfma_f32_16x16x32_bf16 v[32:35], v[56:59], v[88:91], v[32:35]\n"
    "ds_read_b64 v[134:135], v4 offset:2760\n"
    "s_mul_i32 s89, s64, 5\n"
    "v_mfma_f32_16x16x32_bf16 v[36:39], v[56:59], v[92:95], v[36:39]\n"
    "ds_read_b128 v[168:171], v5 offset:3328\n"
    "s_add_u32 s89, s89, s88\n"
    "v_mfma_f32_16x16x32_bf16 v[40:43], v[56:59], v[96:99], v[40:43]\n"
    "ds_read_b64 v[136:137], v4 offset:3264\n"
    "buffer_load_dwordx4 a[0:3], v1, s[12:15], s89 offen offset:0 sc1\n"
    "v_mfma_f32_16x16x32_bf16 v[44:47], v[56:59], v[100:103], v[44:47]\n"
    "ds_read_b64 v[138:139], v4 offset:3272\n"
    "v_mfma_f32_16x16x32_bf16 v[28:31], v[60:63], v[80:83], v[28:31]\n"
    "ds_read_b128 v[172:175], v5 offset:3840\n"
    "v_mfma_f32_16x16x32_bf16 v[32:35], v[60:63], v[84:87], v[32:35]\n"
    "ds_read_b64 v[140:141], v4 offset:3776\n"
    "v_mfma_f32_16x16x32_bf16 v[36:39], v[60:63], v[88:91], v[36:39]\n"
    "ds_read_b64 v[142:143], v4 offset:3784\n"
    "v_mfma_f32_16x16x32_bf16 v[40:43], v[60:63], v[92:95], v[40:43]\n"
    "buffer_load_dwordx4 a[4:7], v1, s[12:15], s89 offen offset:1024 sc1\n"
    "v_mfma_f32_16x16x32_bf16 v[44:47], v[60:63], v[96:99], v[44:47]\n"
    "v_mfma_f32_16x16x32_bf16 v[32:35], v[64:67], v[80:83], v[32:35]\n"
    "v_mfma_f32_16x16x32_bf16 v[36:39], v[64:67], v[84:87], v[36:39]\n"
    "v_mfma_f32_16x16x32_bf16 v[40:43], v[64:67], v[88:91], v[40:43]\n"
    "v_mfma_f32_16x16x32_bf16 v[44:47], v[64:67], v[92:95], v[44:47]\n"
    "buffer_load_dwordx4 a[8:11], v1, s[12:15], s89 offen offset:2048 sc1\n"
    "v_mfma_f32_16x16x32_bf16 v[36:39], v[68:71], v[80:83], v[36:39]\n"
    "v_mfma_f32_16x16x32_bf16 v[40:43], v[68:71], v[84:87], v[40:43]\n"
    "v_mfma_f32_16x16x32_bf16 v[44:47], v[68:71], v[88:91], v[44:47]\n"
    "v_mfma_f32_16x16x32_bf16 v[40:43], v[72:75], v[80:83], v[40:43]\n"
    "v_mfma_f32_16x16x32_bf16 v[44:47], v[72:75], v[84:87], v[44:47]\n"
    "buffer_load_dwordx4 a[12:15], v1, s[12:15], s89 offen offset:3072 sc1\n"
    "v_mfma_f32_16x16x32_bf16 v[44:47], v[76:79], v[80:83], v[44:47]\n"
    "s_waitcnt lgkmcnt(0)\n"
    "v_mfma_f32_16x16x32_bf16 v[16:19], v[112:115], v[144:147], v[16:19]\n"
    "ds_read_b128 v[80:83], v5 offset:192\n"
    "v_mfma_f32_16x16x32_bf16 v[20:23], v[112:115], v[148:151], v[20:23]\n"
    "ds_read_b64 v[48:49], v4 offset:256\n"
    "v_mfma_f32_16x16x32_bf16 v[24:27], v[112:115], v[152:155], v[24:27]\n"
    "ds_read_b64 v[50:51], v4 offset:264\n"
    "v_mfma_f32_16x16x32_bf16 v[28:31], v[112:115], v[156:159], v[28:31]\n"
    "ds_read_b128 v[84:87], v5 offset:704\n"
    "buffer_load_dwordx4 a[16:19], v228, s[12:15], s89 offen offset:0 sc1\n"
    "v_mfma_f32_16x16x32_bf16 v[32:35], v[112:115], v[160:163], v[32:35]\n"
    "ds_read_b64 v[52:53], v4 offset:768\n"
    "s_mul_i32 s89, s65, 5\n"
    "v_mfma_f32_16x16x32_bf16 v[36:39], v[112:115], v[164:167], v[36:39]\n"
    "ds_read_b64 v[54:55], v4 offset:776\n"
    "s_add_u32 s89, s89, s88\n"
    "v_mfma_f32_16x16x32_bf16 v[40:43], v[112:115], v[168:171], v[40:43]\n"
    "ds_read_b128 v[88:91], v5 offset:1216\n"
    "v_mfma_f32_16x16x32_bf16 v[44:47], v[112:115], v[172:175], v[44:47]\n"
    "ds_read_b64 v[56:57], v4 offset:1280\n"
    "v_mfma_f32_16x16x32_bf16 v[20:23], v[116:119], v[144:147], v[20:23]\n"
    "ds_read_b64 v[58:59], v4 offset:1288\n"
    "buffer_load_dwordx4 a[20:23], v1, s[12:15], s89 offen offset:0 sc1\n"
    "v_mfma_f32_16x16x32_bf16 v[24:27], v[116:119], v[148:151], v[24:27]\n"
    "ds_read_b128 v[92:95], v5 offset:1728\n"
    "v_mfma_f32_16x16x32_bf16 v[28:31], v[116:119], v[152:155], v[28:31]\n"
    "ds_read_b64 v[60:61], v4 offset:1792\n"
    "v_mfma_f32_16x16x32_bf16 v[32:35], v[116:119], v[156:159], v[32:35]\n"
    "ds_read_b64 v[62:63], v4 offset:1800\n"
    "v_mfma_f32_16x16x32_bf16 v[36:39], v[116:119], v[160:163], v[36:39]\n"
    "ds_read_b128 v[96:99], v5 offset:2240\n"
    "v_mfma_f32_16x16x32_bf16 v[40:43], v[116:119], v[164:167], v[40:43]\n"
    "ds_read_b64 v[64:65], v4 offset:2304\n"
    "buffer_load_dwordx4 a[24:27], v1, s[12:15], s89 offen offset:1024 sc1\n"
    "v_mfma_f32_16x16x32_bf16 v[44:47], v[116:119], v[168:171], v[44:47]\n"
    "ds_read_b64 v[66:67], v4 offset:2312\n"
    "v_mfma_f32_16x16x32_bf16 v[24:27], v[120:123], v[144:147], v[24:27]\n"
    "ds_read_b128 v[100:103], v5 offset:2752\n"
    "v_mfma_f32_16x16x32_bf16 v[28:31], v[120:123], v[148:151], v[28:31]\n"
    "ds_read_b64 v[68:69], v4 offset:2816\n"
    "v_mfma_f32_16x16x32_bf16 v[32:35], v[120:123], v[152:155], v[32:35]\n"
    "ds_read_b64 v[70:71], v4 offset:2824\n"
    "v_mfma_f32_16x16x32_bf16 v[36:39], v[120:123], v[156:159], v[36:39]\n"
    "ds_read_b128 v[104:107], v5 offset:3264\n"
    "buffer_load_dwordx4 a[28:31], v1, s[12:15], s89 offen offset:2048 sc1\n"
    "v_mfma_f32_16x16x32_bf16 v[40:43], v[120:123], v[160:163], v[40:43]\n"
    "ds_read_b64 v[72:73], v4 offset:3328\n"
    "v_mfma_f32_16x16x32_bf16 v[44:47], v[120:123], v[164:167], v[44:47]\n"
    "ds_read_b64 v[74:75], v4 offset:3336\n"
    "v_mfma_f32_16x16x32_bf16 v[28:31], v[124:127], v[144:147], v[28:31]\n"
    "ds_read_b128 v[108:111], v5 offset:3776\n"
    "v_mfma_f32_16x16x32_bf16 v[32:35], v[124:127], v[148:151], v[32:35]\n"
    "ds_read_b64 v[76:77], v4 offset:3840\n"
    "v_mfma_f32_16x16x32_bf16 v[36:39], v[124:127], v[152:155], v[36:39]\n"
    "ds_read_b64 v[78:79], v4 offset:3848\n"
    "buffer_load_dwordx4 a[32:35], v1, s[12:15], s89 offen offset:3072 sc1\n"
    "v_mfma_f32_16x16x32_bf16 v[40:43], v[124:127], v[156:159], v[40:43]\n"
    "v_mfma_f32_16x16x32_bf16 v[44:47], v[124:127], v[160:163], v[44:47]\n"
    "v_mfma_f32_16x16x32_bf16 v[32:35], v[128:131], v[144:147], v[32:35]\n"
    "v_mfma_f32_16x16x32_bf16 v[36:39], v[128:131], v[148:151], v[36:39]\n"
    "v_mfma_f32_16x16x32_bf16 v[40:43], v[128:131], v[152:155], v[40:43]\n"
    "buffer_load_dwordx4 a[36:39], v228, s[12:15], s89 offen offset:0 sc1\n"
    "s_mul_i32 s89, s66, 5\n"
    "v_mfma_f32_16x16x32_bf16 v[44:47], v[128:131], v[156:159], v[44:47]\n"
    "s_add_u32 s89, s89, s88\n"
    "v_mfma_f32_16x16x32_bf16 v[36:39], v[132:135], v[144:147], v[36:39]\n"
    "v_mfma_f32_16x16x32_bf16 v[40:43], v[132:135], v[148:151], v[40:43]\n"
    "v_mfma_f32_16x16x32_bf16 v[44:47], v[132:135], v[152:155], v[44:47]\n"
    "v_mfma_f32_16x16x32_bf16 v[40:43], v[136:139], v[144:147], v[40:43]\n"
    "buffer_load_dwordx4 a[40:43], v1, s[12:15], s89 offen offset:0 sc1\n"
    "v_mfma_f32_16x16x32_bf16 v[44:47], v[136:139], v[148:151], v[44:47]\n"
    "v_mfma_f32_16x16x32_bf16 v[44:47], v[140:143], v[144:147], v[44:47]\n"
    "s_waitcnt lgkmcnt(0)\n"
    "v_mfma_f32_16x16x32_bf16 v[16:19], v[48:51], v[80:83], v[16:19]\n"
    "ds_read_b128 v[144:147], v5 offset:128\n"
    "v_mfma_f32_16x16x32_bf16 v[20:23], v[48:51], v[84:87], v[20:23]\n"
    "ds_read_b64 v[112:113], v4 offset:320\n"
    "v_mfma_f32_16x16x32_bf16 v[24:27], v[48:51], v[88:91], v[24:27]\n"
    "ds_read_b64 v[114:115], v4 offset:328\n"
    "buffer_load_dwordx4 a[44:47], v1, s[12:15], s89 offen offset:1024 sc1\n"
    "v_mfma_f32_16x16x32_bf16 v[28:31], v[48:51], v[92:95], v[28:31]\n"
    "ds_read_b128 v[148:151], v5 offset:640\n"
    "v_mfma_f32_16x16x32_bf16 v[32:35], v[48:51], v[96:99], v[32:35]\n"
    "ds_read_b64 v[116:117], v4 offset:832\n"
    "v_mfma_f32_16x16x32_bf16 v[36:39], v[48:51], v[100:103], v[36:39]\n"
    "ds_read_b64 v[118:119], v4 offset:840\n"
    "v_mfma_f32_16x16x32_bf16 v[40:43], v[48:51], v[104:107], v[40:43]\n"
    "ds_read_b128 v[152:155], v5 offset:1152\n"
    "v_mfma_f32_16x16x32_bf16 v[44:47], v[48:51], v[108:111], v[44:47]\n"
    "ds_read_b64 v[120:121], v4 offset:1344\n"
    "buffer_load_dwordx4 a[48:51], v1, s[12:15], s89 offen offset:2048 sc1\n"
    "v_mfma_f32_16x16x32_bf16 v[20:23], v[52:55], v[80:83], v[20:23]\n"
    "ds_read_b64 v[122:123], v4 offset:1352\n"
    "v_mfma_f32_16x16x32_bf16 v[24:27], v[52:55], v[84:87], v[24:27]\n"
    "ds_read_b128 v[156:159], v5 offset:1664\n"
    "v_mfma_f32_16x16x32_bf16 v[28:31], v[52:55], v[88:91], v[28:31]\n"
    "ds_read_b64 v[124:125], v4 offset:1856\n"
    "v_mfma_f32_16x16x32_bf16 v[32:35], v[52:55], v[92:95], v[32:35]\n"
    "ds_read_b64 v[126:127], v4 offset:1864\n"
    "v_mfma_f32_16x16x32_bf16 v[36:39], v[52:55], v[96:99], v[36:39]\n"
    "ds_read_b128 v[160:163], v5 offset:2176\n"
    "buffer_load_dwordx4 a[52:55], v1, s[12:15], s89 offen offset:3072 sc1\n"
    "v_mfma_f32_16x16x32_bf16 v[40:43], v[52:55], v[100:103], v[40:43]\n"
    "ds_read_b64 v[128:129], v4 offset:2368\n"
    "v_mfma_f32_16x16x32_bf16 v[44:47], v[52:55], v[104:107], v[44:47]\n"
    "ds_read_b64 v[130:131], v4 offset:2376\n"
    "v_mfma_f32_16x16x32_bf16 v[24:27], v[56:59], v[80:83], v[24:27]\n"
    "ds_read_b128 v[164:167], v5 offset:2688\n"
    "v_mfma_f32_16x16x32_bf16 v[28:31], v[56:59], v[84:87], v[28:31]\n"
    "ds_read_b64 v[132:133], v4 offset:2880\n"
    "v_mfma_f32_16x16x32_bf16 v[32:35], v[56:59], v[88:91], v[32:35]\n"
    "ds_read_b64 v[134:135], v4 offset:2888\n"
    "buffer_load_dwordx4 a[56:59], v228, s[12:15], s89 offen offset:0 sc1\n"
    "v_mfma_f32_16x16x32_bf16 v[36:39], v[56:59], v[92:95], v[36:39]\n"
    "ds_read_b128 v[168:171], v5 offset:3200\n"
    "s_mul_i32 s89, s67, 5\n"
    "v_mfma_f32_16x16x32_bf16 v[40:43], v[56:59], v[96:99], v[40:43]\n"
    "ds_read_b64 v[136:137], v4 offset:3392\n"
    "s_add_u32 s89, s89, s88\n"
    "v_mfma_f32_16x16x32_bf16 v[44:47], v[56:59], v[100:103], v[44:47]\n"
    "ds_read_b64 v[138:139], v4 offset:3400\n"
    "v_mfma_f32_16x16x32_bf16 v[28:31], v[60:63], v[80:83], v[28:31]\n"
    "ds_read_b128 v[172:175], v5 offset:3712\n"
    "v_mfma_f32_16x16x32_bf16 v[32:35], v[60:63], v[84:87], v[32:35]\n"
    "ds_read_b64 v[140:141], v4 offset:3904\n"
    "buffer_load_dwordx4 a[60:63], v1, s[12:15], s89 offen offset:0 sc1\n"
    "v_mfma_f32_16x16x32_bf16 v[36:39], v[60:63], v[88:91], v[36:39]\n"
    "ds_read_b64 v[142:143], v4 offset:3912\n"
    "v_mfma_f32_16x16x32_bf16 v[40:43], v[60:63], v[92:95], v[40:43]\n"
    "v_mfma_f32_16x16x32_bf16 v[44:47], v[60:63], v[96:99], v[44:47]\n"
    "v_mfma_f32_16x16x32_bf16 v[32:35], v[64:67], v[80:83], v[32:35]\n"
    "v_mfma_f32_16x16x32_bf16 v[36:39], v[64:67], v[84:87], v[36:39]\n"
    "buffer_load_dwordx4 a[64:67], v1, s[12:15], s89 offen offset:1024 sc1\n"
    "v_mfma_f32_16x16x32_bf16 v[40:43], v[64:67], v[88:91], v[40:43]\n"
    "v_mfma_f32_16x16x32_bf16 v[44:47], v[64:67], v[92:95], v[44:47]\n"
    "v_mfma_f32_16x16x32_bf16 v[36:39], v[68:71], v[80:83], v[36:39]\n"
    "v_mfma_f32_16x16x32_bf16 v[40:43], v[68:71], v[84:87], v[40:43]\n"
    "v_mfma_f32_16x16x32_bf16 v[44:47], v[68:71], v[88:91], v[44:47]\n"
    "buffer_load_dwordx2 v[210:211], v12, s[12:15], s91 offen offset:0 sc1\n"
    "v_mfma_f32_16x16x32_bf16 v[40:43], v[72:75], v[80:83], v[40:43]\n"
    "v_mfma_f32_16x16x32_bf16 v[44:47], v[72:75], v[84:87], v[44:47]\n"
    "v_mfma_f32_16x16x32_bf16 v[44:47], v[76:79], v[80:83], v[44:47]\n"
    "s_waitcnt lgkmcnt(0)\n"
    "v_mfma_f32_16x16x32_bf16 v[16:19], v[112:115], v[144:147], v[16:19]\n"
    "ds_read_b128 v[80:83], v5 offset:64\n"
    "v_mfma_f32_16x16x32_bf16 v[20:23], v[112:115], v[148:151], v[20:23]\n"
    "ds_read_b64 v[48:49], v4 offset:384\n"
    "buffer_load_dwordx2 v[226:227], v12, s[12:15], s90 offen offset:0 sc1\n"
    "v_mfma_f32_16x16x32_bf16 v[24:27], v[112:115], v[152:155], v[24:27]\n"
    "ds_read_b64 v[50:51], v4 offset:392\n"
    "v_mfma_f32_16x16x32_bf16 v[28:31], v[112:115], v[156:159], v[28:31]\n"
    "ds_read_b128 v[84:87], v5 offset:576\n"
    "v_mfma_f32_16x16x32_bf16 v[32:35], v[112:115], v[160:163], v[32:35]\n"
    "ds_read_b64 v[52:53], v4 offset:896\n"
    "v_mfma_f32_16x16x32_bf16 v[36:39], v[112:115], v[164:167], v[36:39]\n"
    "ds_read_b64 v[54:55], v4 offset:904\n"
    "v_mfma_f32_16x16x32_bf16 v[40:43], v[112:115], v[168:171], v[40:43]\n"
    "ds_read_b128 v[88:91], v5 offset:1088\n"
    "buffer_load_dwordx4 a[68:71], v1, s[12:15], s89 offen offset:2048 sc1\n"
    "v_mfma_f32_16x16x32_bf16 v[44:47], v[112:115], v[172:175], v[44:47]\n"
    "ds_read_b64 v[56:57], v4 offset:1408\n"
    "v_mfma_f32_16x16x32_bf16 v[20:23], v[116:119], v[144:147], v[20:23]\n"
    "ds_read_b64 v[58:59], v4 offset:1416\n"
    "v_mfma_f32_16x16x32_bf16 v[24:27], v[116:119], v[148:151], v[24:27]\n"
    "ds_read_b128 v[92:95], v5 offset:1600\n"
    "v_mfma_f32_16x16x32_bf16 v[28:31], v[116:119], v[152:155], v[28:31]\n"
    "ds_read_b64 v[60:61], v4 offset:1920\n"
    "v_mfma_f32_16x16x32_bf16 v[32:35], v[116:119], v[156:159], v[32:35]\n"
    "ds_read_b64 v[62:63], v4 offset:1928\n"
    "buffer_load_dwordx4 a[72:75], v1, s[12:15], s89 offen offset:3072 sc1\n"
    "v_mfma_f32_16x16x32_bf16 v[36:39], v[116:119], v[160:163], v[36:39]\n"
    "ds_read_b128 v[96:99], v5 offset:2112\n"
    "v_mfma_f32_16x16x32_bf16 v[40:43], v[116:119], v[164:167], v[40:43]\n"
    "ds_read_b64 v[64:65], v4 offset:2432\n"
    "v_mfma_f32_16x16x32_bf16 v[44:47], v[116:119], v[168:171], v[44:47]\n"
    "ds_read_b64 v[66:67], v4 offset:2440\n"
    "v_mfma_f32_16x16x32_bf16 v[24:27], v[120:123], v[144:147], v[24:27]\n"
    "ds_read_b128 v[100:103], v5 offset:2624\n"
    "v_mfma_f32_16x16x32_bf16 v[28:31], v[120:123], v[148:151], v[28:31]\n"
    "ds_read_b64 v[68:69], v4 offset:2944\n"
    "buffer_load_dwordx4 a[76:79], v228, s[12:15], s89 offen offset:0 sc1\n"
    "v_mfma_f32_16x16x32_bf16 v[32:35], v[120:123], v[152:155], v[32:35]\n"
    "ds_read_b64 v[70:71], v4 offset:2952\n"
    "s_mul_i32 s89, s68, 5\n"
    "v_mfma_f32_16x16x32_bf16 v[36:39], v[120:123], v[156:159], v[36:39]\n"
    "ds_read_b128 v[104:107], v5 offset:3136\n"
    "s_add_u32 s89, s89, s88\n"
    "v_mfma_f32_16x16x32_bf16 v[40:43], v[120:123], v[160:163], v[40:43]\n"
    "ds_read_b64 v[72:73], v4 offset:3456\n"
    "v_mfma_f32_16x16x32_bf16 v[44:47], v[120:123], v[164:167], v[44:47]\n"
    "ds_read_b64 v[74:75], v4 offset:3464\n"
    "v_mfma_f32_16x16x32_bf16 v[28:31], v[124:127], v[144:147], v[28:31]\n"
    "ds_read_b128 v[108:111], v5 offset:3648\n"
    "buffer_load_dwordx4 a[80:83], v1, s[12:15], s89 offen offset:0 sc1\n"
    "v_mfma_f32_16x16x32_bf16 v[32:35], v[124:127], v[148:151], v[32:35]\n"
    "ds_read_b64 v[76:77], v4 offset:3968\n"
    "v_mfma_f32_16x16x32_bf16 v[36:39], v[124:127], v[152:155], v[36:39]\n"
    "ds_read_b64 v[78:79], v4 offset:3976\n"
    "v_mfma_f32_16x16x32_bf16 v[40:43], v[124:127], v[156:159], v[40:43]\n"
    "v_mfma_f32_16x16x32_bf16 v[44:47], v[124:127], v[160:163], v[44:47]\n"
    "v_mfma_f32_16x16x32_bf16 v[32:35], v[128:131], v[144:147], v[32:35]\n"
    "buffer_load_dwordx4 a[84:87], v1, s[12:15], s89 offen offset:1024 sc1\n"
    "v_mfma_f32_16x16x32_bf16 v[36:39], v[128:131], v[148:151], v[36:39]\n"
    "v_mfma_f32_16x16x32_bf16 v[40:43], v[128:131], v[152:155], v[40:43]\n"
    "v_mfma_f32_16x16x32_bf16 v[44:47], v[128:131], v[156:159], v[44:47]\n"
    "v_mfma_f32_16x16x32_bf16 v[36:39], v[132:135], v[144:147], v[36:39]\n"
    "v_mfma_f32_16x16x32_bf16 v[40:43], v[132:135], v[148:151], v[40:43]\n"
    "buffer_load_dwordx4 a[88:91], v1, s[12:15], s89 offen offset:2048 sc1\n"
    "v_mfma_f32_16x16x32_bf16 v[44:47], v[132:135], v[152:155], v[44:47]\n"
    "v_mfma_f32_16x16x32_bf16 v[40:43], v[136:139], v[144:147], v[40:43]\n"
    "v_mfma_f32_16x16x32_bf16 v[44:47], v[136:139], v[148:151], v[44:47]\n"
    "v_mfma_f32_16x16x32_bf16 v[44:47], v[140:143], v[144:147], v[44:47]\n"
    "s_waitcnt lgkmcnt(0)\n"
    "v_mfma_f32_16x16x32_bf16 v[16:19], v[48:51], v[80:83], v[16:19]\n"
    "ds_read_b128 v[144:147], v5 offset:0\n"
    "buffer_load_dwordx4 a[92:95], v1, s[12:15], s89 offen offset:3072 sc1\n"
    "v_mfma_f32_16x16x32_bf16 v[20:23], v[48:51], v[84:87], v[20:23]\n"
    "ds_read_b64 v[112:113], v4 offset:448\n"
    "v_mfma_f32_16x16x32_bf16 v[24:27], v[48:51], v[88:91], v[24:27]\n"
    "ds_read_b64 v[114:115], v4 offset:456\n"
    "v_mfma_f32_16x16x32_bf16 v[28:31], v[48:51], v[92:95], v[28:31]\n"
    "ds_read_b128 v[148:151], v5 offset:512\n"
    "v_mfma_f32_16x16x32_bf16 v[32:35], v[48:51], v[96:99], v[32:35]\n"
    "ds_read_b64 v[116:117], v4 offset:960\n"
    "v_mfma_f32_16x16x32_bf16 v[36:39], v[48:51], v[100:103], v[36:39]\n"
    "ds_read_b64 v[118:119], v4 offset:968\n"
    "buffer_load_dwordx4 a[96:99], v228, s[12:15], s89 offen offset:0 sc1\n"
    "v_mfma_f32_16x16x32_bf16 v[40:43], v[48:51], v[104:107], v[40:43]\n"
    "ds_read_b128 v[152:155], v5 offset:1024\n"
    "v_mfma_f32_16x16x32_bf16 v[44:47], v[48:51], v[108:111], v[44:47]\n"
    "ds_read_b64 v[120:121], v4 offset:1472\n"
    "v_mfma_f32_16x16x32_bf16 v[20:23], v[52:55], v[80:83], v[20:23]\n"
    "ds_read_b64 v[122:123], v4 offset:1480\n"
    "v_mfma_f32_16x16x32_bf16 v[24:27], v[52:55], v[84:87], v[24:27]\n"
    "ds_read_b128 v[156:159], v5 offset:1536\n"
    "v_mfma_f32_16x16x32_bf16 v[28:31], v[52:55], v[88:91], v[28:31]\n"
    "ds_read_b64 v[124:125], v4 offset:1984\n"
    "v_mfma_f32_16x16x32_bf16 v[32:35], v[52:55], v[92:95], v[32:35]\n"
    "ds_read_b64 v[126:127], v4 offset:1992\n"
    "v_mfma_f32_16x16x32_bf16 v[36:39], v[52:55], v[96:99], v[36:39]\n"
    "ds_read_b128 v[160:163], v5 offset:2048\n"
    "v_mfma_f32_16x16x32_bf16 v[40:43], v[52:55], v[100:103], v[40:43]\n"
    "ds_read_b64 v[128:129], v4 offset:2496\n"
    "v_mfma_f32_16x16x32_bf16 v[44:47], v[52:55], v[104:107], v[44:47]\n"
    "ds_read_b64 v[130:131], v4 offset:2504\n"
    "v_mfma_f32_16x16x32_bf16 v[24:27], v[56:59], v[80:83], v[24:27]\n"
    "ds_read_b128 v[164:167], v5 offset:2560\n"
    "s_waitcnt vmcnt(9)\n"
    "v_cmp_ne_u64 vcc, s[60:61], v[210:211]\n"
    "s_cbranch_vccnz Lslow_19_%=\n"
    "Lback_20_%=:\n"
    "v_mfma_f32_16x16x32_bf16 v[28:31], v[56:59], v[84:87], v[28:31]\n"
    "ds_read_b64 v[132:133], v4 offset:3008\n"
    "buffer_load_dwordx4 v[176:179], v229, s[12:15], s4 offen offset:0 sc1\n"
    "v_mfma_f32_16x16x32_bf16 v[32:35], v[56:59], v[88:91], v[32:35]\n"
    "ds_read_b64 v[134:135], v4 offset:3016\n"
    "v_mfma_f32_16x16x32_bf16 v[36:39], v[56:59], v[92:95], v[36:39]\n"
    "ds_read_b128 v[168:171], v5 offset:3072\n"
    "v_mfma_f32_16x16x32_bf16 v[40:43], v[56:59], v[96:99], v[40:43]\n"
    "ds_read_b64 v[136:137], v4 offset:3520\n"
    "v_mfma_f32_16x16x32_bf16 v[44:47], v[56:59], v[100:103], v[44:47]\n"
    "ds_read_b64 v[138:139], v4 offset:3528\n"
    "s_waitcnt vmcnt(9)\n"
    "v_cmp_ne_u64 vcc, s[60:61], v[226:227]\n"
    "s_cbranch_vccnz Lslow_23_%=\n"
    "Lback_24_%=:\n"
    "v_mfma_f32_16x16x32_bf16 v[28:31], v[60:63], v[80:83], v[28:31]\n"
    "ds_read_b128 v[172:175], v5 offset:3584\n"
    "buffer_load_dwordx4 v[180:183], v229, s[12:15], s4 offen offset:1024 sc1\n"
    "v_mfma_f32_16x16x32_bf16 v[32:35], v[60:63], v[84:87], v[32:35]\n"
    "ds_read_b64 v[140:141], v4 offset:4032\n"
    "s_mul_i32 s86, s5, 143360\n"
    "v_mfma_f32_16x16x32_bf16 v[36:39], v[60:63], v[88:91], v[36:39]\n"
    "ds_read_b64 v[142:143], v4 offset:4040\n"
    "s_mul_i32 s87, s9, 7168\n"
    "v_mfma_f32_16x16x32_bf16 v[40:43], v[60:63], v[92:95], v[40:43]\n"
    "s_add_u32 s86, s86, s87\n"
    "s_add_u32 s86, s86, 7524352\n"
    "v_mfma_f32_16x16x32_bf16 v[44:47], v[60:63], v[96:99], v[44:47]\n"
    "s_mul_i32 s87, s78, 7\n"
    "s_add_u32 s87, s87, s86\n"
    "v_mfma_f32_16x16x32_bf16 v[32:35], v[64:67], v[80:83], v[32:35]\n"
    "buffer_load_dwordx4 v[184:187], v229, s[12:15], s4 offen offset:2048 sc1\n"
    "s_add_u32 m0, s97, 28672\n"
    "v_mfma_f32_16x16x32_bf16 v[36:39], v[64:67], v[84:87], v[36:39]\n"
    "v_mfma_f32_16x16x32_bf16 v[40:43], v[64:67], v[88:91], v[40:43]\n"
    "v_mfma_f32_16x16x32_bf16 v[44:47], v[64:67], v[92:95], v[44:47]\n"
    "v_mfma_f32_16x16x32_bf16 v[36:39], v[68:71], v[80:83], v[36:39]\n"
    "v_mfma_f32_16x16x32_bf16 v[40:43], v[68:71], v[84:87], v[40:43]\n"
    "buffer_load_dwordx4 v[188:191], v229, s[12:15], s4 offen offset:3072 sc1\n"
    "s_lshl_b32 s92, s9, 12\n"
    "v_mfma_f32_16x16x32_bf16 v[44:47], v[68:71], v[88:91], v[44:47]\n"
    "s_add_u32 s92, s92, 7507968\n"
    "v_mfma_f32_16x16x32_bf16 v[40:43], v[72:75], v[80:83], v[40:43]\n"
    "v_mfma_f32_16x16x32_bf16 v[44:47], v[72:75], v[84:87], v[44:47]\n"
    "v_mfma_f32_16x16x32_bf16 v[44:47], v[76:79], v[80:83], v[44:47]\n"
    "s_waitcnt lgkmcnt(0)\n"
    "v_mfma_f32_16x16x32_bf16 v[16:19], v[112:115], v[144:147], v[16:19]\n"
    "ds_read_b64 v[212:213], v6 offset:0\n"
    "buffer_load_dwordx4 v[192:195], v229, s[12:15], s92 offen offset:0 sc1\n"
    "v_mfma_f32_16x16x32_bf16 v[20:23], v[112:115], v[148:151], v[20:23]\n"
    "ds_read_b64 v[214:215], v6 offset:512\n"
    "v_mfma_f32_16x16x32_bf16 v[24:27], v[112:115], v[152:155], v[24:27]\n"
    "ds_read_b64 v[216:217], v6 offset:1024\n"
    "v_mfma_f32_16x16x32_bf16 v[28:31], v[112:115], v[156:159], v[28:31]\n"
    "ds_read_b64 v[218:219], v6 offset:1536\n"
    "v_mfma_f32_16x16x32_bf16 v[32:35], v[112:115], v[160:163], v[32:35]\n"
    "ds_read_b64 v[220:221], v6 offset:2048\n"
    "v_mfma_f32_16x16x32_bf16 v[36:39], v[112:115], v[164:167], v[36:39]\n"
    "ds_read_b64 v[222:223], v6 offset:2560\n"
    "buffer_load_dwordx4 v[196:199], v229, s[12:15], s92 offen offset:1024 sc1\n"
    "v_mfma_f32_16x16x32_bf16 v[40:43], v[112:115], v[168:171], v[40:43]\n"
    "ds_read_b64 v[224:225], v6 offset:3072\n"
    "v_mfma_f32_16x16x32_bf16 v[44:47], v[112:115], v[172:175], v[44:47]\n"
    "ds_read_b64 v[226:227], v6 offset:3584\n"
    "v_mfma_f32_16x16x32_bf16 v[20:23], v[116:119], v[144:147], v[20:23]\n"
    "v_mfma_f32_16x16x32_bf16 v[24:27], v[116:119], v[148:151], v[24:27]\n"
    "s_waitcnt lgkmcnt(7)\n"
    "v_and_b32 v48, 0xffff0000, v213\n"
    "v_lshlrev_b32 v49, 16, v213\n"
    "v_mfma_f32_16x16x32_bf16 v[28:31], v[116:119], v[152:155], v[28:31]\n"
    "buffer_load_dwordx4 v[200:203], v229, s[12:15], s92 offen offset:2048 sc1\n"
    "v_and_b32 v50, 0xffff0000, v212\n"
    "v_mfma_f32_16x16x32_bf16 v[32:35], v[116:119], v[156:159], v[32:35]\n"
    "v_lshlrev_b32 v51, 16, v212\n"
    "v_fmac_f32 v16, s98, v48\n"
    "v_mfma_f32_16x16x32_bf16 v[36:39], v[116:119], v[160:163], v[36:39]\n"
    "v_fmac_f32 v17, s98, v49\n"
    "v_fmac_f32 v18, s98, v50\n"
    "v_mfma_f32_16x16x32_bf16 v[40:43], v[116:119], v[164:167], v[40:43]\n"
    "v_fmac_f32 v19, s98, v51\n"
    "v_cvt_pk_bf16_f32 v80, v16, v17\n"
    "v_mfma_f32_16x16x32_bf16 v[44:47], v[116:119], v[168:171], v[44:47]\n"
    "v_cvt_pk_bf16_f32 v81, v18, v19\n"
    "ds_write_b64 v7, v[80:81] offset:0\n"
    "v_mfma_f32_16x16x32_bf16 v[24:27], v[120:123], v[144:147], v[24:27]\n"
    "buffer_load_dwordx4 v[204:207], v229, s[12:15], s92 offen offset:3072 sc1\n"
    "s_waitcnt lgkmcnt(7)\n"
    "v_and_b32 v52, 0xffff0000, v215\n"
    "v_mfma_f32_16x16x32_bf16 v[28:31], v[120:123], v[148:151], v[28:31]\n"
    "v_lshlrev_b32 v53, 16, v215\n"
    "v_and_b32 v54, 0xffff0000, v214\n"
    "v_mfma_f32_16x16x32_bf16 v[32:35], v[120:123], v[152:155], v[32:35]\n"
    "v_lshlrev_b32 v55, 16, v214\n"
    "v_fmac_f32 v20, s98, v52\n"
    "v_mfma_f32_16x16x32_bf16 v[36:39], v[120:123], v[156:159], v[36:39]\n"
    "v_fmac_f32 v21, s98, v53\n"
    "v_fmac_f32 v22, s98, v54\n"
    "v_mfma_f32_16x16x32_bf16 v[40:43], v[120:123], v[160:163], v[40:43]\n"
    "v_fmac_f32 v23, s98, v55\n"
    "v_cvt_pk_bf16_f32 v82, v20, v21\n"
    "v_mfma_f32_16x16x32_bf16 v[44:47], v[120:123], v[164:167], v[44:47]\n"
    "buffer_load_dwordx4 v1, s[12:15], s87 offen offset:0 sc1 lds\n"
    "v_cvt_pk_bf16_f32 v83, v22, v23\n"
    "v_mfma_f32_16x16x32_bf16 v[28:31], v[124:127], v[144:147], v[28:31]\n"
    "ds_write_b64 v7, v[82:83] offset:512\n"
    "s_waitcnt lgkmcnt(7)\n"
    "v_and_b32 v56, 0xffff0000, v217\n"
    "v_mfma_f32_16x16x32_bf16 v[32:35], v[124:127], v[148:151], v[32:35]\n"
    "v_lshlrev_b32 v57, 16, v217\n"
    "v_and_b32 v58, 0xffff0000, v216\n"
    "v_mfma_f32_16x16x32_bf16 v[36:39], v[124:127], v[152:155], v[36:39]\n"
    "v_lshlrev_b32 v59, 16, v216\n"
    "v_fmac_f32 v24, s98, v56\n"
    "v_mfma_f32_16x16x32_bf16 v[40:43], v[124:127], v[156:159], v[40:43]\n"
    "v_fmac_f32 v25, s98, v57\n"
    "v_fmac_f32 v26, s98, v58\n"
    "v_mfma_f32_16x16x32_bf16 v[44:47], v[124:127], v[160:163], v[44:47]\n"
    "buffer_load_dwordx4 v1, s[12:15], s87 offen offset:1024 sc1 lds\n"
    "v_fmac_f32 v27, s98, v59\n"
    "v_mfma_f32_16x16x32_bf16 v[32:35], v[128:131], v[144:147], v[32:35]\n"
    "v_cvt_pk_bf16_f32 v84, v24, v25\n"
    "v_cvt_pk_bf16_f32 v85, v26, v27\n"
    "v_mfma_f32_16x16x32_bf16 v[36:39], v[128:131], v[148:151], v[36:39]\n"
    "ds_write_b64 v7, v[84:85] offset:1024\n"
    "s_waitcnt lgkmcnt(7)\n"
    "v_and_b32 v60, 0xffff0000, v219\n"
    "v_mfma_f32_16x16x32_bf16 v[40:43], v[128:131], v[152:155], v[40:43]\n"
    "v_lshlrev_b32 v61, 16, v219\n"
    "v_and_b32 v62, 0xffff0000, v218\n"
    "v_mfma_f32_16x16x32_bf16 v[44:47], v[128:131], v[156:159], v[44:47]\n"
    "v_lshlrev_b32 v63, 16, v218\n"
    "s_waitcnt lgkmcnt(6)\n"
    "v_and_b32 v64, 0xffff0000, v221\n"
    "v_mfma_f32_16x16x32_bf16 v[36:39], v[132:135], v[144:147], v[36:39]\n"
    "buffer_load_dwordx4 v1, s[12:15], s87 offen offset:2048 sc1 lds\n"
    "v_lshlrev_b32 v65, 16, v221\n"
    "v_mfma_f32_16x16x32_bf16 v[40:43], v[132:135], v[148:151], v[40:43]\n"
    "v_and_b32 v66, 0xffff0000, v220\n"
    "v_lshlrev_b32 v67, 16, v220\n"
    "v_mfma_f32_16x16x32_bf16 v[44:47], v[132:135], v[152:155], v[44:47]\n"
    "s_waitcnt lgkmcnt(5)\n"
    "v_and_b32 v68, 0xffff0000, v223\n"
    "v_lshlrev_b32 v69, 16, v223\n"
    "v_mfma_f32_16x16x32_bf16 v[40:43], v[136:139], v[144:147], v[40:43]\n"
    "v_and_b32 v70, 0xffff0000, v222\n"
    "v_lshlrev_b32 v71, 16, v222\n"
    "v_mfma_f32_16x16x32_bf16 v[44:47], v[136:139], v[148:151], v[44:47]\n"
    "s_waitcnt lgkmcnt(4)\n"
    "v_and_b32 v72, 0xffff0000, v225\n"
    "v_lshlrev_b32 v73, 16, v225\n"
    "v_mfma_f32_16x16x32_bf16 v[44:47], v[140:143], v[144:147], v[44:47]\n"
    "buffer_load_dwordx4 v1, s[12:15], s87 offen offset:3072 sc1 lds\n"
    "s_add_u32 s87, s87, 4096\n"
    "s_add_u32 m0, s97, 32768\n"
    "s_nop 0\n"
    "buffer_load_dwordx4 v1, s[12:15], s87 offen offset:0 sc1 lds\n"
    "buffer_load_dwordx4 v1, s[12:15], s87 offen offset:1024 sc1 lds\n"
    "buffer_load_dwordx4 v1, s[12:15], s87 offen offset:2048 sc1 lds\n"
    "v_and_b32 v74, 0xffff0000, v224\n"
    "v_lshlrev_b32 v75, 16, v224\n"
    "s_waitcnt lgkmcnt(3)\n"
    "v_and_b32 v76, 0xffff0000, v227\n"
    "v_lshlrev_b32 v77, 16, v227\n"
    "v_and_b32 v78, 0xffff0000, v226\n"
    "v_lshlrev_b32 v79, 16, v226\n"
    "s_mov_b32 s99, s98\n"
    "v_pk_fma_f32 v[28:29], v[60:61], s[98:99], v[28:29]\n"
    "v_pk_fma_f32 v[30:31], v[62:63], s[98:99], v[30:31]\n"
    "v_cvt_pk_bf16_f32 v86, v28, v29\n"
    "v_cvt_pk_bf16_f32 v87, v30, v31\n"
    "ds_write_b64 v7, v[86:87] offset:1536\n"
    "v_pk_fma_f32 v[32:33], v[64:65], s[98:99], v[32:33]\n"
    "v_pk_fma_f32 v[34:35], v[66:67], s[98:99], v[34:35]\n"
    "v_cvt_pk_bf16_f32 v88, v32, v33\n"
    "v_cvt_pk_bf16_f32 v89, v34, v35\n"
    "ds_write_b64 v7, v[88:89] offset:2048\n"
    "v_pk_fma_f32 v[36:37], v[68:69], s[98:99], v[36:37]\n"
    "v_pk_fma_f32 v[38:39], v[70:71], s[98:99], v[38:39]\n"
    "v_cvt_pk_bf16_f32 v90, v36, v37\n"
    "v_cvt_pk_bf16_f32 v91, v38, v39\n"
    "ds_write_b64 v7, v[90:91] offset:2560\n"
    "v_pk_fma_f32 v[40:41], v[72:73], s[98:99], v[40:41]\n"
    "v_pk_fma_f32 v[42:43], v[74:75], s[98:99], v[42:43]\n"
    "v_cvt_pk_bf16_f32 v92, v40, v41\n"
    "v_cvt_pk_bf16_f32 v93, v42, v43\n"
    "ds_write_b64 v7, v[92:93] offset:3072\n"
    "v_pk_fma_f32 v[44:45], v[76:77], s[98:99], v[44:45]\n"
    "v_pk_fma_f32 v[46:47], v[78:79], s[98:99], v[46:47]\n"
    "v_cvt_pk_bf16_f32 v94, v44, v45\n"
    "v_cvt_pk_bf16_f32 v95, v46, v47\n"
    "ds_write_b64 v7, v[94:95] offset:3584\n"
    "s_waitcnt vmcnt(14)\n"
    "v_lshlrev_b32 v112, 16, v176\n"
    "v_and_b32 v113, 0xffff0000, v176\n"
    "s_waitcnt vmcnt(10)\n"
    "v_pk_fma_f32 v[144:145], v[112:113], v[192:193], v[192:193] op_sel:[0,0,1] op_sel_hi:[1,0,1]\n"
    "v_lshlrev_b32 v114, 16, v177\n"
    "v_and_b32 v115, 0xffff0000, v177\n"
    "v_pk_fma_f32 v[146:147], v[114:115], v[192:193], v[192:193] op_sel:[0,0,1] op_sel_hi:[1,0,1]\n"
    "v_lshlrev_b32 v116, 16, v178\n"
    "v_and_b32 v117, 0xffff0000, v178\n"
    "v_pk_fma_f32 v[148:149], v[116:117], v[194:195], v[194:195] op_sel:[0,0,1] op_sel_hi:[1,0,1]\n"
    "v_lshlrev_b32 v118, 16, v179\n"
    "v_and_b32 v119, 0xffff0000, v179\n"
    "v_pk_fma_f32 v[150:151], v[118:119], v[194:195], v[194:195] op_sel:[0,0,1] op_sel_hi:[1,0,1]\n"
    "s_waitcnt lgkmcnt(0)\n"
    "s_barrier\n"
    "ds_read_b64_tr_b16 v[16:17], v231 offset:0\n"
    "ds_read_b64_tr_b16 v[18:19], v231 offset:64\n"
    "ds_read_b64_tr_b16 v[20:21], v231 offset:256\n"
    "ds_read_b64_tr_b16 v[22:23], v231 offset:320\n"
    "ds_read_b64_tr_b16 v[24:25], v231 offset:512\n"
    "ds_read_b64_tr_b16 v[26:27], v231 offset:576\n"
    "ds_read_b64_tr_b16 v[28:29], v231 offset:768\n"
    "ds_read_b64_tr_b16 v[30:31], v231 offset:832\n"
    "ds_read_b64_tr_b16 v[96:97], v8 offset:0\n"
    "ds_read_b64_tr_b16 v[98:99], v8 offset:32\n"
    "ds_read_b64_tr_b16 v[100:101], v8 offset:256\n"
    "ds_read_b64_tr_b16 v[102:103], v8 offset:288\n"
    "ds_read_b64_tr_b16 v[104:105], v8 offset:512\n"
    "ds_read_b64_tr_b16 v[106:107], v8 offset:544\n"
    "ds_read_b64_tr_b16 v[108:109], v8 offset:768\n"
    "ds_read_b64_tr_b16 v[110:111], v8 offset:800\n"
    "v_lshlrev_b32 v120, 16, v180\n"
    "v_and_b32 v121, 0xffff0000, v180\n"
    "s_waitcnt vmcnt(9)\n"
    "v_pk_fma_f32 v[152:153], v[120:121], v[196:197], v[196:197] op_sel:[0,0,1] op_sel_hi:[1,0,1]\n"
    "v_lshlrev_b32 v122, 16, v181\n"
    "v_and_b32 v123, 0xffff0000, v181\n"
    "v_pk_fma_f32 v[154:155], v[122:123], v[196:197], v[196:197] op_sel:[0,0,1] op_sel_hi:[1,0,1]\n"
    "v_lshlrev_b32 v124, 16, v182\n"
    "v_and_b32 v125, 0xffff0000, v182\n"
    "v_pk_fma_f32 v[156:157], v[124:125], v[198:199], v[198:199] op_sel:[0,0,1] op_sel_hi:[1,0,1]\n"
    "v_lshlrev_b32 v126, 16, v183\n"
    "v_and_b32 v127, 0xffff0000, v183\n"
    "v_pk_fma_f32 v[158:159], v[126:127], v[198:199], v[198:199] op_sel:[0,0,1] op_sel_hi:[1,0,1]\n"
    "v_lshlrev_b32 v128, 16, v184\n"
    "v_and_b32 v129, 0xffff0000, v184\n"
    "s_waitcnt vmcnt(8)\n"
    "v_pk_fma_f32 v[160:161], v[128:129], v[200:201], v[200:201] op_sel:[0,0,1] op_sel_hi:[1,0,1]\n"
    "v_lshlrev_b32 v130, 16, v185\n"
    "v_and_b32 v131, 0xffff0000, v185\n"
    "v_pk_fma_f32 v[162:163], v[130:131], v[200:201], v[200:201] op_sel:[0,0,1] op_sel_hi:[1,0,1]\n"
    "v_lshlrev_b32 v132, 16, v186\n"
    "v_and_b32 v133, 0xffff0000, v186\n"
    "v_pk_fma_f32 v[164:165], v[132:133], v[202:203], v[202:203] op_sel:[0,0,1] op_sel_hi:[1,0,1]\n"
    "v_lshlrev_b32 v134, 16, v187\n"
    "v_and_b32 v135, 0xffff0000, v187\n"
    "v_pk_fma_f32 v[166:167], v[134:135], v[202:203], v[202:203] op_sel:[0,0,1] op_sel_hi:[1,0,1]\n"
    "v_lshlrev_b32 v136, 16, v188\n"
    "v_and_b32 v137, 0xffff0000, v188\n"
    "s_waitcnt vmcnt(7)\n"
    "v_pk_fma_f32 v[168:169], v[136:137], v[204:205], v[204:205] op_sel:[0,0,1] op_sel_hi:[1,0,1]\n"
    "v_lshlrev_b32 v138, 16, v189\n"
    "v_and_b32 v139, 0xffff0000, v189\n"
    "v_pk_fma_f32 v[170:171], v[138:139], v[204:205], v[204:205] op_sel:[0,0,1] op_sel_hi:[1,0,1]\n"
    "v_lshlrev_b32 v140, 16, v190\n"
    "v_and_b32 v141, 0xffff0000, v190\n"
    "v_pk_fma_f32 v[172:173], v[140:141], v[206:207], v[206:207] op_sel:[0,0,1] op_sel_hi:[1,0,1]\n"
    "v_lshlrev_b32 v142, 16, v191\n"
    "v_and_b32 v143, 0xffff0000, v191\n"
    "v_pk_fma_f32 v[174:175], v[142:143], v[206:207], v[206:207] op_sel:[0,0,1] op_sel_hi:[1,0,1]\n"
    "s_lshr_b32 s84, s56, 3\n"
    "s_mul_i32 s85, s84, 52\n"
    "s_lshr_b32 s85, s85, 8\n"
    "s_mul_i32 s85, s85, 5\n"
    "s_sub_u32 s85, s84, s85\n"
    "s_lshl_b32 s84, s84, 11\n"
    "s_and_b32 s83, s56, 7\n"
    "s_and_b32 s86, s56, 1\n"
    "s_lshl_b32 s86, s86, 2\n"
    "s_lshr_b32 s87, s83, 1\n"
    "s_add_u32 s86, s86, s87\n"
    "s_cmp_eq_u32 s85, 2\n"
    "s_cselect_b32 s83, s86, s83\n"
    "s_lshl_b32 s83, s83, 8\n"
    "s_add_u32 s84, s84, s83\n"
    "s_lshl_b32 s85, s56, 8\n"
    "s_waitcnt lgkmcnt(15)\n"
    "v_lshlrev_b32 v48, 16, v16\n"
    "v_and_b32 v49, 0xffff0000, v16\n"
    "v_pk_fma_f32 v[48:49], v[48:49], v[144:145], v[112:113]\n"
    "v_lshlrev_b32 v50, 16, v17\n"
    "v_and_b32 v51, 0xffff0000, v17\n"
    "v_pk_fma_f32 v[50:51], v[50:51], v[146:147], v[114:115]\n"
    "s_waitcnt lgkmcnt(14)\n"
    "v_lshlrev_b32 v52, 16, v18\n"
    "v_and_b32 v53, 0xffff0000, v18\n"
    "v_pk_fma_f32 v[52:53], v[52:53], v[148:149], v[116:117]\n"
    "v_lshlrev_b32 v54, 16, v19\n"
    "v_and_b32 v55, 0xffff0000, v19\n"
    "v_pk_fma_f32 v[54:55], v[54:55], v[150:151], v[118:119]\n"
    "v_cvt_pk_bf16_f32 v32, v48, v49\n"
    "v_cvt_pk_bf16_f32 v33, v50, v51\n"
    "v_cvt_pk_bf16_f32 v34, v52, v53\n"
    "v_cvt_pk_bf16_f32 v35, v54, v55\n"
    "s_add_u32 s83, s84, 12144640\n"
    "buffer_store_dwordx4 v[32:35], v9, s[12:15], s83 offen sc1\n"
    "s_waitcnt lgkmcnt(13)\n"
    "v_lshlrev_b32 v56, 16, v20\n"
    "v_and_b32 v57, 0xffff0000, v20\n"
    "v_pk_fma_f32 v[56:57], v[56:57], v[152:153], v[120:121]\n"
    "v_lshlrev_b32 v58, 16, v21\n"
    "v_and_b32 v59, 0xffff0000, v21\n"
    "v_pk_fma_f32 v[58:59], v[58:59], v[154:155], v[122:123]\n"
    "s_waitcnt lgkmcnt(12)\n"
    "v_lshlrev_b32 v60, 16, v22\n"
    "v_and_b32 v61, 0xffff0000, v22\n"
    "v_pk_fma_f32 v[60:61], v[60:61], v[156:157], v[124:125]\n"
    "v_lshlrev_b32 v62, 16, v23\n"
    "v_and_b32 v63, 0xffff0000, v23\n"
    "v_pk_fma_f32 v[62:63], v[62:63], v[158:159], v[126:127]\n"
    "v_cvt_pk_bf16_f32 v36, v56, v57\n"
    "v_cvt_pk_bf16_f32 v37, v58, v59\n"
    "v_cvt_pk_bf16_f32 v38, v60, v61\n"
    "v_cvt_pk_bf16_f32 v39, v62, v63\n"
    "s_add_u32 s83, s84, 12308480\n"
    "buffer_store_dwordx4 v[36:39], v9, s[12:15], s83 offen sc1\n"
    "s_waitcnt lgkmcnt(11)\n"
    "v_lshlrev_b32 v64, 16, v24\n"
    "v_and_b32 v65, 0xffff0000, v24\n"
    "v_pk_fma_f32 v[64:65], v[64:65], v[160:161], v[128:129]\n"
    "v_lshlrev_b32 v66, 16, v25\n"
    "v_and_b32 v67, 0xffff0000, v25\n"
    "v_pk_fma_f32 v[66:67], v[66:67], v[162:163], v[130:131]\n"
    "s_waitcnt lgkmcnt(10)\n"
    "v_lshlrev_b32 v68, 16, v26\n"
    "v_and_b32 v69, 0xffff0000, v26\n"
    "v_pk_fma_f32 v[68:69], v[68:69], v[164:165], v[132:133]\n"
    "v_lshlrev_b32 v70, 16, v27\n"
    "v_and_b32 v71, 0xffff0000, v27\n"
    "v_pk_fma_f32 v[70:71], v[70:71], v[166:167], v[134:135]\n"
    "v_cvt_pk_bf16_f32 v40, v64, v65\n"
    "v_cvt_pk_bf16_f32 v41, v66, v67\n"
    "v_cvt_pk_bf16_f32 v42, v68, v69\n"
    "v_cvt_pk_bf16_f32 v43, v70, v71\n"
    "s_add_u32 s83, s84, 12472320\n"
    "buffer_store_dwordx4 v[40:43], v9, s[12:15], s83 offen sc1\n"
    "s_waitcnt lgkmcnt(9)\n"
    "v_lshlrev_b32 v72, 16, v28\n"
    "v_and_b32 v73, 0xffff0000, v28\n"
    "v_pk_fma_f32 v[72:73], v[72:73], v[168:169], v[136:137]\n"
    "v_lshlrev_b32 v74, 16, v29\n"
    "v_and_b32 v75, 0xffff0000, v29\n"
    "v_pk_fma_f32 v[74:75], v[74:75], v[170:171], v[138:139]\n"
    "s_waitcnt lgkmcnt(8)\n"
    "v_lshlrev_b32 v76, 16, v30\n"
    "v_and_b32 v77, 0xffff0000, v30\n"
    "v_pk_fma_f32 v[76:77], v[76:77], v[172:173], v[140:141]\n"
    "v_lshlrev_b32 v78, 16, v31\n"
    "v_and_b32 v79, 0xffff0000, v31\n"
    "v_pk_fma_f32 v[78:79], v[78:79], v[174:175], v[142:143]\n"
    "v_cvt_pk_bf16_f32 v44, v72, v73\n"
    "v_cvt_pk_bf16_f32 v45, v74, v75\n"
    "v_cvt_pk_bf16_f32 v46, v76, v77\n"
    "v_cvt_pk_bf16_f32 v47, v78, v79\n"
    "s_add_u32 s83, s84, 12636160\n"
    "buffer_store_dwordx4 v[44:47], v9, s[12:15], s83 offen sc1\n"
    "s_lshl_b32 s83, s9, 12\n"
    "s_lshl_b32 s84, s57, 8\n"
    "s_add_u32 s83, s83, s84\n"
    "s_add_u32 s83, s83, 86016\n"
    "v_mov_b32 v224, 1\n"
    "v_mov_b32 v225, 0\n"
    "s_waitcnt vmcnt(0)\n"
    "s_mov_b64 exec, 1\n"
    "buffer_atomic_add_x2 v[224:225], v1, s[12:15], s83 offen\n"
    "s_mov_b64 exec, -1\n"
    "s_add_u32 s84, s85, 14766080\n"
    "s_waitcnt lgkmcnt(6)\n"
    "buffer_store_dwordx4 v[96:99], v9, s[12:15], s84 offen sc1\n"
    "s_add_u32 s84, s85, 14929920\n"
    "s_waitcnt lgkmcnt(4)\n"
    "buffer_store_dwordx4 v[100:103], v9, s[12:15], s84 offen sc1\n"
    "s_add_u32 s84, s85, 15093760\n"
    "s_waitcnt lgkmcnt(2)\n"
    "buffer_store_dwordx4 v[104:107], v9, s[12:15], s84 offen sc1\n"
    "s_add_u32 s84, s85, 15257600\n"
    "s_waitcnt lgkmcnt(0)\n"
    "buffer_store_dwordx4 v[108:111], v9, s[12:15], s84 offen sc1\n"
    "s_mov_b32 s4, s83\n"
    "s_mov_b32 s28, s50\n"
    "s_and_b32 s29, s51, 0xffff\n"
    "s_mov_b32 s30, 2621440\n"
    "s_mov_b32 s31, 0x20000\n"
    "s_mul_i32 s86, s5, 143360\n"
    "s_mul_i32 s87, s9, 7168\n"
    "s_add_u32 s86, s86, s87\n"
    "s_add_u32 s86, s86, 7524352\n"
    "s_mov_b32 s32, 0x22222222\n"
    "s_mov_b32 s33, 0x22222222\n"
    "s_mov_b32 s34, 0x44444444\n"
    "s_mov_b32 s35, 0x44444444\n"
    "s_mul_i32 s87, s79, 7\n"
    "s_add_u32 s87, s87, s86\n"
    "s_add_u32 m0, s97, 0\n"
    "s_nop 0\n"
    "buffer_load_dwordx4 v1, s[12:15], s87 offen offset:0 sc1 lds\n"
    "s_mov_b32 s36, 0x88888888\n"
    "s_mov_b32 s37, 0x88888888\n"
    "s_mov_b32 s100, 0\n"
    "s_mov_b32 s101, 0xffff0000\n"
    "buffer_load_dwordx4 v1, s[12:15], s87 offen offset:1024 sc1 lds\n"
    "s_mov_b32 s38, 0x3f3504f3\n"
    "s_mov_b32 s39, 0x3acccccd\n"
    "s_mov_b32 s40, 0x3fb8aa3b\n"
    "s_mov_b32 s41, 0x3727c5ac\n"
    "buffer_load_dwordx4 v1, s[12:15], s87 offen offset:2048 sc1 lds\n"
    "s_mov_b32 s42, 0x3f317218\n"
    "s_mov_b32 s43, 0x3f317218\n"
    "s_mov_b32 s44, 0x3f3504f3\n"
    "s_mov_b32 s45, 0x3f3504f3\n"
    "buffer_load_dwordx4 v1, s[12:15], s87 offen offset:3072 sc1 lds\n"
    "s_add_u32 s87, s87, 4096\n"
    "s_add_u32 m0, s97, 4096\n"
    "s_mov_b32 s46, 0x3f000000\n"
    "s_mov_b32 s47, 0x3f000000\n"
    "s_mov_b32 s48, 12144640\n"
    "s_mov_b32 s49, 20008960\n"
    "buffer_load_dwordx4 v1, s[12:15], s87 offen offset:0 sc1 lds\n"
    "s_mov_b32 s52, 0\n"
    "s_lshl_b32 s83, s59, 3\n"
    "s_lshr_b32 s84, s9, 1\n"
    "s_lshl_b32 s84, s84, 2\n"
    "buffer_load_dwordx4 v1, s[12:15], s87 offen offset:1024 sc1 lds\n"
    "s_add_u32 s84, s84, s83\n"
    "s_and_b32 s85, s9, 1\n"
    "s_add_u32 s56, s84, s85\n"
    "s_mul_i32 s50, s56, 384\n"
    "buffer_load_dwordx4 v1, s[12:15], s87 offen offset:2048 sc1 lds\n"
    "s_mul_i32 s87, s80, 7\n"
    "s_add_u32 s87, s87, s86\n"
    "s_add_u32 m0, s97, 7168\n"
    "s_add_u32 s50, s50, 102400\n"
    "s_lshl_b32 s83, s54, 3\n"
    "s_lshr_b32 s84, s55, 3\n"
    "s_lshl_b32 s84, s84, 2\n"
    "buffer_load_dwordx4 v1, s[12:15], s87 offen offset:0 sc1 lds\n"
    "s_add_u32 s57, s84, s83\n"
    "s_mul_i32 s83, s57, 384\n"
    "s_add_u32 s58, s83, 102400\n"
    "s_lshl_b32 s22, s57, 10\n"
    "buffer_load_dwordx4 v1, s[12:15], s87 offen offset:1024 sc1 lds\n"
    "s_add_u32 s22, s22, 34689024\n"
    "s_mov_b32 s23, s22\n"
    "s_and_b32 s84, s55, 7\n"
    "s_lshl_b32 s84, s84, 7\n"
    "buffer_load_dwordx4 v1, s[12:15], s87 offen offset:2048 sc1 lds\n"
    "s_add_u32 s23, s23, s84\n"
    "s_mul_i32 s53, s57, 20480\n"
    "s_and_b32 s83, s55, 1\n"
    "s_min_u32 s84, s9, 2\n"
    "buffer_load_dwordx4 v1, s[12:15], s87 offen offset:3072 sc1 lds\n"
    "s_add_u32 s87, s87, 4096\n"
    "s_add_u32 m0, s97, 11264\n"
    "s_lshl_b32 s85, s84, 1\n"
    "s_add_u32 s19, s85, s83\n"
    "s_add_u32 s20, s19, 1\n"
    "s_cmp_ge_u32 s19, 5\n"
    "s_cselect_b32 s19, 0, s19\n"
    "buffer_load_dwordx4 v1, s[12:15], s87 offen offset:0 sc1 lds\n"
    "s_cmp_ge_u32 s9, 2\n"
    "s_cselect_b32 s20, s19, s20\n"
    "s_cselect_b32 s17, 0, 1.0\n"
    "s_cmp_eq_u32 s9, 3\n"
    "s_cselect_b32 s16, 0, 1.0\n"
    "buffer_load_dwordx4 v1, s[12:15], s87 offen offset:1024 sc1 lds\n"
    "s_and_b32 s85, s55, 7\n"
    "s_mul_i32 s85, s85, 5\n"
    "s_add_u32 s19, s19, s85\n"
    "s_add_u32 s20, s20, s85\n"
    "buffer_load_dwordx4 v1, s[12:15], s87 offen offset:2048 sc1 lds\n"
    "s_mul_i32 s87, s81, 7\n"
    "s_add_u32 s87, s87, s86\n"
    "s_add_u32 m0, s97, 14336\n"
    "s_lshr_b32 s18, s19, 1\n"
    "s_lshl_b32 s18, s18, 10\n"
    "s_and_b32 s83, s55, 1\n"
    "s_lshl_b32 s83, s83, 9\n"
    "buffer_load_dwordx4 v1, s[12:15], s87 offen offset:0 sc1 lds\n"
    "s_cmp_ge_u32 s9, 2\n"
    "s_cselect_b32 s21, s83, 0\n"
    "s_lshl_b32 s83, s5, 11\n"
    "s_add_u32 s83, s83, 9818112\n"
    "buffer_load_dwordx4 v1, s[12:15], s87 offen offset:1024 sc1 lds\n"
    "v_min_u32 v16, 55, v0\n"
    "v_lshlrev_b32 v24, 4, v16\n"
    "buffer_load_dwordx4 v[208:211], v24, s[12:15], s83 offen sc1\n"
    "buffer_load_dwordx4 v[212:215], v24, s[12:15], s83 offen offset:1024 sc1\n"
    "buffer_load_dwordx4 v1, s[12:15], s87 offen offset:2048 sc1 lds\n"
    "v_cmp_lt_u32 vcc, 27, v16\n"
    "v_subrev_u32 v24, 28, v16\n"
    "s_nop 0\n"
    "v_cndmask_b32 v16, v16, v24, vcc\n"
    "v_cndmask_b32 v24, 0, 16, vcc\n"
    "buffer_load_dwordx4 v1, s[12:15], s87 offen offset:3072 sc1 lds\n"
    "s_add_u32 s87, s87, 4096\n"
    "s_add_u32 m0, s97, 18432\n"
    "v_lshrrev_b32 v25, 2, v16\n"
    "v_mul_u32_u24 v25, 7168, v25\n"
    "v_and_b32 v16, 3, v16\n"
    "v_lshl_add_u32 v25, v16, 8, v25\n"
    "buffer_load_dwordx4 v1, s[12:15], s87 offen offset:0 sc1 lds\n"
    "v_add_u32 v25, v25, v24\n"
    "v_add_u32 v216, 6352, v25\n"
    "v_add_u32 v4, 35840, v1\n"
    "v_add_u32 v5, 71680, v1\n"
    "buffer_load_dwordx4 v1, s[12:15], s87 offen offset:1024 sc1 lds\n"
    "s_lshr_b32 s83, s56, 1\n"
    "s_and_b32 s84, s9, 1\n"
    "s_add_u32 s83, s83, s84\n"
    "s_mul_i32 s83, s83, 40960\n"
    "buffer_load_dwordx4 v1, s[12:15], s87 offen offset:2048 sc1 lds\n"
    "s_mul_i32 s87, s82, 7\n"
    "s_add_u32 s87, s87, s86\n"
    "s_add_u32 m0, s97, 21504\n"
    "s_add_u32 s83, s83, 12144640\n"
    "v_add_u32 v14, s83, v1\n"
    "v_add_u32 v15, 4096, v14\n"
    "v_add_u32 v6, 107520, v1\n"
    "buffer_load_dwordx4 v1, s[12:15], s87 offen offset:0 sc1 lds\n"
    "v_lshrrev_b32 v17, 4, v0\n"
    "v_and_b32 v18, 15, v0\n"
    "v_lshlrev_b32 v112, 8, v17\n"
    "v_add_u32 v112, 6352, v112\n"
    "buffer_load_dwordx4 v1, s[12:15], s87 offen offset:1024 sc1 lds\n"
    "v_lshrrev_b32 v19, 2, v18\n"
    "v_lshl_add_u32 v113, v19, 4, v18\n"
    "v_lshlrev_b32 v113, 2, v113\n"
    "v_lshlrev_b32 v114, 2, v18\n"
    "buffer_load_dwordx4 v1, s[12:15], s87 offen offset:2048 sc1 lds\n"
    "v_add_u32 v114, 192, v114\n"
    "s_lshl_b32 s84, s5, 1\n"
    "v_lshrrev_b32 v19, 1, v17\n"
    "v_add_u32 v19, s84, v19\n"
    "buffer_load_dwordx4 v1, s[12:15], s87 offen offset:3072 sc1 lds\n"
    "s_add_u32 s87, s87, 4096\n"
    "s_add_u32 m0, s97, 25600\n"
    "v_lshlrev_b32 v115, 8, v19\n"
    "v_lshl_add_u32 v115, v18, 4, v115\n"
    "v_add_u32 v115, 49152, v115\n"
    "v_and_b32 v17, 1, v17\n"
    "buffer_load_dwordx4 v1, s[12:15], s87 offen offset:0 sc1 lds\n"
    "v_mul_u32_u24 v17, 32767, v17\n"
    "v_lshl_add_u32 v115, v17, 16, v115\n"
    "v_mov_b32 v123, 1\n"
    "buffer_load_dwordx4 v1, s[12:15], s87 offen offset:1024 sc1 lds\n"
    "buffer_load_dwordx4 v1, s[12:15], s87 offen offset:2048 sc1 lds\n"
    "Ljoin_%=:\n"
    "v_min_u32 v12, 9, v0\n"
    "v_lshlrev_b32 v12, 8, v12\n"
    "s_lshr_b32 s83, s11, 1\n"
    "s_lshl_b32 s83, s83, 12\n"
    "s_add_u32 s83, s83, 86016\n"
    "v_add_u32 v12, s83, v12\n"
    "v_mov_b32 v116, v12\n"
    "s_cmp_lg_u32 s9, 0\n"
    "s_cbranch_scc1 Lh4skip_27_%=\n"
    "buffer_load_dwordx2 v[16:17], v12, s[12:15], 0 offen offset:0 sc1\n"
    "s_waitcnt vmcnt(0)\n"
    "v_cmp_ne_u64 vcc, s[62:63], v[16:17]\n"
    "s_cbranch_vccnz Lslow_28_%=\n"
    "Lback_29_%=:\n"
    "Lh4skip_27_%=:\n"
    "s_waitcnt vmcnt(0) lgkmcnt(0)\n"
    "s_barrier\n"
    "Lg1a_%=:\n"
    "s_lshl_b32 s93, s78, 1\n"
    "buffer_load_dwordx4 v[128:131], v14, s[12:15], s93 offen offset:0 sc1\n"
    "buffer_load_dwordx4 v[132:135], v14, s[12:15], s93 offen offset:1024 sc1\n"
    "buffer_load_dwordx4 v[136:139], v14, s[12:15], s93 offen offset:2048 sc1\n"
    "buffer_load_dwordx4 v[140:143], v14, s[12:15], s93 offen offset:3072 sc1\n"
    "buffer_load_dwordx4 v[144:147], v15, s[12:15], s93 offen offset:0 sc1\n"
    "buffer_load_dwordx4 v[148:151], v15, s[12:15], s93 offen offset:1024 sc1\n"
    "buffer_load_dwordx4 v[152:155], v15, s[12:15], s93 offen offset:2048 sc1\n"
    "buffer_load_dwordx4 v[156:159], v15, s[12:15], s93 offen offset:3072 sc1\n"
    "s_lshl_b32 s93, s79, 1\n"
    "buffer_load_dwordx4 v[160:163], v14, s[12:15], s93 offen offset:0 sc1\n"
    "buffer_load_dwordx4 v[164:167], v14, s[12:15], s93 offen offset:1024 sc1\n"
    "buffer_load_dwordx4 v[168:171], v14, s[12:15], s93 offen offset:2048 sc1\n"
    "buffer_load_dwordx4 v[172:175], v14, s[12:15], s93 offen offset:3072 sc1\n"
    "buffer_load_dwordx4 v[176:179], v15, s[12:15], s93 offen offset:0 sc1\n"
    "buffer_load_dwordx4 v[180:183], v15, s[12:15], s93 offen offset:1024 sc1\n"
    "buffer_load_dwordx4 v[184:187], v15, s[12:15], s93 offen offset:2048 sc1\n"
    "buffer_load_dwordx4 v[188:191], v15, s[12:15], s93 offen offset:3072 sc1\n"
    "v_add_f32 v208, v208, v212\n"
    "v_add_f32 v209, v209, v213\n"
    "v_add_f32 v210, v210, v214\n"
    "v_add_f32 v211, v211, v215\n"
    "ds_write_b128 v216, v[208:211]\n"
    "ds_read_b128 v[48:51], v1 offset:28672\n"
    "ds_read_b128 v[52:55], v1 offset:29696\n"
    "ds_read_b128 v[56:59], v1 offset:30720\n"
    "ds_read_b128 v[60:63], v1 offset:31744\n"
    "ds_read_b128 v[64:67], v1 offset:32768\n"
    "ds_read_b128 v[68:71], v1 offset:33792\n"
    "ds_read_b128 v[72:75], v1 offset:34816\n"
    "ds_read_b128 v[76:79], v4 offset:28672\n"
    "ds_read_b128 v[80:83], v4 offset:29696\n"
    "ds_read_b128 v[84:87], v4 offset:30720\n"
    "ds_read_b128 v[88:91], v4 offset:31744\n"
    "ds_read_b128 v[92:95], v4 offset:32768\n"
    "ds_read_b128 v[96:99], v4 offset:33792\n"
    "ds_read_b128 v[100:103], v4 offset:34816\n"
    "s_waitcnt vmcnt(15)\n"
    "v_accvgpr_write_b32 a200, v130\n"
    "v_accvgpr_write_b32 a201, v131\n"
    "s_waitcnt vmcnt(14)\n"
    "v_accvgpr_write_b32 a202, v134\n"
    "v_accvgpr_write_b32 a203, v135\n"
    "v_mov_b32 v130, v132\n"
    "v_mov_b32 v131, v133\n"
    "s_waitcnt vmcnt(12) lgkmcnt(10)\n"
    "s_nop 0\n"
    "v_mfma_f32_16x16x32_bf16 v[16:19], v[48:51], v[128:131], 0\n"
    "ds_read_b128 v[104:107], v5 offset:28672\n"
    "v_accvgpr_write_b32 a204, v138\n"
    "s_lshl_b32 s93, s80, 1\n"
    "v_mfma_f32_16x16x32_bf16 v[20:23], v[52:55], v[128:131], 0\n"
    "ds_read_b128 v[108:111], v5 offset:29696\n"
    "v_accvgpr_write_b32 a205, v139\n"
    "buffer_load_dwordx4 v[192:195], v14, s[12:15], s93 offen offset:0 sc1\n"
    "v_mfma_f32_16x16x32_bf16 v[24:27], v[56:59], v[128:131], 0\n"
    "ds_read_b128 v[48:51], v5 offset:30720\n"
    "v_accvgpr_write_b32 a206, v142\n"
    "v_mfma_f32_16x16x32_bf16 v[28:31], v[60:63], v[128:131], 0\n"
    "ds_read_b128 v[52:55], v5 offset:31744\n"
    "s_waitcnt lgkmcnt(11)\n"
    "v_mfma_f32_16x16x32_bf16 v[32:35], v[64:67], v[128:131], 0\n"
    "ds_read_b128 v[56:59], v5 offset:32768\n"
    "v_accvgpr_write_b32 a207, v143\n"
    "buffer_load_dwordx4 v[196:199], v14, s[12:15], s93 offen offset:1024 sc1\n"
    "v_mfma_f32_16x16x32_bf16 v[36:39], v[68:71], v[128:131], 0\n"
    "ds_read_b128 v[60:63], v5 offset:33792\n"
    "v_mov_b32 v138, v140\n"
    "v_mfma_f32_16x16x32_bf16 v[40:43], v[72:75], v[128:131], 0\n"
    "ds_read_b128 v[64:67], v5 offset:34816\n"
    "v_mov_b32 v139, v141\n"
    "v_mfma_f32_16x16x32_bf16 v[44:47], v[128:131], v[128:131], 0\n"
    "s_waitcnt vmcnt(12) lgkmcnt(10)\n"
    "buffer_load_dwordx4 v[200:203], v14, s[12:15], s93 offen offset:2048 sc1\n"
    "v_mfma_f32_16x16x32_bf16 v[16:19], v[76:79], v[136:139], v[16:19]\n"
    "ds_read_b128 v[68:71], v6 offset:28672\n"
    "v_accvgpr_write_b32 a208, v146\n"
    "v_mfma_f32_16x16x32_bf16 v[20:23], v[80:83], v[136:139], v[20:23]\n"
    "ds_read_b128 v[72:75], v6 offset:29696\n"
    "v_accvgpr_write_b32 a209, v147\n"
    "v_mfma_f32_16x16x32_bf16 v[24:27], v[84:87], v[136:139], v[24:27]\n"
    "ds_read_b128 v[76:79], v6 offset:30720\n"
    "v_accvgpr_write_b32 a210, v150\n"
    "buffer_load_dwordx4 v[204:207], v14, s[12:15], s93 offen offset:3072 sc1\n"
    "v_mfma_f32_16x16x32_bf16 v[28:31], v[88:91], v[136:139], v[28:31]\n"
    "ds_read_b128 v[80:83], v6 offset:31744\n"
    "s_waitcnt lgkmcnt(11)\n"
    "v_mfma_f32_16x16x32_bf16 v[32:35], v[92:95], v[136:139], v[32:35]\n"
    "ds_read_b128 v[84:87], v6 offset:32768\n"
    "v_accvgpr_write_b32 a211, v151\n"
    "v_mfma_f32_16x16x32_bf16 v[36:39], v[96:99], v[136:139], v[36:39]\n"
    "ds_read_b128 v[88:91], v6 offset:33792\n"
    "v_mov_b32 v146, v148\n"
    "buffer_load_dwordx4 v[128:131], v15, s[12:15], s93 offen offset:0 sc1\n"
    "v_mfma_f32_16x16x32_bf16 v[40:43], v[100:103], v[136:139], v[40:43]\n"
    "ds_read_b128 v[92:95], v6 offset:34816\n"
    "v_mov_b32 v147, v149\n"
    "v_mfma_f32_16x16x32_bf16 v[44:47], v[136:139], v[136:139], v[44:47]\n"
    "s_waitcnt vmcnt(13) lgkmcnt(10)\n"
    "v_mfma_f32_16x16x32_bf16 v[16:19], v[104:107], v[144:147], v[16:19]\n"
    "ds_read_b128 v[96:99], v1 offset:0\n"
    "v_accvgpr_write_b32 a212, v154\n"
    "buffer_load_dwordx4 v[132:135], v15, s[12:15], s93 offen offset:1024 sc1\n"
    "v_mfma_f32_16x16x32_bf16 v[20:23], v[108:111], v[144:147], v[20:23]\n"
    "ds_read_b128 v[100:103], v1 offset:1024\n"
    "v_accvgpr_write_b32 a213, v155\n"
    "v_mfma_f32_16x16x32_bf16 v[24:27], v[48:51], v[144:147], v[24:27]\n"
    "ds_read_b128 v[104:107], v1 offset:2048\n"
    "v_accvgpr_write_b32 a214, v158\n"
    "v_mfma_f32_16x16x32_bf16 v[28:31], v[52:55], v[144:147], v[28:31]\n"
    "ds_read_b128 v[108:111], v1 offset:3072\n"
    "s_waitcnt lgkmcnt(11)\n"
    "buffer_load_dwordx4 v[136:139], v15, s[12:15], s93 offen offset:2048 sc1\n"
    "v_mfma_f32_16x16x32_bf16 v[32:35], v[56:59], v[144:147], v[32:35]\n"
    "ds_read_b128 v[48:51], v1 offset:4096\n"
    "v_accvgpr_write_b32 a215, v159\n"
    "v_mfma_f32_16x16x32_bf16 v[36:39], v[60:63], v[144:147], v[36:39]\n"
    "ds_read_b128 v[52:55], v1 offset:5120\n"
    "v_mov_b32 v154, v156\n"
    "v_mfma_f32_16x16x32_bf16 v[40:43], v[64:67], v[144:147], v[40:43]\n"
    "ds_read_b128 v[56:59], v1 offset:6144\n"
    "v_mov_b32 v155, v157\n"
    "buffer_load_dwordx4 v[140:143], v15, s[12:15], s93 offen offset:3072 sc1\n"
    "v_mfma_f32_16x16x32_bf16 v[44:47], v[144:147], v[144:147], v[44:47]\n"
    "s_waitcnt vmcnt(14) lgkmcnt(10)\n"
    "s_lshl_b32 s93, s81, 1\n"
    "v_mfma_f32_16x16x32_bf16 v[16:19], v[68:71], v[152:155], v[16:19]\n"
    "ds_read_b128 v[60:63], v4 offset:0\n"
    "v_accvgpr_write_b32 a216, v162\n"
    "v_mfma_f32_16x16x32_bf16 v[20:23], v[72:75], v[152:155], v[20:23]\n"
    "ds_read_b128 v[64:67], v4 offset:1024\n"
    "v_accvgpr_write_b32 a217, v163\n"
    "buffer_load_dwordx4 v[144:147], v14, s[12:15], s93 offen offset:0 sc1\n"
    "v_mfma_f32_16x16x32_bf16 v[24:27], v[76:79], v[152:155], v[24:27]\n"
    "ds_read_b128 v[68:71], v4 offset:2048\n"
    "v_accvgpr_write_b32 a218, v166\n"
    "v_mfma_f32_16x16x32_bf16 v[28:31], v[80:83], v[152:155], v[28:31]\n"
    "ds_read_b128 v[72:75], v4 offset:3072\n"
    "s_waitcnt lgkmcnt(11)\n"
    "v_mfma_f32_16x16x32_bf16 v[32:35], v[84:87], v[152:155], v[32:35]\n"
    "ds_read_b128 v[76:79], v4 offset:4096\n"
    "v_accvgpr_write_b32 a219, v167\n"
    "buffer_load_dwordx4 v[148:151], v14, s[12:15], s93 offen offset:1024 sc1\n"
    "v_mfma_f32_16x16x32_bf16 v[36:39], v[88:91], v[152:155], v[36:39]\n"
    "ds_read_b128 v[80:83], v4 offset:5120\n"
    "v_mov_b32 v162, v164\n"
    "v_mfma_f32_16x16x32_bf16 v[40:43], v[92:95], v[152:155], v[40:43]\n"
    "ds_read_b128 v[84:87], v4 offset:6144\n"
    "v_mov_b32 v163, v165\n"
    "v_mfma_f32_16x16x32_bf16 v[44:47], v[152:155], v[152:155], v[44:47]\n"
    "s_waitcnt vmcnt(14) lgkmcnt(10)\n"
    "buffer_load_dwordx4 v[152:155], v14, s[12:15], s93 offen offset:2048 sc1\n"
    "v_mfma_f32_16x16x32_bf16 v[16:19], v[96:99], v[160:163], v[16:19]\n"
    "ds_read_b128 v[88:91], v5 offset:0\n"
    "v_accvgpr_write_b32 a220, v170\n"
    "v_mfma_f32_16x16x32_bf16 v[20:23], v[100:103], v[160:163], v[20:23]\n"
    "ds_read_b128 v[92:95], v5 offset:1024\n"
    "v_accvgpr_write_b32 a221, v171\n"
    "v_mfma_f32_16x16x32_bf16 v[24:27], v[104:107], v[160:163], v[24:27]\n"
    "ds_read_b128 v[96:99], v5 offset:2048\n"
    "v_accvgpr_write_b32 a222, v174\n"
    "buffer_load_dwordx4 v[156:159], v14, s[12:15], s93 offen offset:3072 sc1\n"
    "v_mfma_f32_16x16x32_bf16 v[28:31], v[108:111], v[160:163], v[28:31]\n"
    "ds_read_b128 v[100:103], v5 offset:3072\n"
    "s_waitcnt lgkmcnt(11)\n"
    "v_mfma_f32_16x16x32_bf16 v[32:35], v[48:51], v[160:163], v[32:35]\n"
    "ds_read_b128 v[104:107], v5 offset:4096\n"
    "v_accvgpr_write_b32 a223, v175\n"
    "v_mfma_f32_16x16x32_bf16 v[36:39], v[52:55], v[160:163], v[36:39]\n"
    "ds_read_b128 v[108:111], v5 offset:5120\n"
    "v_mov_b32 v170, v172\n"
    "v_mfma_f32_16x16x32_bf16 v[40:43], v[56:59], v[160:163], v[40:43]\n"
    "ds_read_b128 v[48:51], v5 offset:6144\n"
    "v_mov_b32 v171, v173\n"
    "v_mfma_f32_16x16x32_bf16 v[44:47], v[160:163], v[160:163], v[44:47]\n"
    "s_waitcnt vmcnt(14) lgkmcnt(10)\n"
    "buffer_load_dwordx4 v[160:163], v15, s[12:15], s93 offen offset:0 sc1\n"
    "v_mfma_f32_16x16x32_bf16 v[16:19], v[60:63], v[168:171], v[16:19]\n"
    "ds_read_b128 v[52:55], v6 offset:0\n"
    "v_accvgpr_write_b32 a224, v178\n"
    "v_mfma_f32_16x16x32_bf16 v[20:23], v[64:67], v[168:171], v[20:23]\n"
    "ds_read_b128 v[56:59], v6 offset:1024\n"
    "v_accvgpr_write_b32 a225, v179\n"
    "v_mfma_f32_16x16x32_bf16 v[24:27], v[68:71], v[168:171], v[24:27]\n"
    "ds_read_b128 v[60:63], v6 offset:2048\n"
    "v_accvgpr_write_b32 a226, v182\n"
    "buffer_load_dwordx4 v[164:167], v15, s[12:15], s93 offen offset:1024 sc1\n"
    "v_mfma_f32_16x16x32_bf16 v[28:31], v[72:75], v[168:171], v[28:31]\n"
    "ds_read_b128 v[64:67], v6 offset:3072\n"
    "s_waitcnt lgkmcnt(11)\n"
    "v_mfma_f32_16x16x32_bf16 v[32:35], v[76:79], v[168:171], v[32:35]\n"
    "ds_read_b128 v[68:71], v6 offset:4096\n"
    "v_accvgpr_write_b32 a227, v183\n"
    "v_mfma_f32_16x16x32_bf16 v[36:39], v[80:83], v[168:171], v[36:39]\n"
    "ds_read_b128 v[72:75], v6 offset:5120\n"
    "v_mov_b32 v178, v180\n"
    "v_mfma_f32_16x16x32_bf16 v[40:43], v[84:87], v[168:171], v[40:43]\n"
    "ds_read_b128 v[76:79], v6 offset:6144\n"
    "v_mov_b32 v179, v181\n"
    "v_mfma_f32_16x16x32_bf16 v[44:47], v[168:171], v[168:171], v[44:47]\n"
    "s_waitcnt vmcnt(14) lgkmcnt(10)\n"
    "buffer_load_dwordx4 v[168:171], v15, s[12:15], s93 offen offset:2048 sc1\n"
    "v_mfma_f32_16x16x32_bf16 v[16:19], v[88:91], v[176:179], v[16:19]\n"
    "ds_read_b128 v[80:83], v1 offset:7168\n"
    "v_accvgpr_write_b32 a228, v186\n"
    "v_mfma_f32_16x16x32_bf16 v[20:23], v[92:95], v[176:179], v[20:23]\n"
    "ds_read_b128 v[84:87], v1 offset:8192\n"
    "v_accvgpr_write_b32 a229, v187\n"
    "v_mfma_f32_16x16x32_bf16 v[24:27], v[96:99], v[176:179], v[24:27]\n"
    "ds_read_b128 v[88:91], v1 offset:9216\n"
    "v_accvgpr_write_b32 a230, v190\n"
    "buffer_load_dwordx4 v[172:175], v15, s[12:15], s93 offen offset:3072 sc1\n"
    "v_mfma_f32_16x16x32_bf16 v[28:31], v[100:103], v[176:179], v[28:31]\n"
    "ds_read_b128 v[92:95], v1 offset:10240\n"
    "s_waitcnt lgkmcnt(11)\n"
    "v_mfma_f32_16x16x32_bf16 v[32:35], v[104:107], v[176:179], v[32:35]\n"
    "ds_read_b128 v[96:99], v1 offset:11264\n"
    "v_accvgpr_write_b32 a231, v191\n"
    "v_mfma_f32_16x16x32_bf16 v[36:39], v[108:111], v[176:179], v[36:39]\n"
    "ds_read_b128 v[100:103], v1 offset:12288\n"
    "v_mov_b32 v186, v188\n"
    "v_mfma_f32_16x16x32_bf16 v[40:43], v[48:51], v[176:179], v[40:43]\n"
    "ds_read_b128 v[104:107], v1 offset:13312\n"
    "v_mov_b32 v187, v189\n"
    "v_mfma_f32_16x16x32_bf16 v[44:47], v[176:179], v[176:179], v[44:47]\n"
    "s_waitcnt vmcnt(14) lgkmcnt(10)\n"
    "s_lshl_b32 s93, s82, 1\n"
    "buffer_load_dwordx4 v[176:179], v14, s[12:15], s93 offen offset:0 sc1\n"
    "v_mfma_f32_16x16x32_bf16 v[16:19], v[52:55], v[184:187], v[16:19]\n"
    "ds_read_b128 v[108:111], v4 offset:7168\n"
    "v_accvgpr_write_b32 a232, v194\n"
    "v_mfma_f32_16x16x32_bf16 v[20:23], v[56:59], v[184:187], v[20:23]\n"
    "ds_read_b128 v[48:51], v4 offset:8192\n"
    "v_accvgpr_write_b32 a233, v195\n"
    "v_mfma_f32_16x16x32_bf16 v[24:27], v[60:63], v[184:187], v[24:27]\n"
    "ds_read_b128 v[52:55], v4 offset:9216\n"
    "v_accvgpr_write_b32 a234, v198\n"
    "buffer_load_dwordx4 v[180:183], v14, s[12:15], s93 offen offset:1024 sc1\n"
    "v_mfma_f32_16x16x32_bf16 v[28:31], v[64:67], v[184:187], v[28:31]\n"
    "ds_read_b128 v[56:59], v4 offset:10240\n"
    "s_waitcnt lgkmcnt(11)\n"
    "v_mfma_f32_16x16x32_bf16 v[32:35], v[68:71], v[184:187], v[32:35]\n"
    "ds_read_b128 v[60:63], v4 offset:11264\n"
    "v_accvgpr_write_b32 a235, v199\n"
    "v_mfma_f32_16x16x32_bf16 v[36:39], v[72:75], v[184:187], v[36:39]\n"
    "ds_read_b128 v[64:67], v4 offset:12288\n"
    "v_mov_b32 v194, v196\n"
    "v_mfma_f32_16x16x32_bf16 v[40:43], v[76:79], v[184:187], v[40:43]\n"
    "ds_read_b128 v[68:71], v4 offset:13312\n"
    "v_mov_b32 v195, v197\n"
    "v_mfma_f32_16x16x32_bf16 v[44:47], v[184:187], v[184:187], v[44:47]\n"
    "s_waitcnt vmcnt(14) lgkmcnt(10)\n"
    "buffer_load_dwordx4 v[184:187], v14, s[12:15], s93 offen offset:2048 sc1\n"
    "v_mfma_f32_16x16x32_bf16 v[16:19], v[80:83], v[192:195], v[16:19]\n"
    "ds_read_b128 v[72:75], v5 offset:7168\n"
    "v_accvgpr_write_b32 a236, v202\n"
    "v_mfma_f32_16x16x32_bf16 v[20:23], v[84:87], v[192:195], v[20:23]\n"
    "ds_read_b128 v[76:79], v5 offset:8192\n"
    "v_accvgpr_write_b32 a237, v203\n"
    "v_mfma_f32_16x16x32_bf16 v[24:27], v[88:91], v[192:195], v[24:27]\n"
    "ds_read_b128 v[80:83], v5 offset:9216\n"
    "v_accvgpr_write_b32 a238, v206\n"
    "buffer_load_dwordx4 v[188:191], v14, s[12:15], s93 offen offset:3072 sc1\n"
    "v_mfma_f32_16x16x32_bf16 v[28:31], v[92:95], v[192:195], v[28:31]\n"
    "ds_read_b128 v[84:87], v5 offset:10240\n"
    "s_waitcnt lgkmcnt(11)\n"
    "v_mfma_f32_16x16x32_bf16 v[32:35], v[96:99], v[192:195], v[32:35]\n"
    "ds_read_b128 v[88:91], v5 offset:11264\n"
    "v_accvgpr_write_b32 a239, v207\n"
    "v_mfma_f32_16x16x32_bf16 v[36:39], v[100:103], v[192:195], v[36:39]\n"
    "ds_read_b128 v[92:95], v5 offset:12288\n"
    "v_mov_b32 v202, v204\n"
    "v_mfma_f32_16x16x32_bf16 v[40:43], v[104:107], v[192:195], v[40:43]\n"
    "ds_read_b128 v[96:99], v5 offset:13312\n"
    "v_mov_b32 v203, v205\n"
    "v_mfma_f32_16x16x32_bf16 v[44:47], v[192:195], v[192:195], v[44:47]\n"
    "s_waitcnt vmcnt(14) lgkmcnt(10)\n"
    "buffer_load_dwordx4 v[192:195], v15, s[12:15], s93 offen offset:0 sc1\n"
    "v_mfma_f32_16x16x32_bf16 v[16:19], v[108:111], v[200:203], v[16:19]\n"
    "ds_read_b128 v[100:103], v6 offset:7168\n"
    "v_accvgpr_write_b32 a240, v130\n"
    "v_mfma_f32_16x16x32_bf16 v[20:23], v[48:51], v[200:203], v[20:23]\n"
    "ds_read_b128 v[104:107], v6 offset:8192\n"
    "v_accvgpr_write_b32 a241, v131\n"
    "v_mfma_f32_16x16x32_bf16 v[24:27], v[52:55], v[200:203], v[24:27]\n"
    "ds_read_b128 v[108:111], v6 offset:9216\n"
    "v_accvgpr_write_b32 a242, v134\n"
    "buffer_load_dwordx4 v[196:199], v15, s[12:15], s93 offen offset:1024 sc1\n"
    "v_mfma_f32_16x16x32_bf16 v[28:31], v[56:59], v[200:203], v[28:31]\n"
    "ds_read_b128 v[48:51], v6 offset:10240\n"
    "s_waitcnt lgkmcnt(11)\n"
    "v_mfma_f32_16x16x32_bf16 v[32:35], v[60:63], v[200:203], v[32:35]\n"
    "ds_read_b128 v[52:55], v6 offset:11264\n"
    "v_accvgpr_write_b32 a243, v135\n"
    "v_mfma_f32_16x16x32_bf16 v[36:39], v[64:67], v[200:203], v[36:39]\n"
    "ds_read_b128 v[56:59], v6 offset:12288\n"
    "v_mov_b32 v130, v132\n"
    "v_mfma_f32_16x16x32_bf16 v[40:43], v[68:71], v[200:203], v[40:43]\n"
    "ds_read_b128 v[60:63], v6 offset:13312\n"
    "v_mov_b32 v131, v133\n"
    "v_mfma_f32_16x16x32_bf16 v[44:47], v[200:203], v[200:203], v[44:47]\n"
    "s_waitcnt vmcnt(14) lgkmcnt(10)\n"
    "buffer_load_dwordx4 v[200:203], v15, s[12:15], s93 offen offset:2048 sc1\n"
    "v_mfma_f32_16x16x32_bf16 v[16:19], v[72:75], v[128:131], v[16:19]\n"
    "ds_read_b128 v[64:67], v1 offset:14336\n"
    "v_accvgpr_write_b32 a244, v138\n"
    "v_mfma_f32_16x16x32_bf16 v[20:23], v[76:79], v[128:131], v[20:23]\n"
    "ds_read_b128 v[68:71], v1 offset:15360\n"
    "v_accvgpr_write_b32 a245, v139\n"
    "v_mfma_f32_16x16x32_bf16 v[24:27], v[80:83], v[128:131], v[24:27]\n"
    "ds_read_b128 v[72:75], v1 offset:16384\n"
    "v_accvgpr_write_b32 a246, v142\n"
    "buffer_load_dwordx4 v[204:207], v15, s[12:15], s93 offen offset:3072 sc1\n"
    "v_mfma_f32_16x16x32_bf16 v[28:31], v[84:87], v[128:131], v[28:31]\n"
    "ds_read_b128 v[76:79], v1 offset:17408\n"
    "s_waitcnt lgkmcnt(11)\n"
    "v_mfma_f32_16x16x32_bf16 v[32:35], v[88:91], v[128:131], v[32:35]\n"
    "ds_read_b128 v[80:83], v1 offset:18432\n"
    "v_accvgpr_write_b32 a247, v143\n"
    "v_mfma_f32_16x16x32_bf16 v[36:39], v[92:95], v[128:131], v[36:39]\n"
    "ds_read_b128 v[84:87], v1 offset:19456\n"
    "v_mov_b32 v138, v140\n"
    "v_mfma_f32_16x16x32_bf16 v[40:43], v[96:99], v[128:131], v[40:43]\n"
    "ds_read_b128 v[88:91], v1 offset:20480\n"
    "v_mov_b32 v139, v141\n"
    "v_mfma_f32_16x16x32_bf16 v[44:47], v[128:131], v[128:131], v[44:47]\n"
    "s_waitcnt vmcnt(14) lgkmcnt(10)\n"
    "v_mfma_f32_16x16x32_bf16 v[16:19], v[100:103], v[136:139], v[16:19]\n"
    "ds_read_b128 v[92:95], v4 offset:14336\n"
    "v_accvgpr_write_b32 a248, v146\n"
    "v_mfma_f32_16x16x32_bf16 v[20:23], v[104:107], v[136:139], v[20:23]\n"
    "ds_read_b128 v[96:99], v4 offset:15360\n"
    "v_accvgpr_write_b32 a249, v147\n"
    "v_mfma_f32_16x16x32_bf16 v[24:27], v[108:111], v[136:139], v[24:27]\n"
    "ds_read_b128 v[100:103], v4 offset:16384\n"
    "v_accvgpr_write_b32 a250, v150\n"
    "v_mfma_f32_16x16x32_bf16 v[28:31], v[48:51], v[136:139], v[28:31]\n"
    "ds_read_b128 v[104:107], v4 offset:17408\n"
    "s_waitcnt lgkmcnt(11)\n"
    "v_mfma_f32_16x16x32_bf16 v[32:35], v[52:55], v[136:139], v[32:35]\n"
    "ds_read_b128 v[108:111], v4 offset:18432\n"
    "v_accvgpr_write_b32 a251, v151\n"
    "v_mfma_f32_16x16x32_bf16 v[36:39], v[56:59], v[136:139], v[36:39]\n"
    "ds_read_b128 v[48:51], v4 offset:19456\n"
    "v_mov_b32 v146, v148\n"
    "v_mfma_f32_16x16x32_bf16 v[40:43], v[60:63], v[136:139], v[40:43]\n"
    "ds_read_b128 v[52:55], v4 offset:20480\n"
    "v_mov_b32 v147, v149\n"
    "v_mfma_f32_16x16x32_bf16 v[44:47], v[136:139], v[136:139], v[44:47]\n"
    "s_waitcnt vmcnt(12) lgkmcnt(10)\n"
    "v_mfma_f32_16x16x32_bf16 v[16:19], v[64:67], v[144:147], v[16:19]\n"
    "ds_read_b128 v[56:59], v5 offset:14336\n"
    "v_accvgpr_write_b32 a252, v154\n"
    "v_mfma_f32_16x16x32_bf16 v[20:23], v[68:71], v[144:147], v[20:23]\n"
    "ds_read_b128 v[60:63], v5 offset:15360\n"
    "v_accvgpr_write_b32 a253, v155\n"
    "v_mfma_f32_16x16x32_bf16 v[24:27], v[72:75], v[144:147], v[24:27]\n"
    "ds_read_b128 v[64:67], v5 offset:16384\n"
    "v_accvgpr_write_b32 a254, v158\n"
    "v_mfma_f32_16x16x32_bf16 v[28:31], v[76:79], v[144:147], v[28:31]\n"
    "ds_read_b128 v[68:71], v5 offset:17408\n"
    "s_waitcnt lgkmcnt(11)\n"
    "v_mov_b32 v224, 1\n"
    "v_mfma_f32_16x16x32_bf16 v[32:35], v[80:83], v[144:147], v[32:35]\n"
    "ds_read_b128 v[72:75], v5 offset:18432\n"
    "v_accvgpr_write_b32 a255, v159\n"
    "v_mov_b32 v225, 0\n"
    "v_mfma_f32_16x16x32_bf16 v[36:39], v[84:87], v[144:147], v[36:39]\n"
    "ds_read_b128 v[76:79], v5 offset:19456\n"
    "v_mov_b32 v154, v156\n"
    "s_cmp_ge_u32 s10, 20\n"
    "s_cbranch_scc1 Lnoy_32_%=\n"
    "s_mov_b64 exec, 1\n"
    "buffer_atomic_add_x2 v[224:225], v1, s[12:15], s4 offen offset:128\n"
    "s_mov_b64 exec, -1\n"
    "Lnoy_32_%=:\n"
    "v_mfma_f32_16x16x32_bf16 v[40:43], v[88:91], v[144:147], v[40:43]\n"
    "ds_read_b128 v[80:83], v5 offset:20480\n"
    "v_mov_b32 v155, v157\n"
    "v_mfma_f32_16x16x32_bf16 v[44:47], v[144:147], v[144:147], v[44:47]\n"
    "s_waitcnt vmcnt(10) lgkmcnt(10)\n"
    "v_mfma_f32_16x16x32_bf16 v[16:19], v[92:95], v[152:155], v[16:19]\n"
    "ds_read_b128 v[84:87], v6 offset:14336\n"
    "v_mov_b32 v232, v162\n"
    "v_mfma_f32_16x16x32_bf16 v[20:23], v[96:99], v[152:155], v[20:23]\n"
    "ds_read_b128 v[88:91], v6 offset:15360\n"
    "v_mov_b32 v233, v163\n"
    "v_mfma_f32_16x16x32_bf16 v[24:27], v[100:103], v[152:155], v[24:27]\n"
    "ds_read_b128 v[92:95], v6 offset:16384\n"
    "v_mov_b32 v234, v166\n"
    "v_mfma_f32_16x16x32_bf16 v[28:31], v[104:107], v[152:155], v[28:31]\n"
    "ds_read_b128 v[96:99], v6 offset:17408\n"
    "s_waitcnt lgkmcnt(11)\n"
    "v_mfma_f32_16x16x32_bf16 v[32:35], v[108:111], v[152:155], v[32:35]\n"
    "ds_read_b128 v[100:103], v6 offset:18432\n"
    "v_mov_b32 v235, v167\n"
    "v_mfma_f32_16x16x32_bf16 v[36:39], v[48:51], v[152:155], v[36:39]\n"
    "ds_read_b128 v[104:107], v6 offset:19456\n"
    "v_mov_b32 v162, v164\n"
    "v_mfma_f32_16x16x32_bf16 v[40:43], v[52:55], v[152:155], v[40:43]\n"
    "ds_read_b128 v[108:111], v6 offset:20480\n"
    "v_mov_b32 v163, v165\n"
    "v_mfma_f32_16x16x32_bf16 v[44:47], v[152:155], v[152:155], v[44:47]\n"
    "s_waitcnt vmcnt(8) lgkmcnt(10)\n"
    "v_mfma_f32_16x16x32_bf16 v[16:19], v[56:59], v[160:163], v[16:19]\n"
    "ds_read_b128 v[48:51], v1 offset:21504\n"
    "v_mov_b32 v236, v170\n"
    "v_mfma_f32_16x16x32_bf16 v[20:23], v[60:63], v[160:163], v[20:23]\n"
    "ds_read_b128 v[52:55], v1 offset:22528\n"
    "v_mov_b32 v237, v171\n"
    "v_mfma_f32_16x16x32_bf16 v[24:27], v[64:67], v[160:163], v[24:27]\n"
    "ds_read_b128 v[56:59], v1 offset:23552\n"
    "v_mov_b32 v238, v174\n"
    "v_mfma_f32_16x16x32_bf16 v[28:31], v[68:71], v[160:163], v[28:31]\n"
    "ds_read_b128 v[60:63], v1 offset:24576\n"
    "s_waitcnt lgkmcnt(11)\n"
    "v_mfma_f32_16x16x32_bf16 v[32:35], v[72:75], v[160:163], v[32:35]\n"
    "ds_read_b128 v[64:67], v1 offset:25600\n"
    "v_mov_b32 v239, v175\n"
    "v_mfma_f32_16x16x32_bf16 v[36:39], v[76:79], v[160:163], v[36:39]\n"
    "ds_read_b128 v[68:71], v1 offset:26624\n"
    "v_mov_b32 v170, v172\n"
    "v_mfma_f32_16x16x32_bf16 v[40:43], v[80:83], v[160:163], v[40:43]\n"
    "ds_read_b128 v[72:75], v1 offset:27648\n"
    "v_mov_b32 v171, v173\n"
    "v_mfma_f32_16x16x32_bf16 v[44:47], v[160:163], v[160:163], v[44:47]\n"
    "s_waitcnt vmcnt(6) lgkmcnt(10)\n"
    "v_mfma_f32_16x16x32_bf16 v[16:19], v[84:87], v[168:171], v[16:19]\n"
    "ds_read_b128 v[76:79], v4 offset:21504\n"
    "v_mov_b32 v240, v178\n"
    "v_mfma_f32_16x16x32_bf16 v[20:23], v[88:91], v[168:171], v[20:23]\n"
    "ds_read_b128 v[80:83], v4 offset:22528\n"
    "v_mov_b32 v241, v179\n"
    "v_mfma_f32_16x16x32_bf16 v[24:27], v[92:95], v[168:171], v[24:27]\n"
    "ds_read_b128 v[84:87], v4 offset:23552\n"
    "v_mov_b32 v242, v182\n"
    "v_mfma_f32_16x16x32_bf16 v[28:31], v[96:99], v[168:171], v[28:31]\n"
    "ds_read_b128 v[88:91], v4 offset:24576\n"
    "s_waitcnt lgkmcnt(11)\n"
    "v_mfma_f32_16x16x32_bf16 v[32:35], v[100:103], v[168:171], v[32:35]\n"
    "ds_read_b128 v[92:95], v4 offset:25600\n"
    "v_mov_b32 v243, v183\n"
    "v_mfma_f32_16x16x32_bf16 v[36:39], v[104:107], v[168:171], v[36:39]\n"
    "ds_read_b128 v[96:99], v4 offset:26624\n"
    "v_mov_b32 v178, v180\n"
    "v_mfma_f32_16x16x32_bf16 v[40:43], v[108:111], v[168:171], v[40:43]\n"
    "ds_read_b128 v[100:103], v4 offset:27648\n"
    "v_mov_b32 v179, v181\n"
    "v_mfma_f32_16x16x32_bf16 v[44:47], v[168:171], v[168:171], v[44:47]\n"
    "s_waitcnt vmcnt(4) lgkmcnt(10)\n"
    "v_mfma_f32_16x16x32_bf16 v[16:19], v[48:51], v[176:179], v[16:19]\n"
    "ds_read_b128 v[104:107], v5 offset:21504\n"
    "v_mov_b32 v244, v186\n"
    "v_mfma_f32_16x16x32_bf16 v[20:23], v[52:55], v[176:179], v[20:23]\n"
    "ds_read_b128 v[108:111], v5 offset:22528\n"
    "v_mov_b32 v245, v187\n"
    "v_mfma_f32_16x16x32_bf16 v[24:27], v[56:59], v[176:179], v[24:27]\n"
    "ds_read_b128 v[48:51], v5 offset:23552\n"
    "v_mov_b32 v246, v190\n"
    "v_mfma_f32_16x16x32_bf16 v[28:31], v[60:63], v[176:179], v[28:31]\n"
    "ds_read_b128 v[52:55], v5 offset:24576\n"
    "s_waitcnt lgkmcnt(11)\n"
    "v_mfma_f32_16x16x32_bf16 v[32:35], v[64:67], v[176:179], v[32:35]\n"
    "ds_read_b128 v[56:59], v5 offset:25600\n"
    "v_mov_b32 v247, v191\n"
    "v_mfma_f32_16x16x32_bf16 v[36:39], v[68:71], v[176:179], v[36:39]\n"
    "ds_read_b128 v[60:63], v5 offset:26624\n"
    "v_mov_b32 v186, v188\n"
    "v_mfma_f32_16x16x32_bf16 v[40:43], v[72:75], v[176:179], v[40:43]\n"
    "ds_read_b128 v[64:67], v5 offset:27648\n"
    "v_mov_b32 v187, v189\n"
    "v_mfma_f32_16x16x32_bf16 v[44:47], v[176:179], v[176:179], v[44:47]\n"
    "s_waitcnt vmcnt(2) lgkmcnt(10)\n"
    "v_mfma_f32_16x16x32_bf16 v[16:19], v[76:79], v[184:187], v[16:19]\n"
    "ds_read_b128 v[68:71], v6 offset:21504\n"
    "v_mov_b32 v248, v194\n"
    "v_mfma_f32_16x16x32_bf16 v[20:23], v[80:83], v[184:187], v[20:23]\n"
    "ds_read_b128 v[72:75], v6 offset:22528\n"
    "v_mov_b32 v249, v195\n"
    "v_mfma_f32_16x16x32_bf16 v[24:27], v[84:87], v[184:187], v[24:27]\n"
    "ds_read_b128 v[76:79], v6 offset:23552\n"
    "v_mov_b32 v250, v198\n"
    "v_mfma_f32_16x16x32_bf16 v[28:31], v[88:91], v[184:187], v[28:31]\n"
    "ds_read_b128 v[80:83], v6 offset:24576\n"
    "s_waitcnt lgkmcnt(11)\n"
    "v_mfma_f32_16x16x32_bf16 v[32:35], v[92:95], v[184:187], v[32:35]\n"
    "ds_read_b128 v[84:87], v6 offset:25600\n"
    "v_mov_b32 v251, v199\n"
    "v_mfma_f32_16x16x32_bf16 v[36:39], v[96:99], v[184:187], v[36:39]\n"
    "ds_read_b128 v[88:91], v6 offset:26624\n"
    "v_mov_b32 v194, v196\n"
    "v_mfma_f32_16x16x32_bf16 v[40:43], v[100:103], v[184:187], v[40:43]\n"
    "ds_read_b128 v[92:95], v6 offset:27648\n"
    "v_mov_b32 v195, v197\n"
    "v_mfma_f32_16x16x32_bf16 v[44:47], v[184:187], v[184:187], v[44:47]\n"
    "s_waitcnt vmcnt(0) lgkmcnt(10)\n"
    "v_mfma_f32_16x16x32_bf16 v[16:19], v[104:107], v[192:195], v[16:19]\n"
    "ds_read_b128 v[96:99], v1 offset:28672\n"
    "v_mov_b32 v252, v202\n"
    "v_mfma_f32_16x16x32_bf16 v[20:23], v[108:111], v[192:195], v[20:23]\n"
    "ds_read_b128 v[100:103], v1 offset:29696\n"
    "v_mov_b32 v253, v203\n"
    "v_mfma_f32_16x16x32_bf16 v[24:27], v[48:51], v[192:195], v[24:27]\n"
    "ds_read_b128 v[104:107], v1 offset:30720\n"
    "v_mov_b32 v254, v206\n"
    "v_mfma_f32_16x16x32_bf16 v[28:31], v[52:55], v[192:195], v[28:31]\n"
    "ds_read_b128 v[108:111], v1 offset:31744\n"
    "s_waitcnt lgkmcnt(11)\n"
    "v_mfma_f32_16x16x32_bf16 v[32:35], v[56:59], v[192:195], v[32:35]\n"
    "ds_read_b128 v[48:51], v1 offset:32768\n"
    "v_mov_b32 v255, v207\n"
    "v_mfma_f32_16x16x32_bf16 v[36:39], v[60:63], v[192:195], v[36:39]\n"
    "ds_read_b128 v[52:55], v1 offset:33792\n"
    "v_mov_b32 v202, v204\n"
    "v_mfma_f32_16x16x32_bf16 v[40:43], v[64:67], v[192:195], v[40:43]\n"
    "ds_read_b128 v[56:59], v1 offset:34816\n"
    "v_mov_b32 v203, v205\n"
    "v_mfma_f32_16x16x32_bf16 v[44:47], v[192:195], v[192:195], v[44:47]\n"
    "s_waitcnt lgkmcnt(10)\n"
    "v_mfma_f32_16x16x32_bf16 v[16:19], v[68:71], v[200:203], v[16:19]\n"
    "ds_read_b128 v[60:63], v4 offset:28672\n"
    "v_mfma_f32_16x16x32_bf16 v[20:23], v[72:75], v[200:203], v[20:23]\n"
    "ds_read_b128 v[64:67], v4 offset:29696\n"
    "v_mfma_f32_16x16x32_bf16 v[24:27], v[76:79], v[200:203], v[24:27]\n"
    "ds_read_b128 v[68:71], v4 offset:30720\n"
    "v_mfma_f32_16x16x32_bf16 v[28:31], v[80:83], v[200:203], v[28:31]\n"
    "ds_read_b128 v[72:75], v4 offset:31744\n"
    "s_waitcnt lgkmcnt(11)\n"
    "v_mfma_f32_16x16x32_bf16 v[32:35], v[84:87], v[200:203], v[32:35]\n"
    "ds_read_b128 v[76:79], v4 offset:32768\n"
    "v_mfma_f32_16x16x32_bf16 v[36:39], v[88:91], v[200:203], v[36:39]\n"
    "ds_read_b128 v[80:83], v4 offset:33792\n"
    "v_mfma_f32_16x16x32_bf16 v[40:43], v[92:95], v[200:203], v[40:43]\n"
    "ds_read_b128 v[84:87], v4 offset:34816\n"
    "v_mfma_f32_16x16x32_bf16 v[44:47], v[200:203], v[200:203], v[44:47]\n"
    "ds_read_b128 v[208:211], v112 offset:0\n"
    "ds_read_b128 v[212:215], v112 offset:16\n"
    "s_add_u32 s90, s56, 0\n"
    "s_mul_i32 s90, s90, 57344\n"
    "s_and_b32 s88, s55, 7\n"
    "s_add_u32 s90, s90, s49\n"
    "s_mul_i32 s91, s5, 3072\n"
    "s_add_u32 s91, s91, s90\n"
    "v_cndmask_b32 v44, v44, v45, s[32:33]\n"
    "s_mul_i32 s88, s88, 286720\n"
    "v_cndmask_b32 v44, v44, v46, s[34:35]\n"
    "v_cndmask_b32 v44, v44, v47, s[36:37]\n"
    "ds_bpermute_b32 v46, v114, v40\n"
    "ds_bpermute_b32 v45, v113, v44\n"
    "s_add_u32 s88, s88, 9850880\n"
    "s_waitcnt lgkmcnt(1)\n"
    "v_mul_f32 v46, s39, v46\n"
    "v_mul_f32 v47, v46, v46\n"
    "s_waitcnt lgkmcnt(0)\n"
    "v_fma_f32 v44, v45, s39, -v47\n"
    "v_max_f32 v44, 0, v44\n"
    "v_add_u32 v124, 4096, v1\n"
    "v_add_f32 v44, s41, v44\n"
    "v_rsq_f32 v44, v44\n"
    "s_nop 0\n"
    "v_mul_f32 v44, s40, v44\n"
    "s_mul_i32 s89, s69, 5\n"
    "v_mul_f32 v45, -v46, v44\n"
    "ds_read_b128 v[216:219], v112 offset:7168\n"
    "ds_read_b128 v[220:223], v112 offset:7184\n"
    "v_pk_fma_f32 v[212:213], v[208:209], v[44:45], v[212:213] op_sel:[0,1,0] op_sel_hi:[1,1,1]\n"
    "s_add_u32 s89, s89, s88\n"
    "v_pk_fma_f32 v[214:215], v[210:211], v[44:45], v[214:215] op_sel:[0,1,0] op_sel_hi:[1,1,1]\n"
    "v_pk_fma_f32 v[16:17], v[16:17], v[44:45], v[212:213] op_sel_hi:[1,0,1]\n"
    "v_pk_fma_f32 v[18:19], v[18:19], v[44:45], v[214:215] op_sel_hi:[1,0,1]\n"
    "v_exp_f32 v208, v16\n"
    "buffer_load_dwordx4 a[100:103], v1, s[12:15], s89 offen offset:0 sc1\n"
    "v_exp_f32 v209, v17\n"
    "v_exp_f32 v210, v18\n"
    "v_exp_f32 v211, v19\n"
    "v_pk_fma_f32 v[208:209], v[208:209], s[44:45], s[44:45]\n"
    "v_pk_fma_f32 v[210:211], v[210:211], s[44:45], s[44:45]\n"
    "buffer_load_dwordx4 a[104:107], v1, s[12:15], s89 offen offset:1024 sc1\n"
    "v_pk_fma_f32 v[208:209], v[208:209], v[208:209], s[46:47]\n"
    "v_pk_fma_f32 v[210:211], v[210:211], v[210:211], s[46:47]\n"
    "v_rcp_f32 v208, v208\n"
    "v_rcp_f32 v209, v209\n"
    "v_rcp_f32 v210, v210\n"
    "buffer_load_dwordx4 a[108:111], v1, s[12:15], s89 offen offset:2048 sc1\n"
    "v_rcp_f32 v211, v211\n"
    "v_pk_fma_f32 v[16:17], v[16:17], v[208:209], v[16:17] neg_lo:[1,0,0] neg_hi:[1,0,0]\n"
    "v_pk_fma_f32 v[18:19], v[18:19], v[210:211], v[18:19] neg_lo:[1,0,0] neg_hi:[1,0,0]\n"
    "v_cvt_pk_bf16_f32 v224, v16, v17\n"
    "v_cvt_pk_bf16_f32 v225, v18, v19\n"
    "ds_read_b128 v[208:211], v112 offset:14336\n"
    "ds_read_b128 v[212:215], v112 offset:14352\n"
    "buffer_load_dwordx4 a[112:115], v1, s[12:15], s89 offen offset:3072 sc1\n"
    "s_waitcnt lgkmcnt(2)\n"
    "v_pk_fma_f32 v[220:221], v[216:217], v[44:45], v[220:221] op_sel:[0,1,0] op_sel_hi:[1,1,1]\n"
    "v_pk_fma_f32 v[222:223], v[218:219], v[44:45], v[222:223] op_sel:[0,1,0] op_sel_hi:[1,1,1]\n"
    "v_pk_fma_f32 v[20:21], v[20:21], v[44:45], v[220:221] op_sel_hi:[1,0,1]\n"
    "v_pk_fma_f32 v[22:23], v[22:23], v[44:45], v[222:223] op_sel_hi:[1,0,1]\n"
    "v_exp_f32 v216, v20\n"
    "v_exp_f32 v217, v21\n"
    "buffer_load_dwordx4 a[116:119], v124, s[12:15], s89 offen offset:0 sc1\n"
    "v_exp_f32 v218, v22\n"
    "v_exp_f32 v219, v23\n"
    "s_mul_i32 s89, s70, 5\n"
    "v_pk_fma_f32 v[216:217], v[216:217], s[44:45], s[44:45]\n"
    "v_pk_fma_f32 v[218:219], v[218:219], s[44:45], s[44:45]\n"
    "v_pk_fma_f32 v[216:217], v[216:217], v[216:217], s[46:47]\n"
    "v_pk_fma_f32 v[218:219], v[218:219], v[218:219], s[46:47]\n"
    "s_add_u32 s89, s89, s88\n"
    "v_rcp_f32 v216, v216\n"
    "v_rcp_f32 v217, v217\n"
    "buffer_load_dwordx4 a[120:123], v1, s[12:15], s89 offen offset:0 sc1\n"
    "v_rcp_f32 v218, v218\n"
    "v_rcp_f32 v219, v219\n"
    "v_pk_fma_f32 v[20:21], v[20:21], v[216:217], v[20:21] neg_lo:[1,0,0] neg_hi:[1,0,0]\n"
    "v_pk_fma_f32 v[22:23], v[22:23], v[218:219], v[22:23] neg_lo:[1,0,0] neg_hi:[1,0,0]\n"
    "v_cvt_pk_bf16_f32 v226, v20, v21\n"
    "v_cvt_pk_bf16_f32 v227, v22, v23\n"
    "buffer_load_dwordx4 a[124:127], v1, s[12:15], s89 offen offset:1024 sc1\n"
    "buffer_store_dwordx4 v[224:227], v1, s[12:15], s91 offen offset:0 sc1\n"
    "ds_read_b128 v[216:219], v112 offset:21504\n"
    "ds_read_b128 v[220:223], v112 offset:21520\n"
    "s_waitcnt lgkmcnt(2)\n"
    "v_pk_fma_f32 v[212:213], v[208:209], v[44:45], v[212:213] op_sel:[0,1,0] op_sel_hi:[1,1,1]\n"
    "v_pk_fma_f32 v[214:215], v[210:211], v[44:45], v[214:215] op_sel:[0,1,0] op_sel_hi:[1,1,1]\n"
    "v_pk_fma_f32 v[24:25], v[24:25], v[44:45], v[212:213] op_sel_hi:[1,0,1]\n"
    "v_pk_fma_f32 v[26:27], v[26:27], v[44:45], v[214:215] op_sel_hi:[1,0,1]\n"
    "v_exp_f32 v208, v24\n"
    "buffer_load_dwordx4 a[128:131], v1, s[12:15], s89 offen offset:2048 sc1\n"
    "v_exp_f32 v209, v25\n"
    "v_exp_f32 v210, v26\n"
    "v_exp_f32 v211, v27\n"
    "v_pk_fma_f32 v[208:209], v[208:209], s[44:45], s[44:45]\n"
    "v_pk_fma_f32 v[210:211], v[210:211], s[44:45], s[44:45]\n"
    "buffer_load_dwordx4 a[132:135], v1, s[12:15], s89 offen offset:3072 sc1\n"
    "v_pk_fma_f32 v[208:209], v[208:209], v[208:209], s[46:47]\n"
    "v_pk_fma_f32 v[210:211], v[210:211], v[210:211], s[46:47]\n"
    "v_rcp_f32 v208, v208\n"
    "v_rcp_f32 v209, v209\n"
    "v_rcp_f32 v210, v210\n"
    "buffer_load_dwordx4 a[136:139], v124, s[12:15], s89 offen offset:0 sc1\n"
    "v_rcp_f32 v211, v211\n"
    "v_pk_fma_f32 v[24:25], v[24:25], v[208:209], v[24:25] neg_lo:[1,0,0] neg_hi:[1,0,0]\n"
    "v_pk_fma_f32 v[26:27], v[26:27], v[210:211], v[26:27] neg_lo:[1,0,0] neg_hi:[1,0,0]\n"
    "v_cvt_pk_bf16_f32 v228, v24, v25\n"
    "v_cvt_pk_bf16_f32 v229, v26, v27\n"
    "ds_read_b128 v[208:211], v112 offset:28672\n"
    "ds_read_b128 v[212:215], v112 offset:28688\n"
    "s_waitcnt lgkmcnt(2)\n"
    "v_pk_fma_f32 v[220:221], v[216:217], v[44:45], v[220:221] op_sel:[0,1,0] op_sel_hi:[1,1,1]\n"
    "v_pk_fma_f32 v[222:223], v[218:219], v[44:45], v[222:223] op_sel:[0,1,0] op_sel_hi:[1,1,1]\n"
    "v_pk_fma_f32 v[28:29], v[28:29], v[44:45], v[220:221] op_sel_hi:[1,0,1]\n"
    "v_pk_fma_f32 v[30:31], v[30:31], v[44:45], v[222:223] op_sel_hi:[1,0,1]\n"
    "v_exp_f32 v216, v28\n"
    "v_exp_f32 v217, v29\n"
    "v_exp_f32 v218, v30\n"
    "v_exp_f32 v219, v31\n"
    "v_pk_fma_f32 v[216:217], v[216:217], s[44:45], s[44:45]\n"
    "v_pk_fma_f32 v[218:219], v[218:219], s[44:45], s[44:45]\n"
    "v_pk_fma_f32 v[216:217], v[216:217], v[216:217], s[46:47]\n"
    "v_pk_fma_f32 v[218:219], v[218:219], v[218:219], s[46:47]\n"
    "v_rcp_f32 v216, v216\n"
    "v_rcp_f32 v217, v217\n"
    "v_rcp_f32 v218, v218\n"
    "v_rcp_f32 v219, v219\n"
    "v_pk_fma_f32 v[28:29], v[28:29], v[216:217], v[28:29] neg_lo:[1,0,0] neg_hi:[1,0,0]\n"
    "v_pk_fma_f32 v[30:31], v[30:31], v[218:219], v[30:31] neg_lo:[1,0,0] neg_hi:[1,0,0]\n"
    "v_cvt_pk_bf16_f32 v230, v28, v29\n"
    "v_cvt_pk_bf16_f32 v231, v30, v31\n"
    "buffer_store_dwordx4 v[228:231], v1, s[12:15], s91 offen offset:1024 sc1\n"
    "ds_read_b128 v[216:219], v112 offset:35840\n"
    "ds_read_b128 v[220:223], v112 offset:35856\n"
    "s_waitcnt lgkmcnt(2)\n"
    "v_pk_fma_f32 v[212:213], v[208:209], v[44:45], v[212:213] op_sel:[0,1,0] op_sel_hi:[1,1,1]\n"
    "v_pk_fma_f32 v[214:215], v[210:211], v[44:45], v[214:215] op_sel:[0,1,0] op_sel_hi:[1,1,1]\n"
    "v_pk_fma_f32 v[32:33], v[32:33], v[44:45], v[212:213] op_sel_hi:[1,0,1]\n"
    "v_pk_fma_f32 v[34:35], v[34:35], v[44:45], v[214:215] op_sel_hi:[1,0,1]\n"
    "v_exp_f32 v208, v32\n"
    "v_exp_f32 v209, v33\n"
    "v_exp_f32 v210, v34\n"
    "v_exp_f32 v211, v35\n"
    "v_pk_fma_f32 v[208:209], v[208:209], s[44:45], s[44:45]\n"
    "v_pk_fma_f32 v[210:211], v[210:211], s[44:45], s[44:45]\n"
    "v_pk_fma_f32 v[208:209], v[208:209], v[208:209], s[46:47]\n"
    "v_pk_fma_f32 v[210:211], v[210:211], v[210:211], s[46:47]\n"
    "v_rcp_f32 v208, v208\n"
    "v_rcp_f32 v209, v209\n"
    "v_rcp_f32 v210, v210\n"
    "v_rcp_f32 v211, v211\n"
    "v_pk_fma_f32 v[32:33], v[32:33], v[208:209], v[32:33] neg_lo:[1,0,0] neg_hi:[1,0,0]\n"
    "v_pk_fma_f32 v[34:35], v[34:35], v[210:211], v[34:35] neg_lo:[1,0,0] neg_hi:[1,0,0]\n"
    "v_cvt_pk_bf16_f32 v224, v32, v33\n"
    "v_cvt_pk_bf16_f32 v225, v34, v35\n"
    "ds_read_b128 v[208:211], v112 offset:43008\n"
    "ds_read_b128 v[212:215], v112 offset:43024\n"
    "s_waitcnt lgkmcnt(2)\n"
    "v_pk_fma_f32 v[220:221], v[216:217], v[44:45], v[220:221] op_sel:[0,1,0] op_sel_hi:[1,1,1]\n"
    "v_pk_fma_f32 v[222:223], v[218:219], v[44:45], v[222:223] op_sel:[0,1,0] op_sel_hi:[1,1,1]\n"
    "v_pk_fma_f32 v[36:37], v[36:37], v[44:45], v[220:221] op_sel_hi:[1,0,1]\n"
    "v_pk_fma_f32 v[38:39], v[38:39], v[44:45], v[222:223] op_sel_hi:[1,0,1]\n"
    "v_exp_f32 v216, v36\n"
    "v_exp_f32 v217, v37\n"
    "v_exp_f32 v218, v38\n"
    "v_exp_f32 v219, v39\n"
    "v_pk_fma_f32 v[216:217], v[216:217], s[44:45], s[44:45]\n"
    "v_pk_fma_f32 v[218:219], v[218:219], s[44:45], s[44:45]\n"
    "v_pk_fma_f32 v[216:217], v[216:217], v[216:217], s[46:47]\n"
    "v_pk_fma_f32 v[218:219], v[218:219], v[218:219], s[46:47]\n"
    "v_rcp_f32 v216, v216\n"
    "v_rcp_f32 v217, v217\n"
    "v_rcp_f32 v218, v218\n"
    "v_rcp_f32 v219, v219\n"
    "v_pk_fma_f32 v[36:37], v[36:37], v[216:217], v[36:37] neg_lo:[1,0,0] neg_hi:[1,0,0]\n"
    "v_pk_fma_f32 v[38:39], v[38:39], v[218:219], v[38:39] neg_lo:[1,0,0] neg_hi:[1,0,0]\n"
    "v_cvt_pk_bf16_f32 v226, v36, v37\n"
    "v_cvt_pk_bf16_f32 v227, v38, v39\n"
    "buffer_store_dwordx4 v[224:227], v1, s[12:15], s91 offen offset:2048 sc1\n"
    "s_waitcnt lgkmcnt(0)\n"
    "v_pk_fma_f32 v[212:213], v[208:209], v[44:45], v[212:213] op_sel:[0,1,0] op_sel_hi:[1,1,1]\n"
    "v_pk_fma_f32 v[214:215], v[210:211], v[44:45], v[214:215] op_sel:[0,1,0] op_sel_hi:[1,1,1]\n"
    "v_pk_fma_f32 v[40:41], v[40:41], v[44:45], v[212:213] op_sel_hi:[1,0,1]\n"
    "v_pk_fma_f32 v[42:43], v[42:43], v[44:45], v[214:215] op_sel_hi:[1,0,1]\n"
    "v_exp_f32 v208, v40\n"
    "v_exp_f32 v209, v41\n"
    "v_exp_f32 v210, v42\n"
    "v_exp_f32 v211, v43\n"
    "v_pk_fma_f32 v[208:209], v[208:209], s[44:45], s[44:45]\n"
    "v_pk_fma_f32 v[210:211], v[210:211], s[44:45], s[44:45]\n"
    "v_pk_fma_f32 v[208:209], v[208:209], v[208:209], s[46:47]\n"
    "v_pk_fma_f32 v[210:211], v[210:211], v[210:211], s[46:47]\n"
    "v_rcp_f32 v208, v208\n"
    "v_rcp_f32 v209, v209\n"
    "v_rcp_f32 v210, v210\n"
    "v_rcp_f32 v211, v211\n"
    "v_pk_fma_f32 v[40:41], v[40:41], v[208:209], v[40:41] neg_lo:[1,0,0] neg_hi:[1,0,0]\n"
    "v_pk_fma_f32 v[42:43], v[42:43], v[210:211], v[42:43] neg_lo:[1,0,0] neg_hi:[1,0,0]\n"
    "v_cvt_pk_bf16_f32 v228, v40, v41\n"
    "v_cvt_pk_bf16_f32 v229, v42, v43\n"
    "v_cndmask_b32 v228, v228, 0, s[100:101]\n"
    "v_cndmask_b32 v229, v229, 0, s[100:101]\n"
    "v_mov_b32 v230, v228\n"
    "v_mov_b32 v231, v229\n"
    "s_nop 0\n"
    "v_permlane16_swap_b32 v228, v230\n"
    "v_permlane16_swap_b32 v229, v231\n"
    "buffer_store_dwordx4 v[228:231], v115, s[12:15], s90 offen sc1\n"
    "v_mfma_f32_16x16x32_bf16 v[16:19], v[96:99], a[200:203], 0\n"
    "ds_read_b128 v[88:91], v5 offset:28672\n"
    "v_and_b32 v12, 1, v0\n"
    "v_mfma_f32_16x16x32_bf16 v[20:23], v[100:103], a[200:203], 0\n"
    "ds_read_b128 v[92:95], v5 offset:29696\n"
    "v_mul_u32_u24 v12, 384, v12\n"
    "v_mfma_f32_16x16x32_bf16 v[24:27], v[104:107], a[200:203], 0\n"
    "ds_read_b128 v[96:99], v5 offset:30720\n"
    "v_add_u32 v12, s58, v12\n"
    "v_mfma_f32_16x16x32_bf16 v[28:31], v[108:111], a[200:203], 0\n"
    "ds_read_b128 v[100:103], v5 offset:31744\n"
    "v_mfma_f32_16x16x32_bf16 v[32:35], v[48:51], a[200:203], 0\n"
    "ds_read_b128 v[104:107], v5 offset:32768\n"
    "s_and_b32 s88, s55, 7\n"
    "v_mfma_f32_16x16x32_bf16 v[36:39], v[52:55], a[200:203], 0\n"
    "ds_read_b128 v[108:111], v5 offset:33792\n"
    "s_mul_i32 s88, s88, 286720\n"
    "v_mfma_f32_16x16x32_bf16 v[40:43], v[56:59], a[200:203], 0\n"
    "ds_read_b128 v[48:51], v5 offset:34816\n"
    "s_add_u32 s88, s88, 9850880\n"
    "v_mfma_f32_16x16x32_bf16 v[44:47], a[200:203], a[200:203], 0\n"
    "v_add_u32 v124, 4096, v1\n"
    "v_mfma_f32_16x16x32_bf16 v[16:19], v[60:63], a[204:207], v[16:19]\n"
    "ds_read_b128 v[52:55], v6 offset:28672\n"
    "s_mul_i32 s89, s71, 5\n"
    "v_mfma_f32_16x16x32_bf16 v[20:23], v[64:67], a[204:207], v[20:23]\n"
    "ds_read_b128 v[56:59], v6 offset:29696\n"
    "s_add_u32 s89, s89, s88\n"
    "v_mfma_f32_16x16x32_bf16 v[24:27], v[68:71], a[204:207], v[24:27]\n"
    "ds_read_b128 v[60:63], v6 offset:30720\n"
    "buffer_load_dwordx4 a[140:143], v1, s[12:15], s89 offen offset:0 sc1\n"
    "v_mfma_f32_16x16x32_bf16 v[28:31], v[72:75], a[204:207], v[28:31]\n"
    "ds_read_b128 v[64:67], v6 offset:31744\n"
    "v_mfma_f32_16x16x32_bf16 v[32:35], v[76:79], a[204:207], v[32:35]\n"
    "ds_read_b128 v[68:71], v6 offset:32768\n"
    "v_mfma_f32_16x16x32_bf16 v[36:39], v[80:83], a[204:207], v[36:39]\n"
    "ds_read_b128 v[72:75], v6 offset:33792\n"
    "v_mfma_f32_16x16x32_bf16 v[40:43], v[84:87], a[204:207], v[40:43]\n"
    "ds_read_b128 v[76:79], v6 offset:34816\n"
    "buffer_load_dwordx4 a[144:147], v1, s[12:15], s89 offen offset:1024 sc1\n"
    "v_mfma_f32_16x16x32_bf16 v[44:47], a[204:207], a[204:207], v[44:47]\n"
    "s_waitcnt lgkmcnt(10)\n"
    "v_mfma_f32_16x16x32_bf16 v[16:19], v[88:91], a[208:211], v[16:19]\n"
    "ds_read_b128 v[80:83], v1 offset:0\n"
    "v_mfma_f32_16x16x32_bf16 v[20:23], v[92:95], a[208:211], v[20:23]\n"
    "ds_read_b128 v[84:87], v1 offset:1024\n"
    "v_mfma_f32_16x16x32_bf16 v[24:27], v[96:99], a[208:211], v[24:27]\n"
    "ds_read_b128 v[88:91], v1 offset:2048\n"
    "buffer_load_dwordx4 a[148:151], v1, s[12:15], s89 offen offset:2048 sc1\n"
    "v_mfma_f32_16x16x32_bf16 v[28:31], v[100:103], a[208:211], v[28:31]\n"
    "ds_read_b128 v[92:95], v1 offset:3072\n"
    "s_waitcnt lgkmcnt(11)\n"
    "v_mfma_f32_16x16x32_bf16 v[32:35], v[104:107], a[208:211], v[32:35]\n"
    "ds_read_b128 v[96:99], v1 offset:4096\n"
    "v_mfma_f32_16x16x32_bf16 v[36:39], v[108:111], a[208:211], v[36:39]\n"
    "ds_read_b128 v[100:103], v1 offset:5120\n"
    "v_mfma_f32_16x16x32_bf16 v[40:43], v[48:51], a[208:211], v[40:43]\n"
    "ds_read_b128 v[104:107], v1 offset:6144\n"
    "buffer_load_dwordx4 a[152:155], v1, s[12:15], s89 offen offset:3072 sc1\n"
    "v_mfma_f32_16x16x32_bf16 v[44:47], a[208:211], a[208:211], v[44:47]\n"
    "s_waitcnt lgkmcnt(10)\n"
    "v_mfma_f32_16x16x32_bf16 v[16:19], v[52:55], a[212:215], v[16:19]\n"
    "ds_read_b128 v[108:111], v4 offset:0\n"
    "v_mfma_f32_16x16x32_bf16 v[20:23], v[56:59], a[212:215], v[20:23]\n"
    "ds_read_b128 v[48:51], v4 offset:1024\n"
    "v_mfma_f32_16x16x32_bf16 v[24:27], v[60:63], a[212:215], v[24:27]\n"
    "ds_read_b128 v[52:55], v4 offset:2048\n"
    "buffer_load_dwordx4 a[156:159], v124, s[12:15], s89 offen offset:0 sc1\n"
    "v_mfma_f32_16x16x32_bf16 v[28:31], v[64:67], a[212:215], v[28:31]\n"
    "ds_read_b128 v[56:59], v4 offset:3072\n"
    "s_waitcnt lgkmcnt(11)\n"
    "v_mfma_f32_16x16x32_bf16 v[32:35], v[68:71], a[212:215], v[32:35]\n"
    "ds_read_b128 v[60:63], v4 offset:4096\n"
    "s_mul_i32 s89, s72, 5\n"
    "v_mfma_f32_16x16x32_bf16 v[36:39], v[72:75], a[212:215], v[36:39]\n"
    "ds_read_b128 v[64:67], v4 offset:5120\n"
    "s_add_u32 s89, s89, s88\n"
    "v_mfma_f32_16x16x32_bf16 v[40:43], v[76:79], a[212:215], v[40:43]\n"
    "ds_read_b128 v[68:71], v4 offset:6144\n"
    "buffer_load_dwordx4 a[160:163], v1, s[12:15], s89 offen offset:0 sc1\n"
    "v_mfma_f32_16x16x32_bf16 v[44:47], a[212:215], a[212:215], v[44:47]\n"
    "s_waitcnt lgkmcnt(10)\n"
    "v_mfma_f32_16x16x32_bf16 v[16:19], v[80:83], a[216:219], v[16:19]\n"
    "ds_read_b128 v[72:75], v5 offset:0\n"
    "v_mfma_f32_16x16x32_bf16 v[20:23], v[84:87], a[216:219], v[20:23]\n"
    "ds_read_b128 v[76:79], v5 offset:1024\n"
    "v_mfma_f32_16x16x32_bf16 v[24:27], v[88:91], a[216:219], v[24:27]\n"
    "ds_read_b128 v[80:83], v5 offset:2048\n"
    "buffer_load_dwordx4 a[164:167], v1, s[12:15], s89 offen offset:1024 sc1\n"
    "v_mfma_f32_16x16x32_bf16 v[28:31], v[92:95], a[216:219], v[28:31]\n"
    "ds_read_b128 v[84:87], v5 offset:3072\n"
    "s_waitcnt lgkmcnt(11)\n"
    "v_mfma_f32_16x16x32_bf16 v[32:35], v[96:99], a[216:219], v[32:35]\n"
    "ds_read_b128 v[88:91], v5 offset:4096\n"
    "v_mfma_f32_16x16x32_bf16 v[36:39], v[100:103], a[216:219], v[36:39]\n"
    "ds_read_b128 v[92:95], v5 offset:5120\n"
    "v_mfma_f32_16x16x32_bf16 v[40:43], v[104:107], a[216:219], v[40:43]\n"
    "ds_read_b128 v[96:99], v5 offset:6144\n"
    "buffer_load_dwordx4 a[168:171], v1, s[12:15], s89 offen offset:2048 sc1\n"
    "v_mfma_f32_16x16x32_bf16 v[44:47], a[216:219], a[216:219], v[44:47]\n"
    "s_waitcnt lgkmcnt(10)\n"
    "s_waitcnt vmcnt(8)\n"
    "v_mfma_f32_16x16x32_bf16 v[16:19], v[108:111], a[220:223], v[16:19]\n"
    "ds_read_b128 v[100:103], v6 offset:0\n"
    "s_add_u32 s51, s50, s52\n"
    "v_mfma_f32_16x16x32_bf16 v[20:23], v[48:51], a[220:223], v[20:23]\n"
    "ds_read_b128 v[104:107], v6 offset:1024\n"
    "s_mov_b64 exec, 1\n"
    "buffer_atomic_add v123, v1, s[12:15], s51 offen offset:0\n"
    "s_mov_b64 exec, -1\n"
    "v_mfma_f32_16x16x32_bf16 v[24:27], v[52:55], a[220:223], v[24:27]\n"
    "ds_read_b128 v[108:111], v6 offset:2048\n"
    "buffer_load_dwordx4 a[172:175], v1, s[12:15], s89 offen offset:3072 sc1\n"
    "v_mfma_f32_16x16x32_bf16 v[28:31], v[56:59], a[220:223], v[28:31]\n"
    "ds_read_b128 v[48:51], v6 offset:3072\n"
    "s_waitcnt lgkmcnt(11)\n"
    "v_mfma_f32_16x16x32_bf16 v[32:35], v[60:63], a[220:223], v[32:35]\n"
    "ds_read_b128 v[52:55], v6 offset:4096\n"
    "v_mfma_f32_16x16x32_bf16 v[36:39], v[64:67], a[220:223], v[36:39]\n"
    "ds_read_b128 v[56:59], v6 offset:5120\n"
    "v_mfma_f32_16x16x32_bf16 v[40:43], v[68:71], a[220:223], v[40:43]\n"
    "ds_read_b128 v[60:63], v6 offset:6144\n"
    "buffer_load_dwordx4 a[176:179], v124, s[12:15], s89 offen offset:0 sc1\n"
    "v_mfma_f32_16x16x32_bf16 v[44:47], a[220:223], a[220:223], v[44:47]\n"
    "s_waitcnt lgkmcnt(10)\n"
    "s_mul_i32 s89, s73, 5\n"
    "v_mfma_f32_16x16x32_bf16 v[16:19], v[72:75], a[224:227], v[16:19]\n"
    "ds_read_b128 v[64:67], v1 offset:7168\n"
    "s_add_u32 s89, s89, s88\n"
    "v_mfma_f32_16x16x32_bf16 v[20:23], v[76:79], a[224:227], v[20:23]\n"
    "ds_read_b128 v[68:71], v1 offset:8192\n"
    "v_mfma_f32_16x16x32_bf16 v[24:27], v[80:83], a[224:227], v[24:27]\n"
    "ds_read_b128 v[72:75], v1 offset:9216\n"
    "buffer_load_dwordx4 a[180:183], v1, s[12:15], s89 offen offset:0 sc1\n"
    "v_mfma_f32_16x16x32_bf16 v[28:31], v[84:87], a[224:227], v[28:31]\n"
    "ds_read_b128 v[76:79], v1 offset:10240\n"
    "s_waitcnt lgkmcnt(11)\n"
    "v_mfma_f32_16x16x32_bf16 v[32:35], v[88:91], a[224:227], v[32:35]\n"
    "ds_read_b128 v[80:83], v1 offset:11264\n"
    "v_mfma_f32_16x16x32_bf16 v[36:39], v[92:95], a[224:227], v[36:39]\n"
    "ds_read_b128 v[84:87], v1 offset:12288\n"
    "v_mfma_f32_16x16x32_bf16 v[40:43], v[96:99], a[224:227], v[40:43]\n"
    "ds_read_b128 v[88:91], v1 offset:13312\n"
    "buffer_load_dwordx4 a[184:187], v1, s[12:15], s89 offen offset:1024 sc1\n"
    "v_mfma_f32_16x16x32_bf16 v[44:47], a[224:227], a[224:227], v[44:47]\n"
    "s_waitcnt lgkmcnt(10)\n"
    "v_mfma_f32_16x16x32_bf16 v[16:19], v[100:103], a[228:231], v[16:19]\n"
    "ds_read_b128 v[92:95], v4 offset:7168\n"
    "v_mfma_f32_16x16x32_bf16 v[20:23], v[104:107], a[228:231], v[20:23]\n"
    "ds_read_b128 v[96:99], v4 offset:8192\n"
    "v_mfma_f32_16x16x32_bf16 v[24:27], v[108:111], a[228:231], v[24:27]\n"
    "ds_read_b128 v[100:103], v4 offset:9216\n"
    "buffer_load_dwordx4 a[188:191], v1, s[12:15], s89 offen offset:2048 sc1\n"
    "v_mfma_f32_16x16x32_bf16 v[28:31], v[48:51], a[228:231], v[28:31]\n"
    "ds_read_b128 v[104:107], v4 offset:10240\n"
    "s_waitcnt lgkmcnt(11)\n"
    "v_mfma_f32_16x16x32_bf16 v[32:35], v[52:55], a[228:231], v[32:35]\n"
    "ds_read_b128 v[108:111], v4 offset:11264\n"
    "s_lshl_b32 s83, s9, 10\n"
    "v_mfma_f32_16x16x32_bf16 v[36:39], v[56:59], a[228:231], v[36:39]\n"
    "ds_read_b128 v[48:51], v4 offset:12288\n"
    "s_add_u32 s83, s83, 143360\n"
    "v_mfma_f32_16x16x32_bf16 v[40:43], v[60:63], a[228:231], v[40:43]\n"
    "ds_read_b128 v[52:55], v4 offset:13312\n"
    "buffer_load_dwordx4 a[192:195], v1, s[12:15], s89 offen offset:3072 sc1\n"
    "v_mfma_f32_16x16x32_bf16 v[44:47], a[228:231], a[228:231], v[44:47]\n"
    "s_waitcnt lgkmcnt(10)\n"
    "v_add_u32 v8, s83, v1\n"
    "v_mfma_f32_16x16x32_bf16 v[16:19], v[64:67], a[232:235], v[16:19]\n"
    "ds_read_b128 v[56:59], v5 offset:7168\n"
    "s_and_b32 s86, s55, 7\n"
    "v_mfma_f32_16x16x32_bf16 v[20:23], v[68:71], a[232:235], v[20:23]\n"
    "ds_read_b128 v[60:63], v5 offset:8192\n"
    "s_mul_i32 s86, s86, 5\n"
    "v_mfma_f32_16x16x32_bf16 v[24:27], v[72:75], a[232:235], v[24:27]\n"
    "ds_read_b128 v[64:67], v5 offset:9216\n"
    "buffer_load_dwordx4 a[196:199], v124, s[12:15], s89 offen offset:0 sc1\n"
    "v_mfma_f32_16x16x32_bf16 v[28:31], v[76:79], a[232:235], v[28:31]\n"
    "ds_read_b128 v[68:71], v5 offset:10240\n"
    "s_waitcnt lgkmcnt(11)\n"
    "v_mfma_f32_16x16x32_bf16 v[32:35], v[80:83], a[232:235], v[32:35]\n"
    "ds_read_b128 v[72:75], v5 offset:11264\n"
    "s_mul_i32 s89, s74, 5\n"
    "v_mfma_f32_16x16x32_bf16 v[36:39], v[84:87], a[232:235], v[36:39]\n"
    "ds_read_b128 v[76:79], v5 offset:12288\n"
    "s_add_u32 s89, s89, s88\n"
    "v_mfma_f32_16x16x32_bf16 v[40:43], v[88:91], a[232:235], v[40:43]\n"
    "ds_read_b128 v[80:83], v5 offset:13312\n"
    "buffer_load_dwordx4 a[200:203], v1, s[12:15], s89 offen offset:0 sc1\n"
    "v_mfma_f32_16x16x32_bf16 v[44:47], a[232:235], a[232:235], v[44:47]\n"
    "s_waitcnt lgkmcnt(10)\n"
    "s_sub_u32 s84, s19, s86\n"
    "v_mfma_f32_16x16x32_bf16 v[16:19], v[92:95], a[236:239], v[16:19]\n"
    "ds_read_b128 v[84:87], v6 offset:7168\n"
    "s_sub_u32 s85, s20, s86\n"
    "v_mfma_f32_16x16x32_bf16 v[20:23], v[96:99], a[236:239], v[20:23]\n"
    "ds_read_b128 v[88:91], v6 offset:8192\n"
    "s_lshl_b32 s83, s84, 12\n"
    "v_mfma_f32_16x16x32_bf16 v[24:27], v[100:103], a[236:239], v[24:27]\n"
    "ds_read_b128 v[92:95], v6 offset:9216\n"
    "buffer_load_dwordx4 a[204:207], v1, s[12:15], s89 offen offset:1024 sc1\n"
    "v_mfma_f32_16x16x32_bf16 v[28:31], v[104:107], a[236:239], v[28:31]\n"
    "ds_read_b128 v[96:99], v6 offset:10240\n"
    "s_waitcnt lgkmcnt(11)\n"
    "v_mfma_f32_16x16x32_bf16 v[32:35], v[108:111], a[236:239], v[32:35]\n"
    "ds_read_b128 v[100:103], v6 offset:11264\n"
    "s_add_u32 s83, s83, 143360\n"
    "v_mfma_f32_16x16x32_bf16 v[36:39], v[48:51], a[236:239], v[36:39]\n"
    "ds_read_b128 v[104:107], v6 offset:12288\n"
    "v_add_u32 v9, s83, v1\n"
    "v_mfma_f32_16x16x32_bf16 v[40:43], v[52:55], a[236:239], v[40:43]\n"
    "ds_read_b128 v[108:111], v6 offset:13312\n"
    "buffer_load_dwordx4 a[208:211], v1, s[12:15], s89 offen offset:2048 sc1\n"
    "v_mfma_f32_16x16x32_bf16 v[44:47], a[236:239], a[236:239], v[44:47]\n"
    "s_waitcnt lgkmcnt(10)\n"
    "s_lshl_b32 s83, s85, 12\n"
    "v_mfma_f32_16x16x32_bf16 v[16:19], v[56:59], a[240:243], v[16:19]\n"
    "ds_read_b128 v[48:51], v1 offset:14336\n"
    "s_add_u32 s83, s83, 143360\n"
    "v_mfma_f32_16x16x32_bf16 v[20:23], v[60:63], a[240:243], v[20:23]\n"
    "ds_read_b128 v[52:55], v1 offset:15360\n"
    "v_add_u32 v10, s83, v1\n"
    "v_mfma_f32_16x16x32_bf16 v[24:27], v[64:67], a[240:243], v[24:27]\n"
    "ds_read_b128 v[56:59], v1 offset:16384\n"
    "buffer_load_dwordx4 a[212:215], v1, s[12:15], s89 offen offset:3072 sc1\n"
    "v_mfma_f32_16x16x32_bf16 v[28:31], v[68:71], a[240:243], v[28:31]\n"
    "ds_read_b128 v[60:63], v1 offset:17408\n"
    "s_waitcnt lgkmcnt(11)\n"
    "v_mfma_f32_16x16x32_bf16 v[32:35], v[72:75], a[240:243], v[32:35]\n"
    "ds_read_b128 v[64:67], v1 offset:18432\n"
    "s_cmp_eq_u32 s16, 0\n"
    "s_cselect_b32 s84, 2147418112, 0\n"
    "v_mfma_f32_16x16x32_bf16 v[36:39], v[76:79], a[240:243], v[36:39]\n"
    "ds_read_b128 v[68:71], v1 offset:19456\n"
    "s_cmp_eq_u32 s17, 0\n"
    "s_cselect_b32 s85, 2147418112, 0\n"
    "v_mfma_f32_16x16x32_bf16 v[40:43], v[80:83], a[240:243], v[40:43]\n"
    "ds_read_b128 v[72:75], v1 offset:20480\n"
    "buffer_load_dwordx4 a[216:219], v124, s[12:15], s89 offen offset:0 sc1\n"
    "v_mfma_f32_16x16x32_bf16 v[44:47], a[240:243], a[240:243], v[44:47]\n"
    "s_waitcnt lgkmcnt(10)\n"
    "s_mul_i32 s89, s75, 5\n"
    "v_mfma_f32_16x16x32_bf16 v[16:19], v[84:87], a[244:247], v[16:19]\n"
    "ds_read_b128 v[76:79], v4 offset:14336\n"
    "s_add_u32 s89, s89, s88\n"
    "v_mfma_f32_16x16x32_bf16 v[20:23], v[88:91], a[244:247], v[20:23]\n"
    "ds_read_b128 v[80:83], v4 offset:15360\n"
    "v_and_b32 v125, 15, v0\n"
    "v_mfma_f32_16x16x32_bf16 v[24:27], v[92:95], a[244:247], v[24:27]\n"
    "ds_read_b128 v[84:87], v4 offset:16384\n"
    "buffer_load_dwordx4 a[220:223], v1, s[12:15], s89 offen offset:0 sc1\n"
    "v_mfma_f32_16x16x32_bf16 v[28:31], v[96:99], a[244:247], v[28:31]\n"
    "ds_read_b128 v[88:91], v4 offset:17408\n"
    "s_waitcnt lgkmcnt(11)\n"
    "v_mfma_f32_16x16x32_bf16 v[32:35], v[100:103], a[244:247], v[32:35]\n"
    "ds_read_b128 v[92:95], v4 offset:18432\n"
    "v_lshrrev_b32 v126, 4, v0\n"
    "v_mfma_f32_16x16x32_bf16 v[36:39], v[104:107], a[244:247], v[36:39]\n"
    "ds_read_b128 v[96:99], v4 offset:19456\n"
    "v_lshrrev_b32 v121, 1, v126\n"
    "v_mfma_f32_16x16x32_bf16 v[40:43], v[108:111], a[244:247], v[40:43]\n"
    "ds_read_b128 v[100:103], v4 offset:20480\n"
    "buffer_load_dwordx4 a[224:227], v1, s[12:15], s89 offen offset:1024 sc1\n"
    "v_mfma_f32_16x16x32_bf16 v[44:47], a[244:247], a[244:247], v[44:47]\n"
    "s_waitcnt lgkmcnt(10)\n"
    "v_lshlrev_b32 v120, 8, v121\n"
    "v_mfma_f32_16x16x32_bf16 v[16:19], v[48:51], a[248:251], v[16:19]\n"
    "ds_read_b128 v[104:107], v5 offset:14336\n"
    "v_lshl_add_u32 v120, v125, 4, v120\n"
    "v_mfma_f32_16x16x32_bf16 v[20:23], v[52:55], a[248:251], v[20:23]\n"
    "ds_read_b128 v[108:111], v5 offset:15360\n"
    "v_add_u32 v120, s21, v120\n"
    "v_mfma_f32_16x16x32_bf16 v[24:27], v[56:59], a[248:251], v[24:27]\n"
    "ds_read_b128 v[48:51], v5 offset:16384\n"
    "buffer_load_dwordx4 a[228:231], v1, s[12:15], s89 offen offset:2048 sc1\n"
    "v_mfma_f32_16x16x32_bf16 v[28:31], v[60:63], a[248:251], v[28:31]\n"
    "ds_read_b128 v[52:55], v5 offset:17408\n"
    "s_waitcnt lgkmcnt(11)\n"
    "v_mfma_f32_16x16x32_bf16 v[32:35], v[64:67], a[248:251], v[32:35]\n"
    "ds_read_b128 v[56:59], v5 offset:18432\n"
    "v_and_b32 v122, 1, v126\n"
    "v_mfma_f32_16x16x32_bf16 v[36:39], v[68:71], a[248:251], v[36:39]\n"
    "ds_read_b128 v[60:63], v5 offset:19456\n"
    "v_lshl_add_u32 v13, v122, 10, v120\n"
    "v_mfma_f32_16x16x32_bf16 v[40:43], v[72:75], a[248:251], v[40:43]\n"
    "ds_read_b128 v[64:67], v5 offset:20480\n"
    "buffer_load_dwordx4 a[232:235], v1, s[12:15], s89 offen offset:3072 sc1\n"
    "v_mfma_f32_16x16x32_bf16 v[44:47], a[248:251], a[248:251], v[44:47]\n"
    "s_waitcnt lgkmcnt(10)\n"
    "s_cmp_ge_u32 s9, 2\n"
    "s_cselect_b64 vcc, -1, 0\n"
    "v_cndmask_b32 v13, v1, v13, vcc\n"
    "v_mfma_f32_16x16x32_bf16 v[16:19], v[76:79], a[252:255], v[16:19]\n"
    "ds_read_b128 v[68:71], v6 offset:14336\n"
    "v_add_u32 v121, s85, v1\n"
    "v_mfma_f32_16x16x32_bf16 v[20:23], v[80:83], a[252:255], v[20:23]\n"
    "ds_read_b128 v[72:75], v6 offset:15360\n"
    "s_cmp_eq_u32 s9, 2\n"
    "s_cselect_b32 s84, 0, 2147418112\n"
    "v_mfma_f32_16x16x32_bf16 v[24:27], v[84:87], a[252:255], v[24:27]\n"
    "ds_read_b128 v[76:79], v6 offset:16384\n"
    "buffer_load_dwordx4 a[236:239], v124, s[12:15], s89 offen offset:0 sc1\n"
    "v_mfma_f32_16x16x32_bf16 v[28:31], v[88:91], a[252:255], v[28:31]\n"
    "ds_read_b128 v[80:83], v6 offset:17408\n"
    "s_waitcnt lgkmcnt(11)\n"
    "v_mfma_f32_16x16x32_bf16 v[32:35], v[92:95], a[252:255], v[32:35]\n"
    "ds_read_b128 v[84:87], v6 offset:18432\n"
    "s_mul_i32 s89, s76, 5\n"
    "v_mfma_f32_16x16x32_bf16 v[36:39], v[96:99], a[252:255], v[36:39]\n"
    "ds_read_b128 v[88:91], v6 offset:19456\n"
    "s_add_u32 s89, s89, s88\n"
    "v_mfma_f32_16x16x32_bf16 v[40:43], v[100:103], a[252:255], v[40:43]\n"
    "ds_read_b128 v[92:95], v6 offset:20480\n"
    "buffer_load_dwordx4 a[240:243], v1, s[12:15], s89 offen offset:0 sc1\n"
    "v_mfma_f32_16x16x32_bf16 v[44:47], a[252:255], a[252:255], v[44:47]\n"
    "s_waitcnt lgkmcnt(10)\n"
    "v_add_u32 v120, s84, v120\n"
    "v_mfma_f32_16x16x32_bf16 v[16:19], v[104:107], v[232:235], v[16:19]\n"
    "ds_read_b128 v[96:99], v1 offset:21504\n"
    "v_mul_u32_u24 v122, 0x7fff, v122\n"
    "v_mfma_f32_16x16x32_bf16 v[20:23], v[108:111], v[232:235], v[20:23]\n"
    "ds_read_b128 v[100:103], v1 offset:22528\n"
    "v_lshlrev_b32 v122, 16, v122\n"
    "v_mfma_f32_16x16x32_bf16 v[24:27], v[48:51], v[232:235], v[24:27]\n"
    "ds_read_b128 v[104:107], v1 offset:23552\n"
    "buffer_load_dwordx4 a[244:247], v1, s[12:15], s89 offen offset:1024 sc1\n"
    "v_mfma_f32_16x16x32_bf16 v[28:31], v[52:55], v[232:235], v[28:31]\n"
    "ds_read_b128 v[108:111], v1 offset:24576\n"
    "s_waitcnt lgkmcnt(11)\n"
    "v_mfma_f32_16x16x32_bf16 v[32:35], v[56:59], v[232:235], v[32:35]\n"
    "ds_read_b128 v[48:51], v1 offset:25600\n"
    "buffer_load_dword v117, v12, s[12:15], s52 offen offset:0 sc1\n"
    "v_mfma_f32_16x16x32_bf16 v[36:39], v[60:63], v[232:235], v[36:39]\n"
    "ds_read_b128 v[52:55], v1 offset:26624\n"
    "v_or_b32 v120, v120, v122\n"
    "v_mfma_f32_16x16x32_bf16 v[40:43], v[64:67], v[232:235], v[40:43]\n"
    "ds_read_b128 v[56:59], v1 offset:27648\n"
    "buffer_load_dwordx2 v[118:119], v116, s[12:15], 0 offen offset:128 sc1\n"
    "v_mfma_f32_16x16x32_bf16 v[44:47], v[232:235], v[232:235], v[44:47]\n"
    "s_waitcnt lgkmcnt(10)\n"
    "v_min_u32 v120, v120, v121\n"
    "v_mfma_f32_16x16x32_bf16 v[16:19], v[68:71], v[236:239], v[16:19]\n"
    "ds_read_b128 v[60:63], v4 offset:21504\n"
    "v_lshrrev_b32 v122, 2, v125\n"
    "v_mfma_f32_16x16x32_bf16 v[20:23], v[72:75], v[236:239], v[20:23]\n"
    "ds_read_b128 v[64:67], v4 offset:22528\n"
    "v_lshlrev_b32 v122, 8, v122\n"
    "v_mfma_f32_16x16x32_bf16 v[24:27], v[76:79], v[236:239], v[24:27]\n"
    "ds_read_b128 v[68:71], v4 offset:23552\n"
    "buffer_load_dwordx4 a[248:251], v1, s[12:15], s89 offen offset:2048 sc1\n"
    "v_mfma_f32_16x16x32_bf16 v[28:31], v[80:83], v[236:239], v[28:31]\n"
    "ds_read_b128 v[72:75], v4 offset:24576\n"
    "s_waitcnt lgkmcnt(11)\n"
    "v_mfma_f32_16x16x32_bf16 v[32:35], v[84:87], v[236:239], v[32:35]\n"
    "ds_read_b128 v[76:79], v4 offset:25600\n"
    "v_and_b32 v125, 3, v125\n"
    "v_mfma_f32_16x16x32_bf16 v[36:39], v[88:91], v[236:239], v[36:39]\n"
    "ds_read_b128 v[80:83], v4 offset:26624\n"
    "v_lshl_add_u32 v122, v125, 3, v122\n"
    "v_mfma_f32_16x16x32_bf16 v[40:43], v[92:95], v[236:239], v[40:43]\n"
    "ds_read_b128 v[84:87], v4 offset:27648\n"
    "buffer_load_dwordx4 a[252:255], v1, s[12:15], s89 offen offset:3072 sc1\n"
    "v_mfma_f32_16x16x32_bf16 v[44:47], v[236:239], v[236:239], v[44:47]\n"
    "s_waitcnt lgkmcnt(10)\n"
    "v_add_u32 v122, 56528, v122\n"
    "v_mfma_f32_16x16x32_bf16 v[16:19], v[96:99], v[240:243], v[16:19]\n"
    "ds_read_b128 v[88:91], v5 offset:21504\n"
    "s_mul_i32 s83, s9, 7168\n"
    "v_mfma_f32_16x16x32_bf16 v[20:23], v[100:103], v[240:243], v[20:23]\n"
    "ds_read_b128 v[92:95], v5 offset:22528\n"
    "v_add_u32 v125, s83, v122\n"
    "v_mfma_f32_16x16x32_bf16 v[24:27], v[104:107], v[240:243], v[24:27]\n"
    "ds_read_b128 v[96:99], v5 offset:23552\n"
    "buffer_load_dwordx4 v[232:235], v124, s[12:15], s89 offen offset:0 sc1\n"
    "v_mfma_f32_16x16x32_bf16 v[28:31], v[108:111], v[240:243], v[28:31]\n"
    "ds_read_b128 v[100:103], v5 offset:24576\n"
    "s_waitcnt lgkmcnt(11)\n"
    "v_mfma_f32_16x16x32_bf16 v[32:35], v[48:51], v[240:243], v[32:35]\n"
    "ds_read_b128 v[104:107], v5 offset:25600\n"
    "s_mul_i32 s89, s77, 5\n"
    "v_mfma_f32_16x16x32_bf16 v[36:39], v[52:55], v[240:243], v[36:39]\n"
    "ds_read_b128 v[108:111], v5 offset:26624\n"
    "s_add_u32 s89, s89, s88\n"
    "v_mfma_f32_16x16x32_bf16 v[40:43], v[56:59], v[240:243], v[40:43]\n"
    "ds_read_b128 v[48:51], v5 offset:27648\n"
    "buffer_load_dwordx4 v[236:239], v1, s[12:15], s89 offen offset:0 sc1\n"
    "v_mfma_f32_16x16x32_bf16 v[44:47], v[240:243], v[240:243], v[44:47]\n"
    "s_waitcnt lgkmcnt(10)\n"
    "v_mul_u32_u24 v126, 7168, v126\n"
    "v_mfma_f32_16x16x32_bf16 v[16:19], v[60:63], v[244:247], v[16:19]\n"
    "ds_read_b128 v[52:55], v6 offset:21504\n"
    "v_add_u32 v7, v126, v122\n"
    "v_mfma_f32_16x16x32_bf16 v[20:23], v[64:67], v[244:247], v[20:23]\n"
    "ds_read_b128 v[56:59], v6 offset:22528\n"
    "v_and_b32 v122, 1, v0\n"
    "v_mfma_f32_16x16x32_bf16 v[24:27], v[68:71], v[244:247], v[24:27]\n"
    "ds_read_b128 v[60:63], v6 offset:23552\n"
    "buffer_load_dwordx4 v[240:243], v1, s[12:15], s89 offen offset:1024 sc1\n"
    "v_mfma_f32_16x16x32_bf16 v[28:31], v[72:75], v[244:247], v[28:31]\n"
    "ds_read_b128 v[64:67], v6 offset:24576\n"
    "s_waitcnt lgkmcnt(11)\n"
    "v_mfma_f32_16x16x32_bf16 v[32:35], v[76:79], v[244:247], v[32:35]\n"
    "ds_read_b128 v[68:71], v6 offset:25600\n"
    "v_mul_u32_u24 v122, 384, v122\n"
    "v_mfma_f32_16x16x32_bf16 v[36:39], v[80:83], v[244:247], v[36:39]\n"
    "ds_read_b128 v[72:75], v6 offset:26624\n"
    "s_lshl_b32 s83, s9, 2\n"
    "v_mfma_f32_16x16x32_bf16 v[40:43], v[84:87], v[244:247], v[40:43]\n"
    "ds_read_b128 v[76:79], v6 offset:27648\n"
    "v_lshrrev_b32 v11, 4, v0\n"
    "v_mfma_f32_16x16x32_bf16 v[44:47], v[244:247], v[244:247], v[44:47]\n"
    "s_waitcnt lgkmcnt(10)\n"
    "buffer_load_dwordx4 v[244:247], v1, s[12:15], s89 offen offset:2048 sc1\n"
    "v_mfma_f32_16x16x32_bf16 v[16:19], v[88:91], v[248:251], v[16:19]\n"
    "v_add_u32 v11, s83, v11\n"
    "v_min_u32 v11, 1, v11\n"
    "v_mfma_f32_16x16x32_bf16 v[20:23], v[92:95], v[248:251], v[20:23]\n"
    "v_mul_u32_u24 v11, 0x7fff, v11\n"
    "v_lshl_add_u32 v11, v11, 16, v2\n"
    "v_mfma_f32_16x16x32_bf16 v[24:27], v[96:99], v[248:251], v[24:27]\n"
    "v_mfma_f32_16x16x32_bf16 v[28:31], v[100:103], v[248:251], v[28:31]\n"
    "s_waitcnt lgkmcnt(7)\n"
    "v_mfma_f32_16x16x32_bf16 v[32:35], v[104:107], v[248:251], v[32:35]\n"
    "v_mfma_f32_16x16x32_bf16 v[36:39], v[108:111], v[248:251], v[36:39]\n"
    "v_mfma_f32_16x16x32_bf16 v[40:43], v[48:51], v[248:251], v[40:43]\n"
    "v_mfma_f32_16x16x32_bf16 v[44:47], v[248:251], v[248:251], v[44:47]\n"
    "s_waitcnt lgkmcnt(3)\n"
    "buffer_load_dwordx4 v[248:251], v1, s[12:15], s89 offen offset:3072 sc1\n"
    "v_mfma_f32_16x16x32_bf16 v[16:19], v[52:55], v[252:255], v[16:19]\n"
    "v_mfma_f32_16x16x32_bf16 v[20:23], v[56:59], v[252:255], v[20:23]\n"
    "v_mfma_f32_16x16x32_bf16 v[24:27], v[60:63], v[252:255], v[24:27]\n"
    "v_mfma_f32_16x16x32_bf16 v[28:31], v[64:67], v[252:255], v[28:31]\n"
    "s_waitcnt lgkmcnt(0)\n"
    "v_mfma_f32_16x16x32_bf16 v[32:35], v[68:71], v[252:255], v[32:35]\n"
    "v_mfma_f32_16x16x32_bf16 v[36:39], v[72:75], v[252:255], v[36:39]\n"
    "v_mfma_f32_16x16x32_bf16 v[40:43], v[76:79], v[252:255], v[40:43]\n"
    "v_mfma_f32_16x16x32_bf16 v[44:47], v[252:255], v[252:255], v[44:47]\n"
    "buffer_load_dwordx4 v[252:255], v124, s[12:15], s89 offen offset:0 sc1\n"
    "s_waitcnt vmcnt(9)\n"
    "v_cmp_ne_u32 vcc, 16, v117\n"
    "s_cbranch_vccnz Lslow_33_%=\n"
    "Lback_34_%=:\n"
    "s_waitcnt vmcnt(8)\n"
    "v_cmp_ne_u64 vcc, s[62:63], v[118:119]\n"
    "s_cbranch_vccnz Lslow_37_%=\n"
    "Lback_38_%=:\n"
    "s_add_u32 s92, s57, 0\n"
    "s_mul_i32 s92, s92, 57344\n"
    "s_add_u32 s92, s92, s49\n"
    "v_add_u32 v14, s92, v1\n"
    "s_add_u32 s92, s57, 1\n"
    "s_mul_i32 s92, s92, 57344\n"
    "s_add_u32 s92, s92, s49\n"
    "v_add_u32 v15, s92, v1\n"
    "ds_read_b128 v[208:211], v112 offset:0\n"
    "ds_read_b128 v[212:215], v112 offset:16\n"
    "s_add_u32 s90, s56, 2\n"
    "s_mul_i32 s90, s90, 57344\n"
    "buffer_load_dwordx4 v[128:131], v14, s[12:15], s64 offen sc1\n"
    "s_add_u32 s90, s90, s49\n"
    "s_mul_i32 s91, s5, 3072\n"
    "s_add_u32 s91, s91, s90\n"
    "v_cndmask_b32 v44, v44, v45, s[32:33]\n"
    "v_cndmask_b32 v44, v44, v46, s[34:35]\n"
    "v_cndmask_b32 v44, v44, v47, s[36:37]\n"
    "ds_bpermute_b32 v46, v114, v40\n"
    "ds_bpermute_b32 v45, v113, v44\n"
    "buffer_load_dwordx4 v[132:135], v14, s[12:15], s65 offen sc1\n"
    "s_waitcnt lgkmcnt(1)\n"
    "v_mul_f32 v46, s39, v46\n"
    "v_mul_f32 v47, v46, v46\n"
    "s_waitcnt lgkmcnt(0)\n"
    "v_fma_f32 v44, v45, s39, -v47\n"
    "v_max_f32 v44, 0, v44\n"
    "v_add_f32 v44, s41, v44\n"
    "v_rsq_f32 v44, v44\n"
    "s_nop 0\n"
    "v_mul_f32 v44, s40, v44\n"
    "buffer_load_dwordx4 v[136:139], v14, s[12:15], s66 offen sc1\n"
    "v_mul_f32 v45, -v46, v44\n"
    "ds_read_b128 v[216:219], v112 offset:7168\n"
    "ds_read_b128 v[220:223], v112 offset:7184\n"
    "v_pk_fma_f32 v[212:213], v[208:209], v[44:45], v[212:213] op_sel:[0,1,0] op_sel_hi:[1,1,1]\n"
    "v_pk_fma_f32 v[214:215], v[210:211], v[44:45], v[214:215] op_sel:[0,1,0] op_sel_hi:[1,1,1]\n"
    "v_pk_fma_f32 v[16:17], v[16:17], v[44:45], v[212:213] op_sel_hi:[1,0,1]\n"
    "v_pk_fma_f32 v[18:19], v[18:19], v[44:45], v[214:215] op_sel_hi:[1,0,1]\n"
    "v_exp_f32 v208, v16\n"
    "buffer_load_dwordx4 v[140:143], v14, s[12:15], s67 offen sc1\n"
    "v_exp_f32 v209, v17\n"
    "v_exp_f32 v210, v18\n"
    "v_exp_f32 v211, v19\n"
    "v_pk_fma_f32 v[208:209], v[208:209], s[44:45], s[44:45]\n"
    "v_pk_fma_f32 v[210:211], v[210:211], s[44:45], s[44:45]\n"
    "buffer_load_dwordx4 v[144:147], v14, s[12:15], s68 offen sc1\n"
    "v_pk_fma_f32 v[208:209], v[208:209], v[208:209], s[46:47]\n"
    "v_pk_fma_f32 v[210:211], v[210:211], v[210:211], s[46:47]\n"
    "v_rcp_f32 v208, v208\n"
    "v_rcp_f32 v209, v209\n"
    "v_rcp_f32 v210, v210\n"
    "buffer_load_dwordx4 v[148:151], v14, s[12:15], s69 offen sc1\n"
    "v_rcp_f32 v211, v211\n"
    "v_pk_fma_f32 v[16:17], v[16:17], v[208:209], v[16:17] neg_lo:[1,0,0] neg_hi:[1,0,0]\n"
    "v_pk_fma_f32 v[18:19], v[18:19], v[210:211], v[18:19] neg_lo:[1,0,0] neg_hi:[1,0,0]\n"
    "v_cvt_pk_bf16_f32 v224, v16, v17\n"
    "v_cvt_pk_bf16_f32 v225, v18, v19\n"
    "ds_read_b128 v[208:211], v112 offset:14336\n"
    "ds_read_b128 v[212:215], v112 offset:14352\n"
    "buffer_load_dwordx4 v[152:155], v14, s[12:15], s70 offen sc1\n"
    "s_waitcnt lgkmcnt(2)\n"
    "v_pk_fma_f32 v[220:221], v[216:217], v[44:45], v[220:221] op_sel:[0,1,0] op_sel_hi:[1,1,1]\n"
    "v_pk_fma_f32 v[222:223], v[218:219], v[44:45], v[222:223] op_sel:[0,1,0] op_sel_hi:[1,1,1]\n"
    "v_pk_fma_f32 v[20:21], v[20:21], v[44:45], v[220:221] op_sel_hi:[1,0,1]\n"
    "v_pk_fma_f32 v[22:23], v[22:23], v[44:45], v[222:223] op_sel_hi:[1,0,1]\n"
    "v_exp_f32 v216, v20\n"
    "v_exp_f32 v217, v21\n"
    "buffer_load_dwordx4 v[156:159], v14, s[12:15], s71 offen sc1\n"
    "v_exp_f32 v218, v22\n"
    "v_exp_f32 v219, v23\n"
    "v_pk_fma_f32 v[216:217], v[216:217], s[44:45], s[44:45]\n"
    "v_pk_fma_f32 v[218:219], v[218:219], s[44:45], s[44:45]\n"
    "v_pk_fma_f32 v[216:217], v[216:217], v[216:217], s[46:47]\n"
    "v_pk_fma_f32 v[218:219], v[218:219], v[218:219], s[46:47]\n"
    "buffer_load_dwordx4 v[160:163], v14, s[12:15], s72 offen sc1\n"
    "v_rcp_f32 v216, v216\n"
    "v_rcp_f32 v217, v217\n"
    "v_rcp_f32 v218, v218\n"
    "v_rcp_f32 v219, v219\n"
    "buffer_load_dwordx4 v[164:167], v14, s[12:15], s73 offen sc1\n"
    "v_pk_fma_f32 v[20:21], v[20:21], v[216:217], v[20:21] neg_lo:[1,0,0] neg_hi:[1,0,0]\n"
    "v_pk_fma_f32 v[22:23], v[22:23], v[218:219], v[22:23] neg_lo:[1,0,0] neg_hi:[1,0,0]\n"
    "v_cvt_pk_bf16_f32 v226, v20, v21\n"
    "v_cvt_pk_bf16_f32 v227, v22, v23\n"
    "buffer_store_dwordx4 v[224:227], v1, s[12:15], s91 offen offset:0 sc1\n"
    "ds_read_b128 v[216:219], v112 offset:21504\n"
    "ds_read_b128 v[220:223], v112 offset:21520\n"
    "s_waitcnt lgkmcnt(2)\n"
    "v_pk_fma_f32 v[212:213], v[208:209], v[44:45], v[212:213] op_sel:[0,1,0] op_sel_hi:[1,1,1]\n"
    "v_pk_fma_f32 v[214:215], v[210:211], v[44:45], v[214:215] op_sel:[0,1,0] op_sel_hi:[1,1,1]\n"
    "v_pk_fma_f32 v[24:25], v[24:25], v[44:45], v[212:213] op_sel_hi:[1,0,1]\n"
    "v_pk_fma_f32 v[26:27], v[26:27], v[44:45], v[214:215] op_sel_hi:[1,0,1]\n"
    "v_exp_f32 v208, v24\n"
    "v_exp_f32 v209, v25\n"
    "v_exp_f32 v210, v26\n"
    "v_exp_f32 v211, v27\n"
    "v_pk_fma_f32 v[208:209], v[208:209], s[44:45], s[44:45]\n"
    "v_pk_fma_f32 v[210:211], v[210:211], s[44:45], s[44:45]\n"
    "v_pk_fma_f32 v[208:209], v[208:209], v[208:209], s[46:47]\n"
    "v_pk_fma_f32 v[210:211], v[210:211], v[210:211], s[46:47]\n"
    "v_rcp_f32 v208, v208\n"
    "v_rcp_f32 v209, v209\n"
    "v_rcp_f32 v210, v210\n"
    "v_rcp_f32 v211, v211\n"
    "v_pk_fma_f32 v[24:25], v[24:25], v[208:209], v[24:25] neg_lo:[1,0,0] neg_hi:[1,0,0]\n"
    "v_pk_fma_f32 v[26:27], v[26:27], v[210:211], v[26:27] neg_lo:[1,0,0] neg_hi:[1,0,0]\n"
    "v_cvt_pk_bf16_f32 v228, v24, v25\n"
    "v_cvt_pk_bf16_f32 v229, v26, v27\n"
    "ds_read_b128 v[208:211], v112 offset:28672\n"
    "ds_read_b128 v[212:215], v112 offset:28688\n"
    "s_waitcnt lgkmcnt(2)\n"
    "v_pk_fma_f32 v[220:221], v[216:217], v[44:45], v[220:221] op_sel:[0,1,0] op_sel_hi:[1,1,1]\n"
    "v_pk_fma_f32 v[222:223], v[218:219], v[44:45], v[222:223] op_sel:[0,1,0] op_sel_hi:[1,1,1]\n"
    "v_pk_fma_f32 v[28:29], v[28:29], v[44:45], v[220:221] op_sel_hi:[1,0,1]\n"
    "v_pk_fma_f32 v[30:31], v[30:31], v[44:45], v[222:223] op_sel_hi:[1,0,1]\n"
    "v_exp_f32 v216, v28\n"
    "v_exp_f32 v217, v29\n"
    "v_exp_f32 v218, v30\n"
    "v_exp_f32 v219, v31\n"
    "v_pk_fma_f32 v[216:217], v[216:217], s[44:45], s[44:45]\n"
    "v_pk_fma_f32 v[218:219], v[218:219], s[44:45], s[44:45]\n"
    "v_pk_fma_f32 v[216:217], v[216:217], v[216:217], s[46:47]\n"
    "v_pk_fma_f32 v[218:219], v[218:219], v[218:219], s[46:47]\n"
    "v_rcp_f32 v216, v216\n"
    "v_rcp_f32 v217, v217\n"
    "v_rcp_f32 v218, v218\n"
    "v_rcp_f32 v219, v219\n"
    "v_pk_fma_f32 v[28:29], v[28:29], v[216:217], v[28:29] neg_lo:[1,0,0] neg_hi:[1,0,0]\n"
    "v_pk_fma_f32 v[30:31], v[30:31], v[218:219], v[30:31] neg_lo:[1,0,0] neg_hi:[1,0,0]\n"
    "v_cvt_pk_bf16_f32 v230, v28, v29\n"
    "v_cvt_pk_bf16_f32 v231, v30, v31\n"
    "buffer_store_dwordx4 v[228:231], v1, s[12:15], s91 offen offset:1024 sc1\n"
    "ds_read_b128 v[216:219], v112 offset:35840\n"
    "ds_read_b128 v[220:223], v112 offset:35856\n"
    "s_waitcnt lgkmcnt(2)\n"
    "v_pk_fma_f32 v[212:213], v[208:209], v[44:45], v[212:213] op_sel:[0,1,0] op_sel_hi:[1,1,1]\n"
    "v_pk_fma_f32 v[214:215], v[210:211], v[44:45], v[214:215] op_sel:[0,1,0] op_sel_hi:[1,1,1]\n"
    "v_pk_fma_f32 v[32:33], v[32:33], v[44:45], v[212:213] op_sel_hi:[1,0,1]\n"
    "v_pk_fma_f32 v[34:35], v[34:35], v[44:45], v[214:215] op_sel_hi:[1,0,1]\n"
    "v_exp_f32 v208, v32\n"
    "v_exp_f32 v209, v33\n"
    "v_exp_f32 v210, v34\n"
    "v_exp_f32 v211, v35\n"
    "v_pk_fma_f32 v[208:209], v[208:209], s[44:45], s[44:45]\n"
    "v_pk_fma_f32 v[210:211], v[210:211], s[44:45], s[44:45]\n"
    "v_pk_fma_f32 v[208:209], v[208:209], v[208:209], s[46:47]\n"
    "v_pk_fma_f32 v[210:211], v[210:211], v[210:211], s[46:47]\n"
    "v_rcp_f32 v208, v208\n"
    "v_rcp_f32 v209, v209\n"
    "v_rcp_f32 v210, v210\n"
    "v_rcp_f32 v211, v211\n"
    "v_pk_fma_f32 v[32:33], v[32:33], v[208:209], v[32:33] neg_lo:[1,0,0] neg_hi:[1,0,0]\n"
    "v_pk_fma_f32 v[34:35], v[34:35], v[210:211], v[34:35] neg_lo:[1,0,0] neg_hi:[1,0,0]\n"
    "v_cvt_pk_bf16_f32 v224, v32, v33\n"
    "v_cvt_pk_bf16_f32 v225, v34, v35\n"
    "ds_read_b128 v[208:211], v112 offset:43008\n"
    "ds_read_b128 v[212:215], v112 offset:43024\n"
    "s_waitcnt lgkmcnt(2)\n"
    "v_pk_fma_f32 v[220:221], v[216:217], v[44:45], v[220:221] op_sel:[0,1,0] op_sel_hi:[1,1,1]\n"
    "v_pk_fma_f32 v[222:223], v[218:219], v[44:45], v[222:223] op_sel:[0,1,0] op_sel_hi:[1,1,1]\n"
    "v_pk_fma_f32 v[36:37], v[36:37], v[44:45], v[220:221] op_sel_hi:[1,0,1]\n"
    "v_pk_fma_f32 v[38:39], v[38:39], v[44:45], v[222:223] op_sel_hi:[1,0,1]\n"
    "v_exp_f32 v216, v36\n"
    "v_exp_f32 v217, v37\n"
    "v_exp_f32 v218, v38\n"
    "v_exp_f32 v219, v39\n"
    "v_pk_fma_f32 v[216:217], v[216:217], s[44:45], s[44:45]\n"
    "v_pk_fma_f32 v[218:219], v[218:219], s[44:45], s[44:45]\n"
    "v_pk_fma_f32 v[216:217], v[216:217], v[216:217], s[46:47]\n"
    "v_pk_fma_f32 v[218:219], v[218:219], v[218:219], s[46:47]\n"
    "v_rcp_f32 v216, v216\n"
    "v_rcp_f32 v217, v217\n"
    "v_rcp_f32 v218, v218\n"
    "v_rcp_f32 v219, v219\n"
    "v_pk_fma_f32 v[36:37], v[36:37], v[216:217], v[36:37] neg_lo:[1,0,0] neg_hi:[1,0,0]\n"
    "v_pk_fma_f32 v[38:39], v[38:39], v[218:219], v[38:39] neg_lo:[1,0,0] neg_hi:[1,0,0]\n"
    "v_cvt_pk_bf16_f32 v226, v36, v37\n"
    "v_cvt_pk_bf16_f32 v227, v38, v39\n"
    "buffer_store_dwordx4 v[224:227], v1, s[12:15], s91 offen offset:2048 sc1\n"
    "s_waitcnt lgkmcnt(0)\n"
    "v_pk_fma_f32 v[212:213], v[208:209], v[44:45], v[212:213] op_sel:[0,1,0] op_sel_hi:[1,1,1]\n"
    "v_pk_fma_f32 v[214:215], v[210:211], v[44:45], v[214:215] op_sel:[0,1,0] op_sel_hi:[1,1,1]\n"
    "v_pk_fma_f32 v[40:41], v[40:41], v[44:45], v[212:213] op_sel_hi:[1,0,1]\n"
    "v_pk_fma_f32 v[42:43], v[42:43], v[44:45], v[214:215] op_sel_hi:[1,0,1]\n"
    "v_exp_f32 v208, v40\n"
    "v_exp_f32 v209, v41\n"
    "v_exp_f32 v210, v42\n"
    "v_exp_f32 v211, v43\n"
    "v_pk_fma_f32 v[208:209], v[208:209], s[44:45], s[44:45]\n"
    "v_pk_fma_f32 v[210:211], v[210:211], s[44:45], s[44:45]\n"
    "v_pk_fma_f32 v[208:209], v[208:209], v[208:209], s[46:47]\n"
    "v_pk_fma_f32 v[210:211], v[210:211], v[210:211], s[46:47]\n"
    "v_rcp_f32 v208, v208\n"
    "v_rcp_f32 v209, v209\n"
    "v_rcp_f32 v210, v210\n"
    "v_rcp_f32 v211, v211\n"
    "v_pk_fma_f32 v[40:41], v[40:41], v[208:209], v[40:41] neg_lo:[1,0,0] neg_hi:[1,0,0]\n"
    "v_pk_fma_f32 v[42:43], v[42:43], v[210:211], v[42:43] neg_lo:[1,0,0] neg_hi:[1,0,0]\n"
    "v_cvt_pk_bf16_f32 v228, v40, v41\n"
    "v_cvt_pk_bf16_f32 v229, v42, v43\n"
    "v_cndmask_b32 v228, v228, 0, s[100:101]\n"
    "v_cndmask_b32 v229, v229, 0, s[100:101]\n"
    "v_mov_b32 v230, v228\n"
    "v_mov_b32 v231, v229\n"
    "s_nop 0\n"
    "v_permlane16_swap_b32 v228, v230\n"
    "v_permlane16_swap_b32 v229, v231\n"
    "buffer_store_dwordx4 v[228:231], v115, s[12:15], s90 offen sc1\n"
    "v_mov_b32 v6, v125\n"
    "buffer_load_dwordx4 v[168:171], v14, s[12:15], s74 offen sc1\n"
    "buffer_load_dwordx4 v[172:175], v14, s[12:15], s75 offen sc1\n"
    "buffer_load_dwordx4 v[176:179], v14, s[12:15], s76 offen sc1\n"
    "buffer_load_dwordx4 v[180:183], v14, s[12:15], s77 offen sc1\n"
    "s_waitcnt vmcnt(17)\n"
    "v_mfma_f32_16x16x32_bf16 v[16:19], a[0:3], v[128:131], 0\n"
    "s_add_u32 s88, s53, 12144640\n"
    "s_lshl_b32 s89, s18, 1\n"
    "v_mfma_f32_16x16x32_bf16 v[20:23], a[4:7], v[128:131], 0\n"
    "s_add_u32 s88, s88, s89\n"
    "buffer_load_dwordx4 v[124:127], v13, s[12:15], s88 offen sc1\n"
    "v_mfma_f32_16x16x32_bf16 v[24:27], a[8:11], v[128:131], 0\n"
    "v_mfma_f32_16x16x32_bf16 v[28:31], a[12:15], v[128:131], 0\n"
    "buffer_load_dwordx4 v[224:227], v13, s[12:15], s88 offen offset:1024 sc1\n"
    "s_add_u32 s89, s53, 14766080\n"
    "v_mfma_f32_16x16x32_bf16 v[32:35], a[16:19], v[128:131], 0\n"
    "buffer_load_dwordx4 v[184:187], v15, s[12:15], s64 offen sc1\n"
    "s_lshl_b32 s88, s19, 10\n"
    "s_waitcnt vmcnt(19)\n"
    "v_mfma_f32_16x16x32_bf16 v[16:19], a[20:23], v[132:135], v[16:19]\n"
    "s_add_u32 s88, s88, s89\n"
    "v_mfma_f32_16x16x32_bf16 v[20:23], a[24:27], v[132:135], v[20:23]\n"
    "buffer_load_dwordx4 v[72:75], v1, s[12:15], s88 offen sc1\n"
    "s_lshl_b32 s88, s20, 10\n"
    "v_mfma_f32_16x16x32_bf16 v[24:27], a[28:31], v[132:135], v[24:27]\n"
    "s_add_u32 s88, s88, s89\n"
    "v_mfma_f32_16x16x32_bf16 v[28:31], a[32:35], v[132:135], v[28:31]\n"
    "buffer_load_dwordx4 v[76:79], v1, s[12:15], s88 offen sc1\n"
    "v_mfma_f32_16x16x32_bf16 v[32:35], a[36:39], v[132:135], v[32:35]\n"
    "buffer_load_dwordx4 v[188:191], v15, s[12:15], s65 offen sc1\n"
    "s_waitcnt vmcnt(21)\n"
    "v_mfma_f32_16x16x32_bf16 v[16:19], a[40:43], v[136:139], v[16:19]\n"
    "v_mfma_f32_16x16x32_bf16 v[20:23], a[44:47], v[136:139], v[20:23]\n"
    "v_mfma_f32_16x16x32_bf16 v[24:27], a[48:51], v[136:139], v[24:27]\n"
    "v_mfma_f32_16x16x32_bf16 v[28:31], a[52:55], v[136:139], v[28:31]\n"
    "v_mfma_f32_16x16x32_bf16 v[32:35], a[56:59], v[136:139], v[32:35]\n"
    "buffer_load_dwordx4 v[192:195], v15, s[12:15], s66 offen sc1\n"
    "s_waitcnt vmcnt(21)\n"
    "v_mfma_f32_16x16x32_bf16 v[16:19], a[60:63], v[140:143], v[16:19]\n"
    "v_mfma_f32_16x16x32_bf16 v[20:23], a[64:67], v[140:143], v[20:23]\n"
    "v_mfma_f32_16x16x32_bf16 v[24:27], a[68:71], v[140:143], v[24:27]\n"
    "v_mfma_f32_16x16x32_bf16 v[28:31], a[72:75], v[140:143], v[28:31]\n"
    "v_mfma_f32_16x16x32_bf16 v[32:35], a[76:79], v[140:143], v[32:35]\n"
    "buffer_load_dwordx4 v[196:199], v15, s[12:15], s67 offen sc1\n"
    "s_waitcnt vmcnt(21)\n"
    "v_mfma_f32_16x16x32_bf16 v[16:19], a[80:83], v[144:147], v[16:19]\n"
    "v_mfma_f32_16x16x32_bf16 v[20:23], a[84:87], v[144:147], v[20:23]\n"
    "v_mfma_f32_16x16x32_bf16 v[24:27], a[88:91], v[144:147], v[24:27]\n"
    "v_mfma_f32_16x16x32_bf16 v[28:31], a[92:95], v[144:147], v[28:31]\n"
    "v_mfma_f32_16x16x32_bf16 v[32:35], a[96:99], v[144:147], v[32:35]\n"
    "buffer_load_dwordx4 v[200:203], v15, s[12:15], s68 offen sc1\n"
    "s_waitcnt vmcnt(21)\n"
    "v_mfma_f32_16x16x32_bf16 v[16:19], a[100:103], v[148:151], v[16:19]\n"
    "v_mfma_f32_16x16x32_bf16 v[20:23], a[104:107], v[148:151], v[20:23]\n"
    "v_mfma_f32_16x16x32_bf16 v[24:27], a[108:111], v[148:151], v[24:27]\n"
    "v_mfma_f32_16x16x32_bf16 v[28:31], a[112:115], v[148:151], v[28:31]\n"
    "v_mfma_f32_16x16x32_bf16 v[32:35], a[116:119], v[148:151], v[32:35]\n"
    "buffer_load_dwordx4 v[204:207], v15, s[12:15], s69 offen sc1\n"
    "s_waitcnt vmcnt(21)\n"
    "v_mfma_f32_16x16x32_bf16 v[16:19], a[120:123], v[152:155], v[16:19]\n"
    "v_mfma_f32_16x16x32_bf16 v[20:23], a[124:127], v[152:155], v[20:23]\n"
    "v_mfma_f32_16x16x32_bf16 v[24:27], a[128:131], v[152:155], v[24:27]\n"
    "v_mfma_f32_16x16x32_bf16 v[28:31], a[132:135], v[152:155], v[28:31]\n"
    "v_mfma_f32_16x16x32_bf16 v[32:35], a[136:139], v[152:155], v[32:35]\n"
    "buffer_load_dwordx4 v[128:131], v15, s[12:15], s70 offen sc1\n"
    "s_waitcnt vmcnt(21)\n"
    "v_mfma_f32_16x16x32_bf16 v[16:19], a[140:143], v[156:159], v[16:19]\n"
    "s_waitcnt vmcnt(15)\n"
    "s_add_u32 s51, s50, s52\n"
    "v_mfma_f32_16x16x32_bf16 v[20:23], a[144:147], v[156:159], v[20:23]\n"
    "s_mov_b64 exec, 1\n"
    "buffer_atomic_add v123, v1, s[12:15], s51 offen offset:768\n"
    "s_mov_b64 exec, -1\n"
    "v_mfma_f32_16x16x32_bf16 v[24:27], a[148:151], v[156:159], v[24:27]\n"
    "v_mfma_f32_16x16x32_bf16 v[28:31], a[152:155], v[156:159], v[28:31]\n"
    "v_mfma_f32_16x16x32_bf16 v[32:35], a[156:159], v[156:159], v[32:35]\n"
    "buffer_load_dwordx4 v[132:135], v15, s[12:15], s71 offen sc1\n"
    "v_mfma_f32_16x16x32_bf16 v[16:19], a[160:163], v[160:163], v[16:19]\n"
    "v_mfma_f32_16x16x32_bf16 v[20:23], a[164:167], v[160:163], v[20:23]\n"
    "v_mfma_f32_16x16x32_bf16 v[24:27], a[168:171], v[160:163], v[24:27]\n"
    "v_mfma_f32_16x16x32_bf16 v[28:31], a[172:175], v[160:163], v[28:31]\n"
    "v_mfma_f32_16x16x32_bf16 v[32:35], a[176:179], v[160:163], v[32:35]\n"
    "buffer_load_dwordx4 v[136:139], v15, s[12:15], s72 offen sc1\n"
    "v_mfma_f32_16x16x32_bf16 v[16:19], a[180:183], v[164:167], v[16:19]\n"
    "v_mfma_f32_16x16x32_bf16 v[20:23], a[184:187], v[164:167], v[20:23]\n"
    "v_mfma_f32_16x16x32_bf16 v[24:27], a[188:191], v[164:167], v[24:27]\n"
    "v_mfma_f32_16x16x32_bf16 v[28:31], a[192:195], v[164:167], v[28:31]\n"
    "v_mfma_f32_16x16x32_bf16 v[32:35], a[196:199], v[164:167], v[32:35]\n"
    "buffer_load_dwordx4 v[140:143], v15, s[12:15], s73 offen sc1\n"
    "s_waitcnt vmcnt(18)\n"
    "v_mfma_f32_16x16x32_bf16 v[16:19], a[200:203], v[168:171], v[16:19]\n"
    "v_mfma_f32_16x16x32_bf16 v[20:23], a[204:207], v[168:171], v[20:23]\n"
    "v_mfma_f32_16x16x32_bf16 v[24:27], a[208:211], v[168:171], v[24:27]\n"
    "v_mfma_f32_16x16x32_bf16 v[28:31], a[212:215], v[168:171], v[28:31]\n"
    "v_mfma_f32_16x16x32_bf16 v[32:35], a[216:219], v[168:171], v[32:35]\n"
    "buffer_load_dwordx4 v[144:147], v15, s[12:15], s74 offen sc1\n"
    "s_waitcnt vmcnt(18)\n"
    "v_mfma_f32_16x16x32_bf16 v[16:19], a[220:223], v[172:175], v[16:19]\n"
    "v_mfma_f32_16x16x32_bf16 v[20:23], a[224:227], v[172:175], v[20:23]\n"
    "v_mfma_f32_16x16x32_bf16 v[24:27], a[228:231], v[172:175], v[24:27]\n"
    "v_mfma_f32_16x16x32_bf16 v[28:31], a[232:235], v[172:175], v[28:31]\n"
    "v_mfma_f32_16x16x32_bf16 v[32:35], a[236:239], v[172:175], v[32:35]\n"
    "buffer_load_dwordx4 v[148:151], v15, s[12:15], s75 offen sc1\n"
    "s_waitcnt vmcnt(18)\n"
    "v_mfma_f32_16x16x32_bf16 v[16:19], a[240:243], v[176:179], v[16:19]\n"
    "v_mfma_f32_16x16x32_bf16 v[20:23], a[244:247], v[176:179], v[20:23]\n"
    "v_mfma_f32_16x16x32_bf16 v[24:27], a[248:251], v[176:179], v[24:27]\n"
    "v_mfma_f32_16x16x32_bf16 v[28:31], a[252:255], v[176:179], v[28:31]\n"
    "s_waitcnt vmcnt(16)\n"
    "v_lshlrev_b32 v56, 16, v124\n"
    "v_and_b32 v57, 0xffff0000, v124\n"
    "v_mfma_f32_16x16x32_bf16 v[32:35], v[232:235], v[176:179], v[32:35]\n"
    "buffer_load_dwordx4 v[152:155], v15, s[12:15], s76 offen sc1\n"
    "v_lshlrev_b32 v58, 16, v125\n"
    "v_mfma_f32_16x16x32_bf16 v[16:19], v[236:239], v[180:183], v[16:19]\n"
    "v_and_b32 v59, 0xffff0000, v125\n"
    "s_waitcnt vmcnt(16)\n"
    "v_lshlrev_b32 v60, 16, v224\n"
    "v_mfma_f32_16x16x32_bf16 v[20:23], v[240:243], v[180:183], v[20:23]\n"
    "v_and_b32 v61, 0xffff0000, v224\n"
    "v_lshlrev_b32 v62, 16, v225\n"
    "v_mfma_f32_16x16x32_bf16 v[24:27], v[244:247], v[180:183], v[24:27]\n"
    "v_and_b32 v63, 0xffff0000, v225\n"
    "v_lshlrev_b32 v208, 16, v126\n"
    "v_mfma_f32_16x16x32_bf16 v[28:31], v[248:251], v[180:183], v[28:31]\n"
    "v_and_b32 v209, 0xffff0000, v126\n"
    "v_lshlrev_b32 v210, 16, v127\n"
    "v_mfma_f32_16x16x32_bf16 v[32:35], v[252:255], v[180:183], v[32:35]\n"
    "buffer_load_dwordx4 v[156:159], v15, s[12:15], s77 offen sc1\n"
    "v_and_b32 v211, 0xffff0000, v127\n"
    "v_lshlrev_b32 v212, 16, v226\n"
    "v_and_b32 v213, 0xffff0000, v226\n"
    "s_waitcnt vmcnt(16)\n"
    "v_mfma_f32_16x16x32_bf16 v[36:39], a[0:3], v[184:187], 0\n"
    "v_lshlrev_b32 v214, 16, v227\n"
    "v_and_b32 v215, 0xffff0000, v227\n"
    "s_add_u32 s88, s53, 12185600\n"
    "s_lshl_b32 s89, s18, 1\n"
    "v_mfma_f32_16x16x32_bf16 v[40:43], a[4:7], v[184:187], 0\n"
    "ds_write_b128 v8, v[16:19] offset:0\n"
    "s_add_u32 s88, s88, s89\n"
    "buffer_load_dwordx4 v[124:127], v13, s[12:15], s88 offen sc1\n"
    "v_mfma_f32_16x16x32_bf16 v[44:47], a[8:11], v[184:187], 0\n"
    "v_mfma_f32_16x16x32_bf16 v[48:51], a[12:15], v[184:187], 0\n"
    "ds_write_b128 v8, v[20:23] offset:4096\n"
    "buffer_load_dwordx4 v[224:227], v13, s[12:15], s88 offen offset:1024 sc1\n"
    "v_mfma_f32_16x16x32_bf16 v[52:55], a[16:19], v[184:187], 0\n"
    "s_waitcnt vmcnt(15)\n"
    "v_mfma_f32_16x16x32_bf16 v[36:39], a[20:23], v[188:191], v[36:39]\n"
    "ds_write_b128 v8, v[24:27] offset:8192\n"
    "v_mfma_f32_16x16x32_bf16 v[40:43], a[24:27], v[188:191], v[40:43]\n"
    "v_mfma_f32_16x16x32_bf16 v[44:47], a[28:31], v[188:191], v[44:47]\n"
    "ds_write_b128 v8, v[28:31] offset:12288\n"
    "v_mfma_f32_16x16x32_bf16 v[48:51], a[32:35], v[188:191], v[48:51]\n"
    "v_mfma_f32_16x16x32_bf16 v[52:55], a[36:39], v[188:191], v[52:55]\n"
    "ds_write_b128 v8, v[32:35] offset:16384\n"
    "s_waitcnt vmcnt(14)\n"
    "v_mfma_f32_16x16x32_bf16 v[36:39], a[40:43], v[192:195], v[36:39]\n"
    "v_mfma_f32_16x16x32_bf16 v[40:43], a[44:47], v[192:195], v[40:43]\n"
    "v_mfma_f32_16x16x32_bf16 v[44:47], a[48:51], v[192:195], v[44:47]\n"
    "v_mfma_f32_16x16x32_bf16 v[48:51], a[52:55], v[192:195], v[48:51]\n"
    "v_mfma_f32_16x16x32_bf16 v[52:55], a[56:59], v[192:195], v[52:55]\n"
    "s_waitcnt vmcnt(13)\n"
    "v_mfma_f32_16x16x32_bf16 v[36:39], a[60:63], v[196:199], v[36:39]\n"
    "v_mfma_f32_16x16x32_bf16 v[40:43], a[64:67], v[196:199], v[40:43]\n"
    "v_mfma_f32_16x16x32_bf16 v[44:47], a[68:71], v[196:199], v[44:47]\n"
    "v_mfma_f32_16x16x32_bf16 v[48:51], a[72:75], v[196:199], v[48:51]\n"
    "s_waitcnt lgkmcnt(0)\n"
    "s_barrier\n"
    "ds_read_b128 v[80:83], v9 offset:0\n"
    "ds_read_b128 v[84:87], v9 offset:1024\n"
    "v_mfma_f32_16x16x32_bf16 v[52:55], a[76:79], v[196:199], v[52:55]\n"
    "ds_read_b128 v[88:91], v9 offset:2048\n"
    "ds_read_b128 v[92:95], v9 offset:3072\n"
    "ds_read_b128 v[96:99], v10 offset:0\n"
    "ds_read_b128 v[100:103], v10 offset:1024\n"
    "s_waitcnt vmcnt(12)\n"
    "v_mfma_f32_16x16x32_bf16 v[36:39], a[80:83], v[200:203], v[36:39]\n"
    "ds_read_b128 v[104:107], v10 offset:2048\n"
    "ds_read_b128 v[108:111], v10 offset:3072\n"
    "v_mfma_f32_16x16x32_bf16 v[40:43], a[84:87], v[200:203], v[40:43]\n"
    "v_mfma_f32_16x16x32_bf16 v[44:47], a[88:91], v[200:203], v[44:47]\n"
    "v_mfma_f32_16x16x32_bf16 v[48:51], a[92:95], v[200:203], v[48:51]\n"
    "v_mfma_f32_16x16x32_bf16 v[52:55], a[96:99], v[200:203], v[52:55]\n"
    "s_waitcnt vmcnt(11)\n"
    "v_mfma_f32_16x16x32_bf16 v[36:39], a[100:103], v[204:207], v[36:39]\n"
    "v_mfma_f32_16x16x32_bf16 v[40:43], a[104:107], v[204:207], v[40:43]\n"
    "v_mfma_f32_16x16x32_bf16 v[44:47], a[108:111], v[204:207], v[44:47]\n"
    "v_mfma_f32_16x16x32_bf16 v[48:51], a[112:115], v[204:207], v[48:51]\n"
    "v_mfma_f32_16x16x32_bf16 v[52:55], a[116:119], v[204:207], v[52:55]\n"
    "buffer_load_dword v229, v12, s[12:15], s52 offen offset:768 sc1\n"
    "s_waitcnt lgkmcnt(0)\n"
    "v_add_f32 v80, v80, v84\n"
    "v_add_f32 v81, v81, v85\n"
    "s_waitcnt vmcnt(11)\n"
    "v_mfma_f32_16x16x32_bf16 v[36:39], a[120:123], v[128:131], v[36:39]\n"
    "v_add_f32 v82, v82, v86\n"
    "v_add_f32 v83, v83, v87\n"
    "v_add_f32 v88, v88, v92\n"
    "v_add_f32 v89, v89, v93\n"
    "v_mfma_f32_16x16x32_bf16 v[40:43], a[124:127], v[128:131], v[40:43]\n"
    "v_add_f32 v90, v90, v94\n"
    "v_add_f32 v91, v91, v95\n"
    "v_add_f32 v80, v80, v88\n"
    "v_add_f32 v81, v81, v89\n"
    "v_mfma_f32_16x16x32_bf16 v[44:47], a[128:131], v[128:131], v[44:47]\n"
    "v_add_f32 v82, v82, v90\n"
    "v_add_f32 v83, v83, v91\n"
    "v_fmac_f32 v56, s42, v80\n"
    "v_fmac_f32 v57, s42, v81\n"
    "v_mfma_f32_16x16x32_bf16 v[48:51], a[132:135], v[128:131], v[48:51]\n"
    "v_fmac_f32 v58, s42, v82\n"
    "v_fmac_f32 v59, s42, v83\n"
    "v_add_f32 v96, v96, v100\n"
    "v_add_f32 v97, v97, v101\n"
    "v_mfma_f32_16x16x32_bf16 v[52:55], a[136:139], v[128:131], v[52:55]\n"
    "v_add_f32 v98, v98, v102\n"
    "v_add_f32 v99, v99, v103\n"
    "v_add_f32 v104, v104, v108\n"
    "v_add_f32 v105, v105, v109\n"
    "s_waitcnt vmcnt(9)\n"
    "v_mfma_f32_16x16x32_bf16 v[36:39], a[140:143], v[132:135], v[36:39]\n"
    "v_add_f32 v106, v106, v110\n"
    "v_add_f32 v107, v107, v111\n"
    "v_add_f32 v96, v96, v104\n"
    "v_add_f32 v97, v97, v105\n"
    "v_mfma_f32_16x16x32_bf16 v[40:43], a[144:147], v[132:135], v[40:43]\n"
    "v_add_f32 v98, v98, v106\n"
    "v_add_f32 v99, v99, v107\n"
    "v_fmac_f32 v60, s42, v96\n"
    "v_fmac_f32 v61, s42, v97\n"
    "v_mfma_f32_16x16x32_bf16 v[44:47], a[148:151], v[132:135], v[44:47]\n"
    "v_fmac_f32 v62, s42, v98\n"
    "v_fmac_f32 v63, s42, v99\n"
    "v_add_f32 v80, v56, v57\n"
    "v_mul_f32 v81, v56, v56\n"
    "v_mfma_f32_16x16x32_bf16 v[48:51], a[152:155], v[132:135], v[48:51]\n"
    "v_add_f32 v82, v60, v61\n"
    "v_mul_f32 v83, v60, v60\n"
    "v_fmac_f32 v81, v57, v57\n"
    "v_fmac_f32 v83, v61, v61\n"
    "v_mfma_f32_16x16x32_bf16 v[52:55], a[156:159], v[132:135], v[52:55]\n"
    "v_add_f32 v80, v80, v58\n"
    "v_fmac_f32 v81, v58, v58\n"
    "v_add_f32 v82, v82, v62\n"
    "v_fmac_f32 v83, v62, v62\n"
    "s_waitcnt vmcnt(8)\n"
    "v_mfma_f32_16x16x32_bf16 v[36:39], a[160:163], v[136:139], v[36:39]\n"
    "v_add_f32 v80, v80, v59\n"
    "v_fmac_f32 v81, v59, v59\n"
    "v_add_f32 v82, v82, v63\n"
    "v_fmac_f32 v83, v63, v63\n"
    "v_mfma_f32_16x16x32_bf16 v[40:43], a[164:167], v[136:139], v[40:43]\n"
    "v_mul_f32 v80, s16, v80\n"
    "v_mul_f32 v81, s16, v81\n"
    "v_fmac_f32 v80, s17, v82\n"
    "v_fmac_f32 v81, s17, v83\n"
    "v_mfma_f32_16x16x32_bf16 v[44:47], a[168:171], v[136:139], v[44:47]\n"
    "v_mov_b32 v82, v80\n"
    "v_mov_b32 v83, v81\n"
    "s_nop 0\n"
    "v_permlane16_swap_b32 v80, v82\n"
    "v_permlane16_swap_b32 v81, v83\n"
    "v_mfma_f32_16x16x32_bf16 v[48:51], a[172:175], v[136:139], v[48:51]\n"
    "v_add_f32 v80, v80, v82\n"
    "v_add_f32 v81, v81, v83\n"
    "v_mov_b32 v82, v80\n"
    "v_mov_b32 v83, v81\n"
    "v_mfma_f32_16x16x32_bf16 v[52:55], a[176:179], v[136:139], v[52:55]\n"
    "v_permlane32_swap_b32 v80, v82\n"
    "v_permlane32_swap_b32 v81, v83\n"
    "v_add_f32 v80, v80, v82\n"
    "v_add_f32 v81, v81, v83\n"
    "s_waitcnt vmcnt(7)\n"
    "v_mfma_f32_16x16x32_bf16 v[36:39], a[180:183], v[140:143], v[36:39]\n"
    "ds_write_b64 v6, v[80:81]\n"
    "v_mfma_f32_16x16x32_bf16 v[40:43], a[184:187], v[140:143], v[40:43]\n"
    "v_mfma_f32_16x16x32_bf16 v[44:47], a[188:191], v[140:143], v[44:47]\n"
    "v_mfma_f32_16x16x32_bf16 v[48:51], a[192:195], v[140:143], v[48:51]\n"
    "v_mfma_f32_16x16x32_bf16 v[52:55], a[196:199], v[140:143], v[52:55]\n"
    "s_waitcnt vmcnt(6)\n"
    "v_mfma_f32_16x16x32_bf16 v[36:39], a[200:203], v[144:147], v[36:39]\n"
    "v_mfma_f32_16x16x32_bf16 v[40:43], a[204:207], v[144:147], v[40:43]\n"
    "v_mfma_f32_16x16x32_bf16 v[44:47], a[208:211], v[144:147], v[44:47]\n"
    "v_mfma_f32_16x16x32_bf16 v[48:51], a[212:215], v[144:147], v[48:51]\n"
    "v_mfma_f32_16x16x32_bf16 v[52:55], a[216:219], v[144:147], v[52:55]\n"
    "s_waitcnt lgkmcnt(0)\n"
    "s_barrier\n"
    "ds_read_b64 v[80:81], v7\n"
    "s_waitcnt vmcnt(5)\n"
    "v_mfma_f32_16x16x32_bf16 v[36:39], a[220:223], v[148:151], v[36:39]\n"
    "v_mfma_f32_16x16x32_bf16 v[40:43], a[224:227], v[148:151], v[40:43]\n"
    "v_mfma_f32_16x16x32_bf16 v[44:47], a[228:231], v[148:151], v[44:47]\n"
    "v_mfma_f32_16x16x32_bf16 v[48:51], a[232:235], v[148:151], v[48:51]\n"
    "v_mfma_f32_16x16x32_bf16 v[52:55], a[236:239], v[148:151], v[52:55]\n"
    "s_waitcnt vmcnt(4)\n"
    "v_mfma_f32_16x16x32_bf16 v[36:39], a[240:243], v[152:155], v[36:39]\n"
    "v_mfma_f32_16x16x32_bf16 v[40:43], a[244:247], v[152:155], v[40:43]\n"
    "v_mfma_f32_16x16x32_bf16 v[44:47], a[248:251], v[152:155], v[44:47]\n"
    "v_mfma_f32_16x16x32_bf16 v[48:51], a[252:255], v[152:155], v[48:51]\n"
    "s_waitcnt lgkmcnt(0)\n"
    "v_mov_b32 v82, v80\n"
    "v_mov_b32 v83, v81\n"
    "s_nop 0\n"
    "v_permlane16_swap_b32 v80, v82\n"
    "v_permlane16_swap_b32 v81, v83\n"
    "v_mfma_f32_16x16x32_bf16 v[52:55], v[232:235], v[152:155], v[52:55]\n"
    "s_waitcnt vmcnt(0)\n"
    "v_cmp_ne_u32 vcc, 16, v229\n"
    "s_cbranch_vccnz Lslow_41_%=\n"
    "Lback_42_%=:\n"
    "s_add_u32 s92, s57, 2\n"
    "s_mul_i32 s92, s92, 57344\n"
    "s_add_u32 s92, s92, s49\n"
    "v_add_u32 v14, s92, v1\n"
    "s_add_u32 s92, s57, 3\n"
    "s_mul_i32 s92, s92, 57344\n"
    "s_add_u32 s92, s92, s49\n"
    "v_add_u32 v15, s92, v1\n"
    "buffer_load_dwordx4 v[160:163], v14, s[12:15], s64 offen sc1\n"
    "v_mfma_f32_16x16x32_bf16 v[36:39], v[236:239], v[156:159], v[36:39]\n"
    "v_add_f32 v80, v80, v82\n"
    "v_add_f32 v81, v81, v83\n"
    "v_mov_b32 v82, v80\n"
    "v_mov_b32 v83, v81\n"
    "v_mfma_f32_16x16x32_bf16 v[40:43], v[240:243], v[156:159], v[40:43]\n"
    "v_permlane32_swap_b32 v80, v82\n"
    "v_permlane32_swap_b32 v81, v83\n"
    "v_add_f32 v80, v80, v82\n"
    "v_add_f32 v81, v81, v83\n"
    "v_mfma_f32_16x16x32_bf16 v[44:47], v[244:247], v[156:159], v[44:47]\n"
    "v_min_u32 v80, -2, v80\n"
    "v_min_u32 v81, -2, v81\n"
    "buffer_store_dwordx2 v[80:81], v11, s[12:15], s23 offen offset:0 sc1\n"
    "v_lshlrev_b32 v64, 16, v124\n"
    "v_mfma_f32_16x16x32_bf16 v[48:51], v[248:251], v[156:159], v[48:51]\n"
    "v_and_b32 v65, 0xffff0000, v124\n"
    "v_lshlrev_b32 v66, 16, v125\n"
    "v_and_b32 v67, 0xffff0000, v125\n"
    "v_lshlrev_b32 v68, 16, v224\n"
    "v_mfma_f32_16x16x32_bf16 v[52:55], v[252:255], v[156:159], v[52:55]\n"
    "buffer_load_dwordx4 v[164:167], v14, s[12:15], s65 offen sc1\n"
    "buffer_load_dwordx4 v[168:171], v14, s[12:15], s66 offen sc1\n"
    "v_and_b32 v69, 0xffff0000, v224\n"
    "v_lshlrev_b32 v70, 16, v225\n"
    "v_and_b32 v71, 0xffff0000, v225\n"
    "v_lshlrev_b32 v216, 16, v126\n"
    "v_and_b32 v217, 0xffff0000, v126\n"
    "v_lshlrev_b32 v218, 16, v127\n"
    "v_and_b32 v219, 0xffff0000, v127\n"
    "v_lshlrev_b32 v220, 16, v226\n"
    "v_and_b32 v221, 0xffff0000, v226\n"
    "v_lshlrev_b32 v222, 16, v227\n"
    "v_and_b32 v223, 0xffff0000, v227\n"
    "ds_write_b128 v8, v[36:39] offset:0\n"
    "ds_write_b128 v8, v[40:43] offset:4096\n"
    "ds_write_b128 v8, v[44:47] offset:8192\n"
    "buffer_load_dwordx4 v[172:175], v14, s[12:15], s67 offen sc1\n"
    "ds_write_b128 v8, v[48:51] offset:12288\n"
    "ds_write_b128 v8, v[52:55] offset:16384\n"
    "buffer_load_dwordx4 v[176:179], v14, s[12:15], s68 offen sc1\n"
    "buffer_load_dwordx4 v[180:183], v14, s[12:15], s69 offen sc1\n"
    "buffer_load_dwordx4 v[184:187], v14, s[12:15], s70 offen sc1\n"
    "s_waitcnt lgkmcnt(0)\n"
    "s_barrier\n"
    "ds_read_b128 v[80:83], v9 offset:0\n"
    "ds_read_b128 v[84:87], v9 offset:1024\n"
    "ds_read_b128 v[88:91], v9 offset:2048\n"
    "ds_read_b128 v[92:95], v9 offset:3072\n"
    "ds_read_b128 v[96:99], v10 offset:0\n"
    "ds_read_b128 v[100:103], v10 offset:1024\n"
    "ds_read_b128 v[104:107], v10 offset:2048\n"
    "ds_read_b128 v[108:111], v10 offset:3072\n"
    "buffer_load_dwordx4 v[188:191], v14, s[12:15], s71 offen sc1\n"
    "buffer_load_dwordx4 v[192:195], v14, s[12:15], s72 offen sc1\n"
    "buffer_load_dwordx4 v[196:199], v14, s[12:15], s73 offen sc1\n"
    "s_waitcnt lgkmcnt(0)\n"
    "v_pk_add_f32 v[80:81], v[80:81], v[84:85]\n"
    "v_pk_add_f32 v[88:89], v[88:89], v[92:93]\n"
    "v_pk_add_f32 v[82:83], v[82:83], v[86:87]\n"
    "v_pk_add_f32 v[90:91], v[90:91], v[94:95]\n"
    "v_pk_add_f32 v[80:81], v[80:81], v[88:89]\n"
    "v_pk_add_f32 v[82:83], v[82:83], v[90:91]\n"
    "v_pk_fma_f32 v[64:65], v[80:81], s[42:43], v[64:65]\n"
    "v_pk_fma_f32 v[66:67], v[82:83], s[42:43], v[66:67]\n"
    "v_pk_add_f32 v[96:97], v[96:97], v[100:101]\n"
    "v_pk_add_f32 v[104:105], v[104:105], v[108:109]\n"
    "v_pk_add_f32 v[98:99], v[98:99], v[102:103]\n"
    "buffer_load_dwordx4 v[200:203], v14, s[12:15], s74 offen sc1\n"
    "v_pk_add_f32 v[106:107], v[106:107], v[110:111]\n"
    "v_pk_add_f32 v[96:97], v[96:97], v[104:105]\n"
    "v_pk_add_f32 v[98:99], v[98:99], v[106:107]\n"
    "v_pk_fma_f32 v[68:69], v[96:97], s[42:43], v[68:69]\n"
    "v_pk_fma_f32 v[70:71], v[98:99], s[42:43], v[70:71]\n"
    "v_add_f32 v80, v64, v65\n"
    "v_mul_f32 v81, v64, v64\n"
    "v_add_f32 v82, v68, v69\n"
    "v_mul_f32 v83, v68, v68\n"
    "v_fmac_f32 v81, v65, v65\n"
    "v_fmac_f32 v83, v69, v69\n"
    "v_add_f32 v80, v80, v66\n"
    "v_fmac_f32 v81, v66, v66\n"
    "v_add_f32 v82, v82, v70\n"
    "v_fmac_f32 v83, v70, v70\n"
    "v_add_f32 v80, v80, v67\n"
    "v_fmac_f32 v81, v67, v67\n"
    "buffer_load_dwordx4 v[204:207], v14, s[12:15], s75 offen sc1\n"
    "v_add_f32 v82, v82, v71\n"
    "v_fmac_f32 v83, v71, v71\n"
    "v_mul_f32 v80, s16, v80\n"
    "v_mul_f32 v81, s16, v81\n"
    "v_fmac_f32 v80, s17, v82\n"
    "v_fmac_f32 v81, s17, v83\n"
    "v_mov_b32 v82, v80\n"
    "v_mov_b32 v83, v81\n"
    "s_nop 0\n"
    "v_permlane16_swap_b32 v80, v82\n"
    "v_permlane16_swap_b32 v81, v83\n"
    "v_add_f32 v80, v80, v82\n"
    "v_add_f32 v81, v81, v83\n"
    "v_mov_b32 v82, v80\n"
    "v_mov_b32 v83, v81\n"
    "s_nop 0\n"
    "v_permlane32_swap_b32 v80, v82\n"
    "v_permlane32_swap_b32 v81, v83\n"
    "v_add_f32 v80, v80, v82\n"
    "buffer_load_dwordx4 v[128:131], v14, s[12:15], s76 offen sc1\n"
    "v_add_f32 v81, v81, v83\n"
    "ds_write_b64 v6, v[80:81]\n"
    "buffer_load_dwordx4 v[132:135], v14, s[12:15], s77 offen sc1\n"
    "buffer_load_dwordx2 v[224:225], v2, s[12:15], s22 offen offset:0 sc1\n"
    "s_waitcnt lgkmcnt(0)\n"
    "s_barrier\n"
    "ds_read_b64 v[80:81], v7\n"
    "buffer_load_dwordx2 v[226:227], v2, s[12:15], s22 offen offset:512 sc1\n"
    "s_waitcnt lgkmcnt(0)\n"
    "v_mov_b32 v82, v80\n"
    "v_mov_b32 v83, v81\n"
    "s_nop 0\n"
    "v_permlane16_swap_b32 v80, v82\n"
    "v_permlane16_swap_b32 v81, v83\n"
    "v_add_f32 v80, v80, v82\n"
    "v_add_f32 v81, v81, v83\n"
    "v_mov_b32 v82, v80\n"
    "v_mov_b32 v83, v81\n"
    "s_nop 0\n"
    "v_permlane32_swap_b32 v80, v82\n"
    "v_permlane32_swap_b32 v81, v83\n"
    "v_add_f32 v80, v80, v82\n"
    "v_add_f32 v81, v81, v83\n"
    "v_min_u32 v80, -2, v80\n"
    "v_min_u32 v81, -2, v81\n"
    "buffer_store_dwordx2 v[80:81], v11, s[12:15], s23 offen offset:1024 sc1\n"
    "s_add_u32 s89, s53, 14807040\n"
    "s_lshl_b32 s88, s19, 10\n"
    "s_add_u32 s88, s88, s89\n"
    "buffer_load_dwordx4 v[112:115], v1, s[12:15], s88 offen sc1\n"
    "s_lshl_b32 s88, s20, 10\n"
    "s_add_u32 s88, s88, s89\n"
    "buffer_load_dwordx4 v[116:119], v1, s[12:15], s88 offen sc1\n"
    "s_waitcnt vmcnt(19)\n"
    "v_mfma_f32_16x16x32_bf16 v[16:19], a[0:3], v[160:163], 0\n"
    "v_mfma_f32_16x16x32_bf16 v[20:23], a[4:7], v[160:163], 0\n"
    "v_mfma_f32_16x16x32_bf16 v[24:27], a[8:11], v[160:163], 0\n"
    "v_mfma_f32_16x16x32_bf16 v[28:31], a[12:15], v[160:163], 0\n"
    "v_mfma_f32_16x16x32_bf16 v[32:35], a[16:19], v[160:163], 0\n"
    "buffer_load_dwordx4 v[136:139], v15, s[12:15], s64 offen sc1\n"
    "s_waitcnt vmcnt(18)\n"
    "v_mfma_f32_16x16x32_bf16 v[16:19], a[20:23], v[164:167], v[16:19]\n"
    "v_mfma_f32_16x16x32_bf16 v[20:23], a[24:27], v[164:167], v[20:23]\n"
    "v_mfma_f32_16x16x32_bf16 v[24:27], a[28:31], v[164:167], v[24:27]\n"
    "v_mfma_f32_16x16x32_bf16 v[28:31], a[32:35], v[164:167], v[28:31]\n"
    "v_mfma_f32_16x16x32_bf16 v[32:35], a[36:39], v[164:167], v[32:35]\n"
    "buffer_load_dwordx4 v[140:143], v15, s[12:15], s65 offen sc1\n"
    "s_waitcnt vmcnt(18)\n"
    "v_mfma_f32_16x16x32_bf16 v[16:19], a[40:43], v[168:171], v[16:19]\n"
    "v_mfma_f32_16x16x32_bf16 v[20:23], a[44:47], v[168:171], v[20:23]\n"
    "v_mfma_f32_16x16x32_bf16 v[24:27], a[48:51], v[168:171], v[24:27]\n"
    "v_mfma_f32_16x16x32_bf16 v[28:31], a[52:55], v[168:171], v[28:31]\n"
    "v_mfma_f32_16x16x32_bf16 v[32:35], a[56:59], v[168:171], v[32:35]\n"
    "buffer_load_dwordx4 v[144:147], v15, s[12:15], s66 offen sc1\n"
    "s_waitcnt vmcnt(18)\n"
    "v_mfma_f32_16x16x32_bf16 v[16:19], a[60:63], v[172:175], v[16:19]\n"
    "v_mfma_f32_16x16x32_bf16 v[20:23], a[64:67], v[172:175], v[20:23]\n"
    "v_mfma_f32_16x16x32_bf16 v[24:27], a[68:71], v[172:175], v[24:27]\n"
    "v_mfma_f32_16x16x32_bf16 v[28:31], a[72:75], v[172:175], v[28:31]\n"
    "v_mfma_f32_16x16x32_bf16 v[32:35], a[76:79], v[172:175], v[32:35]\n"
    "buffer_load_dwordx4 v[148:151], v15, s[12:15], s67 offen sc1\n"
    "s_waitcnt vmcnt(18)\n"
    "v_mfma_f32_16x16x32_bf16 v[16:19], a[80:83], v[176:179], v[16:19]\n"
    "v_mfma_f32_16x16x32_bf16 v[20:23], a[84:87], v[176:179], v[20:23]\n"
    "v_mfma_f32_16x16x32_bf16 v[24:27], a[88:91], v[176:179], v[24:27]\n"
    "v_mfma_f32_16x16x32_bf16 v[28:31], a[92:95], v[176:179], v[28:31]\n"
    "v_mfma_f32_16x16x32_bf16 v[32:35], a[96:99], v[176:179], v[32:35]\n"
    "buffer_load_dwordx4 v[152:155], v15, s[12:15], s68 offen sc1\n"
    "s_waitcnt vmcnt(8)\n"
    "v_max3_u32 v228, v224, v225, v226\n"
    "v_max_u32 v228, v228, v227\n"
    "v_cmp_eq_u32 vcc, -1, v228\n"
    "s_cbranch_vccnz Lslow_45_%=\n"
    "Lback_46_%=:\n"
    "v_mfma_f32_16x16x32_bf16 v[16:19], a[100:103], v[180:183], v[16:19]\n"
    "v_add_f32 v224, v224, v226\n"
    "v_add_f32 v225, v225, v227\n"
    "v_mov_b32 v226, v224\n"
    "v_mov_b32 v227, v225\n"
    "s_nop 0\n"
    "v_permlane16_swap_b32 v224, v226\n"
    "v_mfma_f32_16x16x32_bf16 v[20:23], a[104:107], v[180:183], v[20:23]\n"
    "v_permlane16_swap_b32 v225, v227\n"
    "v_add_f32 v224, v224, v226\n"
    "v_add_f32 v225, v225, v227\n"
    "v_mov_b32 v226, v224\n"
    "v_mov_b32 v227, v225\n"
    "v_mfma_f32_16x16x32_bf16 v[24:27], a[108:111], v[180:183], v[24:27]\n"
    "v_permlane32_swap_b32 v224, v226\n"
    "v_permlane32_swap_b32 v225, v227\n"
    "v_add_f32 v224, v224, v226\n"
    "v_add_f32 v225, v225, v227\n"
    "v_mul_f32 v224, s39, v224\n"
    "v_mfma_f32_16x16x32_bf16 v[28:31], a[112:115], v[180:183], v[28:31]\n"
    "v_mul_f32 v226, v224, v224\n"
    "v_fma_f32 v226, v225, s39, -v226\n"
    "v_max_f32 v226, 0, v226\n"
    "v_add_f32 v226, s41, v226\n"
    "v_mfma_f32_16x16x32_bf16 v[32:35], a[116:119], v[180:183], v[32:35]\n"
    "buffer_load_dwordx4 v[156:159], v15, s[12:15], s69 offen sc1\n"
    "v_rsq_f32 v226, v226\n"
    "s_nop 0\n"
    "v_mul_f32 v224, -v224, v226\n"
    "v_fma_f32 v228, v56, v226, v224\n"
    "v_mfma_f32_16x16x32_bf16 v[16:19], a[120:123], v[184:187], v[16:19]\n"
    "v_fma_f32 v229, v57, v226, v224\n"
    "v_fma_f32 v230, v58, v226, v224\n"
    "v_fma_f32 v231, v59, v226, v224\n"
    "v_lshlrev_b32 v225, 16, v72\n"
    "v_and_b32 v227, 0xffff0000, v72\n"
    "v_mfma_f32_16x16x32_bf16 v[20:23], a[124:127], v[184:187], v[20:23]\n"
    "buffer_load_dwordx2 v[124:125], v2, s[12:15], s22 offen offset:1024 sc1\n"
    "v_fmac_f32 v56, v228, v225\n"
    "v_fmac_f32 v57, v229, v227\n"
    "v_lshlrev_b32 v225, 16, v73\n"
    "v_and_b32 v227, 0xffff0000, v73\n"
    "v_mfma_f32_16x16x32_bf16 v[24:27], a[128:131], v[184:187], v[24:27]\n"
    "v_fmac_f32 v58, v230, v225\n"
    "v_fmac_f32 v59, v231, v227\n"
    "v_cvt_pk_bf16_f32 v56, v56, v57\n"
    "v_cvt_pk_bf16_f32 v57, v58, v59\n"
    "v_fma_f32 v228, v60, v226, v224\n"
    "v_mfma_f32_16x16x32_bf16 v[28:31], a[132:135], v[184:187], v[28:31]\n"
    "buffer_load_dwordx2 v[126:127], v2, s[12:15], s22 offen offset:1536 sc1\n"
    "s_nop 0\n"
    "v_fma_f32 v229, v61, v226, v224\n"
    "v_fma_f32 v230, v62, v226, v224\n"
    "v_fma_f32 v231, v63, v226, v224\n"
    "v_mfma_f32_16x16x32_bf16 v[32:35], a[136:139], v[184:187], v[32:35]\n"
    "buffer_load_dwordx4 v[160:163], v15, s[12:15], s70 offen sc1\n"
    "v_lshlrev_b32 v225, 16, v76\n"
    "v_and_b32 v227, 0xffff0000, v76\n"
    "v_fmac_f32 v60, v228, v225\n"
    "v_fmac_f32 v61, v229, v227\n"
    "v_mfma_f32_16x16x32_bf16 v[16:19], a[140:143], v[188:191], v[16:19]\n"
    "v_lshlrev_b32 v225, 16, v77\n"
    "v_and_b32 v227, 0xffff0000, v77\n"
    "v_fmac_f32 v62, v230, v225\n"
    "v_fmac_f32 v63, v231, v227\n"
    "v_cvt_pk_bf16_f32 v58, v60, v61\n"
    "v_mfma_f32_16x16x32_bf16 v[20:23], a[144:147], v[188:191], v[20:23]\n"
    "v_cvt_pk_bf16_f32 v59, v62, v63\n"
    "s_add_u32 s90, s53, 17387520\n"
    "s_add_u32 s90, s90, s18\n"
    "s_cmp_lg_u32 s9, 2\n"
    "s_cbranch_scc1 Lpair_49_%=\n"
    "v_mov_b32 v58, v56\n"
    "v_mov_b32 v59, v57\n"
    "s_nop 0\n"
    "v_permlane16_swap_b32 v56, v58\n"
    "v_permlane16_swap_b32 v57, v59\n"
    "Lpair_49_%=:\n"
    "v_mfma_f32_16x16x32_bf16 v[24:27], a[148:151], v[188:191], v[24:27]\n"
    "buffer_store_dwordx4 v[56:59], v120, s[12:15], s90 offen sc1\n"
    "v_mfma_f32_16x16x32_bf16 v[28:31], a[152:155], v[188:191], v[28:31]\n"
    "v_mfma_f32_16x16x32_bf16 v[32:35], a[156:159], v[188:191], v[32:35]\n"
    "buffer_load_dwordx4 v[164:167], v15, s[12:15], s71 offen sc1\n"
    "v_mfma_f32_16x16x32_bf16 v[16:19], a[160:163], v[192:195], v[16:19]\n"
    "v_mfma_f32_16x16x32_bf16 v[20:23], a[164:167], v[192:195], v[20:23]\n"
    "v_mfma_f32_16x16x32_bf16 v[24:27], a[168:171], v[192:195], v[24:27]\n"
    "v_mfma_f32_16x16x32_bf16 v[28:31], a[172:175], v[192:195], v[28:31]\n"
    "v_mfma_f32_16x16x32_bf16 v[32:35], a[176:179], v[192:195], v[32:35]\n"
    "buffer_load_dwordx4 v[168:171], v15, s[12:15], s72 offen sc1\n"
    "v_mfma_f32_16x16x32_bf16 v[16:19], a[180:183], v[196:199], v[16:19]\n"
    "v_mfma_f32_16x16x32_bf16 v[20:23], a[184:187], v[196:199], v[20:23]\n"
    "v_mfma_f32_16x16x32_bf16 v[24:27], a[188:191], v[196:199], v[24:27]\n"
    "v_mfma_f32_16x16x32_bf16 v[28:31], a[192:195], v[196:199], v[28:31]\n"
    "v_mfma_f32_16x16x32_bf16 v[32:35], a[196:199], v[196:199], v[32:35]\n"
    "buffer_load_dwordx4 v[172:175], v15, s[12:15], s73 offen sc1\n"
    "v_mfma_f32_16x16x32_bf16 v[16:19], a[200:203], v[200:203], v[16:19]\n"
    "v_mfma_f32_16x16x32_bf16 v[20:23], a[204:207], v[200:203], v[20:23]\n"
    "v_mfma_f32_16x16x32_bf16 v[24:27], a[208:211], v[200:203], v[24:27]\n"
    "v_mfma_f32_16x16x32_bf16 v[28:31], a[212:215], v[200:203], v[28:31]\n"
    "v_mfma_f32_16x16x32_bf16 v[32:35], a[216:219], v[200:203], v[32:35]\n"
    "buffer_load_dwordx4 v[176:179], v15, s[12:15], s74 offen sc1\n"
    "v_mfma_f32_16x16x32_bf16 v[16:19], a[220:223], v[204:207], v[16:19]\n"
    "v_mfma_f32_16x16x32_bf16 v[20:23], a[224:227], v[204:207], v[20:23]\n"
    "v_mfma_f32_16x16x32_bf16 v[24:27], a[228:231], v[204:207], v[24:27]\n"
    "v_mfma_f32_16x16x32_bf16 v[28:31], a[232:235], v[204:207], v[28:31]\n"
    "v_mfma_f32_16x16x32_bf16 v[32:35], a[236:239], v[204:207], v[32:35]\n"
    "buffer_load_dwordx4 v[180:183], v15, s[12:15], s75 offen sc1\n"
    "v_mfma_f32_16x16x32_bf16 v[16:19], a[240:243], v[128:131], v[16:19]\n"
    "v_mfma_f32_16x16x32_bf16 v[20:23], a[244:247], v[128:131], v[20:23]\n"
    "v_mfma_f32_16x16x32_bf16 v[24:27], a[248:251], v[128:131], v[24:27]\n"
    "v_mfma_f32_16x16x32_bf16 v[28:31], a[252:255], v[128:131], v[28:31]\n"
    "v_mfma_f32_16x16x32_bf16 v[32:35], v[232:235], v[128:131], v[32:35]\n"
    "buffer_load_dwordx4 v[184:187], v15, s[12:15], s76 offen sc1\n"
    "v_mfma_f32_16x16x32_bf16 v[16:19], v[236:239], v[132:135], v[16:19]\n"
    "v_mfma_f32_16x16x32_bf16 v[20:23], v[240:243], v[132:135], v[20:23]\n"
    "v_mfma_f32_16x16x32_bf16 v[24:27], v[244:247], v[132:135], v[24:27]\n"
    "v_mfma_f32_16x16x32_bf16 v[28:31], v[248:251], v[132:135], v[28:31]\n"
    "v_mfma_f32_16x16x32_bf16 v[32:35], v[252:255], v[132:135], v[32:35]\n"
    "buffer_load_dwordx4 v[188:191], v15, s[12:15], s77 offen sc1\n"
    "s_waitcnt vmcnt(16)\n"
    "v_mfma_f32_16x16x32_bf16 v[36:39], a[0:3], v[136:139], 0\n"
    "v_mfma_f32_16x16x32_bf16 v[40:43], a[4:7], v[136:139], 0\n"
    "ds_write_b128 v8, v[16:19] offset:0\n"
    "v_mfma_f32_16x16x32_bf16 v[44:47], a[8:11], v[136:139], 0\n"
    "v_mfma_f32_16x16x32_bf16 v[48:51], a[12:15], v[136:139], 0\n"
    "ds_write_b128 v8, v[20:23] offset:4096\n"
    "v_mfma_f32_16x16x32_bf16 v[52:55], a[16:19], v[136:139], 0\n"
    "s_waitcnt vmcnt(15)\n"
    "v_mfma_f32_16x16x32_bf16 v[36:39], a[20:23], v[140:143], v[36:39]\n"
    "ds_write_b128 v8, v[24:27] offset:8192\n"
    "v_mfma_f32_16x16x32_bf16 v[40:43], a[24:27], v[140:143], v[40:43]\n"
    "v_mfma_f32_16x16x32_bf16 v[44:47], a[28:31], v[140:143], v[44:47]\n"
    "ds_write_b128 v8, v[28:31] offset:12288\n"
    "v_mfma_f32_16x16x32_bf16 v[48:51], a[32:35], v[140:143], v[48:51]\n"
    "s_waitcnt vmcnt(9)\n"
    "v_max3_u32 v228, v124, v125, v126\n"
    "v_max_u32 v228, v228, v127\n"
    "v_cmp_eq_u32 vcc, -1, v228\n"
    "s_cbranch_vccnz Lslow_50_%=\n"
    "Lback_51_%=:\n"
    "v_add_f32 v124, v124, v126\n"
    "v_mfma_f32_16x16x32_bf16 v[52:55], a[36:39], v[140:143], v[52:55]\n"
    "v_add_f32 v125, v125, v127\n"
    "v_mov_b32 v126, v124\n"
    "v_mov_b32 v127, v125\n"
    "s_nop 0\n"
    "v_permlane16_swap_b32 v124, v126\n"
    "v_permlane16_swap_b32 v125, v127\n"
    "v_mfma_f32_16x16x32_bf16 v[36:39], a[40:43], v[144:147], v[36:39]\n"
    "v_add_f32 v124, v124, v126\n"
    "v_add_f32 v125, v125, v127\n"
    "v_mov_b32 v126, v124\n"
    "v_mov_b32 v127, v125\n"
    "s_nop 0\n"
    "v_permlane32_swap_b32 v124, v126\n"
    "v_mfma_f32_16x16x32_bf16 v[40:43], a[44:47], v[144:147], v[40:43]\n"
    "v_permlane32_swap_b32 v125, v127\n"
    "v_add_f32 v124, v124, v126\n"
    "v_add_f32 v125, v125, v127\n"
    "v_mul_f32 v124, s39, v124\n"
    "v_mul_f32 v126, v124, v124\n"
    "v_mfma_f32_16x16x32_bf16 v[44:47], a[48:51], v[144:147], v[44:47]\n"
    "v_fma_f32 v126, v125, s39, -v126\n"
    "v_max_f32 v126, 0, v126\n"
    "v_add_f32 v126, s41, v126\n"
    "v_rsq_f32 v126, v126\n"
    "v_mfma_f32_16x16x32_bf16 v[48:51], a[52:55], v[144:147], v[48:51]\n"
    "v_mul_f32 v124, -v124, v126\n"
    "v_fma_f32 v228, v64, v126, v124\n"
    "v_fma_f32 v229, v65, v126, v124\n"
    "v_fma_f32 v230, v66, v126, v124\n"
    "v_fma_f32 v231, v67, v126, v124\n"
    "v_mfma_f32_16x16x32_bf16 v[52:55], a[56:59], v[144:147], v[52:55]\n"
    "v_lshlrev_b32 v125, 16, v74\n"
    "v_and_b32 v127, 0xffff0000, v74\n"
    "v_fmac_f32 v64, v228, v125\n"
    "v_fmac_f32 v65, v229, v127\n"
    "v_lshlrev_b32 v125, 16, v75\n"
    "v_mfma_f32_16x16x32_bf16 v[36:39], a[60:63], v[148:151], v[36:39]\n"
    "v_and_b32 v127, 0xffff0000, v75\n"
    "v_fmac_f32 v66, v230, v125\n"
    "v_fmac_f32 v67, v231, v127\n"
    "v_cvt_pk_bf16_f32 v64, v64, v65\n"
    "v_cvt_pk_bf16_f32 v65, v66, v67\n"
    "v_mfma_f32_16x16x32_bf16 v[40:43], a[64:67], v[148:151], v[40:43]\n"
    "v_fma_f32 v228, v68, v126, v124\n"
    "v_fma_f32 v229, v69, v126, v124\n"
    "v_fma_f32 v230, v70, v126, v124\n"
    "v_fma_f32 v231, v71, v126, v124\n"
    "v_lshlrev_b32 v125, 16, v78\n"
    "v_mfma_f32_16x16x32_bf16 v[44:47], a[68:71], v[148:151], v[44:47]\n"
    "v_and_b32 v127, 0xffff0000, v78\n"
    "v_fmac_f32 v68, v228, v125\n"
    "v_fmac_f32 v69, v229, v127\n"
    "v_lshlrev_b32 v125, 16, v79\n"
    "v_and_b32 v127, 0xffff0000, v79\n"
    "v_mfma_f32_16x16x32_bf16 v[48:51], a[72:75], v[148:151], v[48:51]\n"
    "v_fmac_f32 v70, v230, v125\n"
    "v_fmac_f32 v71, v231, v127\n"
    "v_cvt_pk_bf16_f32 v66, v68, v69\n"
    "v_cvt_pk_bf16_f32 v67, v70, v71\n"
    "s_add_u32 s90, s53, 17408000\n"
    "v_mfma_f32_16x16x32_bf16 v[52:55], a[76:79], v[148:151], v[52:55]\n"
    "s_add_u32 s90, s90, s18\n"
    "s_cmp_lg_u32 s9, 2\n"
    "s_cbranch_scc1 Lpair_54_%=\n"
    "v_mov_b32 v66, v64\n"
    "v_mov_b32 v67, v65\n"
    "s_nop 0\n"
    "v_permlane16_swap_b32 v64, v66\n"
    "v_permlane16_swap_b32 v65, v67\n"
    "Lpair_54_%=:\n"
    "v_mfma_f32_16x16x32_bf16 v[36:39], a[80:83], v[152:155], v[36:39]\n"
    "buffer_store_dwordx4 v[64:67], v120, s[12:15], s90 offen sc1\n"
    "s_nop 0\n"
    "ds_write_b128 v8, v[32:35] offset:16384\n"
    "v_mfma_f32_16x16x32_bf16 v[40:43], a[84:87], v[152:155], v[40:43]\n"
    "v_mfma_f32_16x16x32_bf16 v[44:47], a[88:91], v[152:155], v[44:47]\n"
    "v_mfma_f32_16x16x32_bf16 v[48:51], a[92:95], v[152:155], v[48:51]\n"
    "v_mfma_f32_16x16x32_bf16 v[52:55], a[96:99], v[152:155], v[52:55]\n"
    "v_mfma_f32_16x16x32_bf16 v[36:39], a[100:103], v[156:159], v[36:39]\n"
    "v_mfma_f32_16x16x32_bf16 v[40:43], a[104:107], v[156:159], v[40:43]\n"
    "v_mfma_f32_16x16x32_bf16 v[44:47], a[108:111], v[156:159], v[44:47]\n"
    "v_mfma_f32_16x16x32_bf16 v[48:51], a[112:115], v[156:159], v[48:51]\n"
    "v_mfma_f32_16x16x32_bf16 v[52:55], a[116:119], v[156:159], v[52:55]\n"
    "s_waitcnt lgkmcnt(0)\n"
    "s_barrier\n"
    "ds_read_b128 v[80:83], v9 offset:0\n"
    "ds_read_b128 v[84:87], v9 offset:1024\n"
    "ds_read_b128 v[88:91], v9 offset:2048\n"
    "s_waitcnt vmcnt(9)\n"
    "v_mfma_f32_16x16x32_bf16 v[36:39], a[120:123], v[160:163], v[36:39]\n"
    "ds_read_b128 v[92:95], v9 offset:3072\n"
    "ds_read_b128 v[96:99], v10 offset:0\n"
    "ds_read_b128 v[100:103], v10 offset:1024\n"
    "ds_read_b128 v[104:107], v10 offset:2048\n"
    "ds_read_b128 v[108:111], v10 offset:3072\n"
    "v_mfma_f32_16x16x32_bf16 v[40:43], a[124:127], v[160:163], v[40:43]\n"
    "v_mfma_f32_16x16x32_bf16 v[44:47], a[128:131], v[160:163], v[44:47]\n"
    "v_mfma_f32_16x16x32_bf16 v[48:51], a[132:135], v[160:163], v[48:51]\n"
    "v_mfma_f32_16x16x32_bf16 v[52:55], a[136:139], v[160:163], v[52:55]\n"
    "s_waitcnt vmcnt(7)\n"
    "v_mfma_f32_16x16x32_bf16 v[36:39], a[140:143], v[164:167], v[36:39]\n"
    "v_mfma_f32_16x16x32_bf16 v[40:43], a[144:147], v[164:167], v[40:43]\n"
    "v_mfma_f32_16x16x32_bf16 v[44:47], a[148:151], v[164:167], v[44:47]\n"
    "v_mfma_f32_16x16x32_bf16 v[48:51], a[152:155], v[164:167], v[48:51]\n"
    "v_mfma_f32_16x16x32_bf16 v[52:55], a[156:159], v[164:167], v[52:55]\n"
    "s_waitcnt lgkmcnt(0)\n"
    "v_add_f32 v80, v80, v84\n"
    "v_add_f32 v81, v81, v85\n"
    "v_add_f32 v82, v82, v86\n"
    "v_add_f32 v83, v83, v87\n"
    "s_waitcnt vmcnt(6)\n"
    "v_mfma_f32_16x16x32_bf16 v[36:39], a[160:163], v[168:171], v[36:39]\n"
    "v_add_f32 v88, v88, v92\n"
    "v_add_f32 v89, v89, v93\n"
    "v_add_f32 v90, v90, v94\n"
    "v_add_f32 v91, v91, v95\n"
    "v_add_f32 v80, v80, v88\n"
    "v_mfma_f32_16x16x32_bf16 v[40:43], a[164:167], v[168:171], v[40:43]\n"
    "v_add_f32 v81, v81, v89\n"
    "v_add_f32 v82, v82, v90\n"
    "v_add_f32 v83, v83, v91\n"
    "v_fmac_f32 v208, s42, v80\n"
    "v_fmac_f32 v209, s42, v81\n"
    "v_mfma_f32_16x16x32_bf16 v[44:47], a[168:171], v[168:171], v[44:47]\n"
    "v_fmac_f32 v210, s42, v82\n"
    "v_fmac_f32 v211, s42, v83\n"
    "v_add_f32 v96, v96, v100\n"
    "v_add_f32 v97, v97, v101\n"
    "v_add_f32 v98, v98, v102\n"
    "v_mfma_f32_16x16x32_bf16 v[48:51], a[172:175], v[168:171], v[48:51]\n"
    "v_add_f32 v99, v99, v103\n"
    "v_add_f32 v104, v104, v108\n"
    "v_add_f32 v105, v105, v109\n"
    "v_add_f32 v106, v106, v110\n"
    "v_add_f32 v107, v107, v111\n"
    "v_mfma_f32_16x16x32_bf16 v[52:55], a[176:179], v[168:171], v[52:55]\n"
    "v_add_f32 v96, v96, v104\n"
    "v_add_f32 v97, v97, v105\n"
    "v_add_f32 v98, v98, v106\n"
    "v_add_f32 v99, v99, v107\n"
    "v_fmac_f32 v212, s42, v96\n"
    "s_waitcnt vmcnt(5)\n"
    "v_mfma_f32_16x16x32_bf16 v[36:39], a[180:183], v[172:175], v[36:39]\n"
    "v_fmac_f32 v213, s42, v97\n"
    "v_fmac_f32 v214, s42, v98\n"
    "v_fmac_f32 v215, s42, v99\n"
    "v_add_f32 v80, v208, v209\n"
    "v_mul_f32 v81, v208, v208\n"
    "v_mfma_f32_16x16x32_bf16 v[40:43], a[184:187], v[172:175], v[40:43]\n"
    "v_add_f32 v82, v212, v213\n"
    "v_mul_f32 v83, v212, v212\n"
    "v_fmac_f32 v81, v209, v209\n"
    "v_fmac_f32 v83, v213, v213\n"
    "v_add_f32 v80, v80, v210\n"
    "v_mfma_f32_16x16x32_bf16 v[44:47], a[188:191], v[172:175], v[44:47]\n"
    "v_fmac_f32 v81, v210, v210\n"
    "v_add_f32 v82, v82, v214\n"
    "v_fmac_f32 v83, v214, v214\n"
    "v_add_f32 v80, v80, v211\n"
    "v_fmac_f32 v81, v211, v211\n"
    "v_mfma_f32_16x16x32_bf16 v[48:51], a[192:195], v[172:175], v[48:51]\n"
    "v_add_f32 v82, v82, v215\n"
    "v_fmac_f32 v83, v215, v215\n"
    "v_mul_f32 v80, s16, v80\n"
    "v_mul_f32 v81, s16, v81\n"
    "v_fmac_f32 v80, s17, v82\n"
    "v_mfma_f32_16x16x32_bf16 v[52:55], a[196:199], v[172:175], v[52:55]\n"
    "v_fmac_f32 v81, s17, v83\n"
    "v_mov_b32 v82, v80\n"
    "v_mov_b32 v83, v81\n"
    "s_nop 0\n"
    "v_permlane16_swap_b32 v80, v82\n"
    "v_permlane16_swap_b32 v81, v83\n"
    "s_waitcnt vmcnt(4)\n"
    "v_mfma_f32_16x16x32_bf16 v[36:39], a[200:203], v[176:179], v[36:39]\n"
    "v_add_f32 v80, v80, v82\n"
    "v_add_f32 v81, v81, v83\n"
    "v_mov_b32 v82, v80\n"
    "v_mov_b32 v83, v81\n"
    "s_nop 0\n"
    "v_permlane32_swap_b32 v80, v82\n"
    "v_mfma_f32_16x16x32_bf16 v[40:43], a[204:207], v[176:179], v[40:43]\n"
    "v_permlane32_swap_b32 v81, v83\n"
    "v_add_f32 v80, v80, v82\n"
    "v_add_f32 v81, v81, v83\n"
    "ds_write_b64 v6, v[80:81]\n"
    "v_mfma_f32_16x16x32_bf16 v[44:47], a[208:211], v[176:179], v[44:47]\n"
    "v_mfma_f32_16x16x32_bf16 v[48:51], a[212:215], v[176:179], v[48:51]\n"
    "v_mfma_f32_16x16x32_bf16 v[52:55], a[216:219], v[176:179], v[52:55]\n"
    "s_waitcnt vmcnt(3)\n"
    "v_mfma_f32_16x16x32_bf16 v[36:39], a[220:223], v[180:183], v[36:39]\n"
    "v_mfma_f32_16x16x32_bf16 v[40:43], a[224:227], v[180:183], v[40:43]\n"
    "s_waitcnt vmcnt(0)\n"
    "s_barrier\n"
    "s_cmp_lg_u32 s9, 0\n"
    "s_cbranch_scc1 Lnosig_55_%=\n"
    "s_mov_b64 exec, 3\n"
    "buffer_atomic_add v123, v122, s[12:15], s58 offen offset:256\n"
    "s_mov_b64 exec, -1\n"
    "Lnosig_55_%=:\n"
    "v_mfma_f32_16x16x32_bf16 v[44:47], a[228:231], v[180:183], v[44:47]\n"
    "s_nop 0\n"
    "v_mfma_f32_16x16x32_bf16 v[48:51], a[232:235], v[180:183], v[48:51]\n"
    "v_mfma_f32_16x16x32_bf16 v[52:55], a[236:239], v[180:183], v[52:55]\n"
    "v_mfma_f32_16x16x32_bf16 v[36:39], a[240:243], v[184:187], v[36:39]\n"
    "s_waitcnt lgkmcnt(0)\n"
    "s_barrier\n"
    "ds_read_b64 v[80:81], v7\n"
    "v_mfma_f32_16x16x32_bf16 v[40:43], a[244:247], v[184:187], v[40:43]\n"
    "v_mfma_f32_16x16x32_bf16 v[44:47], a[248:251], v[184:187], v[44:47]\n"
    "v_mfma_f32_16x16x32_bf16 v[48:51], a[252:255], v[184:187], v[48:51]\n"
    "v_mfma_f32_16x16x32_bf16 v[52:55], v[232:235], v[184:187], v[52:55]\n"
    "v_mfma_f32_16x16x32_bf16 v[36:39], v[236:239], v[188:191], v[36:39]\n"
    "v_mfma_f32_16x16x32_bf16 v[40:43], v[240:243], v[188:191], v[40:43]\n"
    "v_mfma_f32_16x16x32_bf16 v[44:47], v[244:247], v[188:191], v[44:47]\n"
    "v_mfma_f32_16x16x32_bf16 v[48:51], v[248:251], v[188:191], v[48:51]\n"
    "v_mfma_f32_16x16x32_bf16 v[52:55], v[252:255], v[188:191], v[52:55]\n"
    "s_waitcnt lgkmcnt(0)\n"
    "v_mov_b32 v82, v80\n"
    "v_mov_b32 v83, v81\n"
    "s_nop 0\n"
    "v_permlane16_swap_b32 v80, v82\n"
    "v_permlane16_swap_b32 v81, v83\n"
    "v_add_f32 v80, v80, v82\n"
    "v_add_f32 v81, v81, v83\n"
    "v_mov_b32 v82, v80\n"
    "v_mov_b32 v83, v81\n"
    "s_nop 0\n"
    "v_permlane32_swap_b32 v80, v82\n"
    "v_permlane32_swap_b32 v81, v83\n"
    "v_add_f32 v80, v80, v82\n"
    "v_add_f32 v81, v81, v83\n"
    "v_min_u32 v80, -2, v80\n"
    "v_min_u32 v81, -2, v81\n"
    "buffer_store_dwordx2 v[80:81], v11, s[12:15], s23 offen offset:2048 sc1\n"
    "ds_write_b128 v8, v[36:39] offset:0\n"
    "ds_write_b128 v8, v[40:43] offset:4096\n"
    "ds_write_b128 v8, v[44:47] offset:8192\n"
    "ds_write_b128 v8, v[48:51] offset:12288\n"
    "ds_write_b128 v8, v[52:55] offset:16384\n"
    "s_nop 0\n"
    "v_add_u32 v4, 35840, v1\n"
    "v_add_u32 v5, 71680, v1\n"
    "s_mul_i32 s83, s56, 20480\n"
    "s_add_u32 s83, s83, 17387520\n"
    "v_add_u32 v14, s83, v1\n"
    "v_add_u32 v15, 40960, v14\n"
    "v_mov_b32 v12, s50\n"
    "s_waitcnt lgkmcnt(0)\n"
    "s_barrier\n"
    "ds_read_b128 v[80:83], v9 offset:0\n"
    "ds_read_b128 v[84:87], v9 offset:1024\n"
    "ds_read_b128 v[88:91], v9 offset:2048\n"
    "ds_read_b128 v[92:95], v9 offset:3072\n"
    "ds_read_b128 v[96:99], v10 offset:0\n"
    "ds_read_b128 v[100:103], v10 offset:1024\n"
    "ds_read_b128 v[104:107], v10 offset:2048\n"
    "ds_read_b128 v[108:111], v10 offset:3072\n"
    "s_waitcnt lgkmcnt(0)\n"
    "v_pk_add_f32 v[80:81], v[80:81], v[84:85]\n"
    "v_pk_add_f32 v[88:89], v[88:89], v[92:93]\n"
    "v_pk_add_f32 v[82:83], v[82:83], v[86:87]\n"
    "v_pk_add_f32 v[90:91], v[90:91], v[94:95]\n"
    "v_pk_add_f32 v[80:81], v[80:81], v[88:89]\n"
    "v_pk_add_f32 v[82:83], v[82:83], v[90:91]\n"
    "v_pk_fma_f32 v[216:217], v[80:81], s[42:43], v[216:217]\n"
    "v_pk_fma_f32 v[218:219], v[82:83], s[42:43], v[218:219]\n"
    "v_pk_add_f32 v[96:97], v[96:97], v[100:101]\n"
    "v_pk_add_f32 v[104:105], v[104:105], v[108:109]\n"
    "v_pk_add_f32 v[98:99], v[98:99], v[102:103]\n"
    "v_pk_add_f32 v[106:107], v[106:107], v[110:111]\n"
    "v_pk_add_f32 v[96:97], v[96:97], v[104:105]\n"
    "v_pk_add_f32 v[98:99], v[98:99], v[106:107]\n"
    "v_pk_fma_f32 v[220:221], v[96:97], s[42:43], v[220:221]\n"
    "v_pk_fma_f32 v[222:223], v[98:99], s[42:43], v[222:223]\n"
    "v_add_f32 v80, v216, v217\n"
    "v_mul_f32 v81, v216, v216\n"
    "v_add_f32 v82, v220, v221\n"
    "v_mul_f32 v83, v220, v220\n"
    "v_fmac_f32 v81, v217, v217\n"
    "v_fmac_f32 v83, v221, v221\n"
    "v_add_f32 v80, v80, v218\n"
    "v_fmac_f32 v81, v218, v218\n"
    "v_add_f32 v82, v82, v222\n"
    "v_fmac_f32 v83, v222, v222\n"
    "v_add_f32 v80, v80, v219\n"
    "v_fmac_f32 v81, v219, v219\n"
    "v_add_f32 v82, v82, v223\n"
    "v_fmac_f32 v83, v223, v223\n"
    "v_mul_f32 v80, s16, v80\n"
    "v_mul_f32 v81, s16, v81\n"
    "v_fmac_f32 v80, s17, v82\n"
    "v_fmac_f32 v81, s17, v83\n"
    "v_mov_b32 v82, v80\n"
    "v_mov_b32 v83, v81\n"
    "s_nop 0\n"
    "v_permlane16_swap_b32 v80, v82\n"
    "v_permlane16_swap_b32 v81, v83\n"
    "v_add_f32 v80, v80, v82\n"
    "v_add_f32 v81, v81, v83\n"
    "v_mov_b32 v82, v80\n"
    "v_mov_b32 v83, v81\n"
    "s_nop 0\n"
    "v_permlane32_swap_b32 v80, v82\n"
    "v_permlane32_swap_b32 v81, v83\n"
    "v_add_f32 v80, v80, v82\n"
    "v_add_f32 v81, v81, v83\n"
    "ds_write_b64 v6, v[80:81]\n"
    "buffer_load_dwordx2 v[224:225], v2, s[12:15], s22 offen offset:2048 sc1\n"
    "v_add_u32 v6, 107520, v1\n"
    "buffer_load_dword v19, v12, s[12:15], 0 offen offset:256 sc1\n"
    "buffer_load_dwordx2 v[226:227], v2, s[12:15], s22 offen offset:2560 sc1\n"
    "s_waitcnt lgkmcnt(0)\n"
    "s_barrier\n"
    "ds_read_b64 v[80:81], v7\n"
    "s_waitcnt lgkmcnt(0)\n"
    "v_mov_b32 v82, v80\n"
    "v_mov_b32 v83, v81\n"
    "s_nop 0\n"
    "v_permlane16_swap_b32 v80, v82\n"
    "v_permlane16_swap_b32 v81, v83\n"
    "v_add_f32 v80, v80, v82\n"
    "v_add_f32 v81, v81, v83\n"
    "v_mov_b32 v82, v80\n"
    "v_mov_b32 v83, v81\n"
    "s_nop 0\n"
    "v_permlane32_swap_b32 v80, v82\n"
    "v_permlane32_swap_b32 v81, v83\n"
    "v_add_f32 v80, v80, v82\n"
    "v_add_f32 v81, v81, v83\n"
    "v_min_u32 v80, -2, v80\n"
    "v_min_u32 v81, -2, v81\n"
    "buffer_store_dwordx2 v[80:81], v11, s[12:15], s23 offen offset:3072 sc1\n"
    "s_mov_b32 s49, 27348992\n"
    "s_movk_i32 s52, 128\n"
    "s_waitcnt vmcnt(2)\n"
    "v_cmp_ne_u32 vcc, 8, v19\n"
    "s_cbranch_vccnz Lslow_66_%=\n"
    "Lback_67_%=:\n"
    "Lg1b_%=:\n"
    "buffer_load_dwordx4 v[128:131], v14, s[12:15], s78 offen offset:0 sc1\n"
    "buffer_load_dwordx4 v[132:135], v14, s[12:15], s78 offen offset:1024 sc1\n"
    "buffer_load_dwordx4 v[136:139], v14, s[12:15], s78 offen offset:2048 sc1\n"
    "buffer_load_dwordx4 v[140:143], v14, s[12:15], s78 offen offset:3072 sc1\n"
    "buffer_load_dwordx4 v[144:147], v14, s[12:15], s79 offen offset:0 sc1\n"
    "buffer_load_dwordx4 v[148:151], v14, s[12:15], s79 offen offset:1024 sc1\n"
    "ds_read_b128 v[48:51], v1 offset:28672\n"
    "ds_read_b128 v[52:55], v1 offset:29696\n"
    "ds_read_b128 v[56:59], v1 offset:30720\n"
    "ds_read_b128 v[60:63], v1 offset:31744\n"
    "ds_read_b128 v[64:67], v1 offset:32768\n"
    "ds_read_b128 v[68:71], v1 offset:33792\n"
    "ds_read_b128 v[72:75], v1 offset:34816\n"
    "ds_read_b128 v[76:79], v4 offset:28672\n"
    "ds_read_b128 v[80:83], v4 offset:29696\n"
    "ds_read_b128 v[84:87], v4 offset:30720\n"
    "ds_read_b128 v[88:91], v4 offset:31744\n"
    "ds_read_b128 v[92:95], v4 offset:32768\n"
    "ds_read_b128 v[96:99], v4 offset:33792\n"
    "ds_read_b128 v[100:103], v4 offset:34816\n"
    "s_waitcnt vmcnt(7)\n"
    "v_max3_u32 v228, v224, v225, v226\n"
    "v_max_u32 v228, v228, v227\n"
    "v_cmp_eq_u32 vcc, -1, v228\n"
    "s_cbranch_vccnz Lslow_56_%=\n"
    "Lback_57_%=:\n"
    "v_add_f32 v224, v224, v226\n"
    "v_add_f32 v225, v225, v227\n"
    "v_mov_b32 v226, v224\n"
    "v_mov_b32 v227, v225\n"
    "s_nop 0\n"
    "v_permlane16_swap_b32 v224, v226\n"
    "v_permlane16_swap_b32 v225, v227\n"
    "v_add_f32 v224, v224, v226\n"
    "v_add_f32 v225, v225, v227\n"
    "v_mov_b32 v226, v224\n"
    "v_mov_b32 v227, v225\n"
    "s_nop 0\n"
    "v_permlane32_swap_b32 v224, v226\n"
    "v_permlane32_swap_b32 v225, v227\n"
    "v_add_f32 v224, v224, v226\n"
    "v_add_f32 v225, v225, v227\n"
    "v_mul_f32 v224, s39, v224\n"
    "v_mul_f32 v226, v224, v224\n"
    "v_fma_f32 v226, v225, s39, -v226\n"
    "v_max_f32 v226, 0, v226\n"
    "v_add_f32 v226, s41, v226\n"
    "v_rsq_f32 v226, v226\n"
    "s_nop 0\n"
    "v_mul_f32 v224, -v224, v226\n"
    "v_lshlrev_b32 v228, 16, v112\n"
    "v_and_b32 v229, 0xffff0000, v112\n"
    "v_lshlrev_b32 v230, 16, v113\n"
    "v_and_b32 v231, 0xffff0000, v113\n"
    "v_pk_fma_f32 v[112:113], v[208:209], v[226:227], v[224:225] op_sel_hi:[1,0,0]\n"
    "v_pk_fma_f32 v[208:209], v[112:113], v[228:229], v[208:209]\n"
    "v_pk_fma_f32 v[112:113], v[210:211], v[226:227], v[224:225] op_sel_hi:[1,0,0]\n"
    "v_pk_fma_f32 v[210:211], v[112:113], v[230:231], v[210:211]\n"
    "v_cvt_pk_bf16_f32 v208, v208, v209\n"
    "v_cvt_pk_bf16_f32 v209, v210, v211\n"
    "v_lshlrev_b32 v228, 16, v116\n"
    "v_and_b32 v229, 0xffff0000, v116\n"
    "v_lshlrev_b32 v230, 16, v117\n"
    "v_and_b32 v231, 0xffff0000, v117\n"
    "v_pk_fma_f32 v[116:117], v[212:213], v[226:227], v[224:225] op_sel_hi:[1,0,0]\n"
    "v_pk_fma_f32 v[212:213], v[116:117], v[228:229], v[212:213]\n"
    "v_pk_fma_f32 v[116:117], v[214:215], v[226:227], v[224:225] op_sel_hi:[1,0,0]\n"
    "v_pk_fma_f32 v[214:215], v[116:117], v[230:231], v[214:215]\n"
    "v_cvt_pk_bf16_f32 v210, v212, v213\n"
    "v_cvt_pk_bf16_f32 v211, v214, v215\n"
    "buffer_load_dwordx2 v[124:125], v2, s[12:15], s22 offen offset:3072 sc1\n"
    "buffer_load_dwordx2 v[126:127], v2, s[12:15], s22 offen offset:3584 sc1\n"
    "buffer_load_dwordx4 v[152:155], v14, s[12:15], s79 offen offset:2048 sc1\n"
    "buffer_load_dwordx4 v[156:159], v14, s[12:15], s79 offen offset:3072 sc1\n"
    "buffer_load_dwordx4 v[160:163], v14, s[12:15], s80 offen offset:0 sc1\n"
    "buffer_load_dwordx4 v[164:167], v14, s[12:15], s80 offen offset:1024 sc1\n"
    "s_add_u32 s90, s53, 17428480\n"
    "s_add_u32 s90, s90, s18\n"
    "s_cmp_lg_u32 s9, 2\n"
    "s_cbranch_scc1 Lpair_70_%=\n"
    "v_mov_b32 v210, v208\n"
    "v_mov_b32 v211, v209\n"
    "s_nop 0\n"
    "v_permlane16_swap_b32 v208, v210\n"
    "v_permlane16_swap_b32 v209, v211\n"
    "Lpair_70_%=:\n"
    "buffer_store_dwordx4 v[208:211], v120, s[12:15], s90 offen sc1\n"
    "s_waitcnt vmcnt(12) lgkmcnt(10)\n"
    "v_mfma_f32_16x16x32_bf16 v[16:19], v[48:51], v[128:131], 0\n"
    "ds_read_b128 v[104:107], v5 offset:28672\n"
    "buffer_load_dwordx4 v[168:171], v14, s[12:15], s80 offen offset:2048 sc1\n"
    "v_mfma_f32_16x16x32_bf16 v[20:23], v[52:55], v[128:131], 0\n"
    "ds_read_b128 v[108:111], v5 offset:29696\n"
    "v_mfma_f32_16x16x32_bf16 v[24:27], v[56:59], v[128:131], 0\n"
    "ds_read_b128 v[48:51], v5 offset:30720\n"
    "v_mfma_f32_16x16x32_bf16 v[28:31], v[60:63], v[128:131], 0\n"
    "ds_read_b128 v[52:55], v5 offset:31744\n"
    "s_waitcnt lgkmcnt(11)\n"
    "v_mfma_f32_16x16x32_bf16 v[32:35], v[64:67], v[128:131], 0\n"
    "ds_read_b128 v[56:59], v5 offset:32768\n"
    "buffer_load_dwordx4 v[172:175], v14, s[12:15], s80 offen offset:3072 sc1\n"
    "v_mfma_f32_16x16x32_bf16 v[36:39], v[68:71], v[128:131], 0\n"
    "ds_read_b128 v[60:63], v5 offset:33792\n"
    "v_mfma_f32_16x16x32_bf16 v[40:43], v[72:75], v[128:131], 0\n"
    "ds_read_b128 v[64:67], v5 offset:34816\n"
    "v_mfma_f32_16x16x32_bf16 v[44:47], v[128:131], v[128:131], 0\n"
    "s_waitcnt vmcnt(13) lgkmcnt(10)\n"
    "v_mfma_f32_16x16x32_bf16 v[16:19], v[76:79], v[132:135], v[16:19]\n"
    "ds_read_b128 v[68:71], v6 offset:28672\n"
    "buffer_load_dwordx4 v[176:179], v14, s[12:15], s81 offen offset:0 sc1\n"
    "v_mfma_f32_16x16x32_bf16 v[20:23], v[80:83], v[132:135], v[20:23]\n"
    "ds_read_b128 v[72:75], v6 offset:29696\n"
    "v_mfma_f32_16x16x32_bf16 v[24:27], v[84:87], v[132:135], v[24:27]\n"
    "ds_read_b128 v[76:79], v6 offset:30720\n"
    "v_mfma_f32_16x16x32_bf16 v[28:31], v[88:91], v[132:135], v[28:31]\n"
    "ds_read_b128 v[80:83], v6 offset:31744\n"
    "s_waitcnt lgkmcnt(11)\n"
    "v_mfma_f32_16x16x32_bf16 v[32:35], v[92:95], v[132:135], v[32:35]\n"
    "ds_read_b128 v[84:87], v6 offset:32768\n"
    "buffer_load_dwordx4 v[180:183], v14, s[12:15], s81 offen offset:1024 sc1\n"
    "v_mfma_f32_16x16x32_bf16 v[36:39], v[96:99], v[132:135], v[36:39]\n"
    "ds_read_b128 v[88:91], v6 offset:33792\n"
    "v_mfma_f32_16x16x32_bf16 v[40:43], v[100:103], v[132:135], v[40:43]\n"
    "ds_read_b128 v[92:95], v6 offset:34816\n"
    "v_mfma_f32_16x16x32_bf16 v[44:47], v[132:135], v[132:135], v[44:47]\n"
    "s_waitcnt vmcnt(14) lgkmcnt(10)\n"
    "v_mfma_f32_16x16x32_bf16 v[16:19], v[104:107], v[136:139], v[16:19]\n"
    "ds_read_b128 v[96:99], v1 offset:0\n"
    "buffer_load_dwordx4 v[184:187], v14, s[12:15], s81 offen offset:2048 sc1\n"
    "v_mfma_f32_16x16x32_bf16 v[20:23], v[108:111], v[136:139], v[20:23]\n"
    "ds_read_b128 v[100:103], v1 offset:1024\n"
    "v_mfma_f32_16x16x32_bf16 v[24:27], v[48:51], v[136:139], v[24:27]\n"
    "ds_read_b128 v[104:107], v1 offset:2048\n"
    "v_mfma_f32_16x16x32_bf16 v[28:31], v[52:55], v[136:139], v[28:31]\n"
    "ds_read_b128 v[108:111], v1 offset:3072\n"
    "s_waitcnt lgkmcnt(11)\n"
    "s_waitcnt vmcnt(10)\n"
    "v_max3_u32 v228, v124, v125, v126\n"
    "v_max_u32 v228, v228, v127\n"
    "v_cmp_eq_u32 vcc, -1, v228\n"
    "s_cbranch_vccnz Lslow_60_%=\n"
    "Lback_61_%=:\n"
    "v_mfma_f32_16x16x32_bf16 v[32:35], v[56:59], v[136:139], v[32:35]\n"
    "ds_read_b128 v[48:51], v1 offset:4096\n"
    "v_add_f32 v124, v124, v126\n"
    "v_add_f32 v125, v125, v127\n"
    "v_mov_b32 v126, v124\n"
    "v_mov_b32 v127, v125\n"
    "s_nop 0\n"
    "v_permlane16_swap_b32 v124, v126\n"
    "v_mfma_f32_16x16x32_bf16 v[36:39], v[60:63], v[136:139], v[36:39]\n"
    "ds_read_b128 v[52:55], v1 offset:5120\n"
    "v_permlane16_swap_b32 v125, v127\n"
    "v_add_f32 v124, v124, v126\n"
    "v_add_f32 v125, v125, v127\n"
    "v_mov_b32 v126, v124\n"
    "v_mov_b32 v127, v125\n"
    "v_mfma_f32_16x16x32_bf16 v[40:43], v[64:67], v[136:139], v[40:43]\n"
    "ds_read_b128 v[56:59], v1 offset:6144\n"
    "v_permlane32_swap_b32 v124, v126\n"
    "v_permlane32_swap_b32 v125, v127\n"
    "v_add_f32 v124, v124, v126\n"
    "v_add_f32 v125, v125, v127\n"
    "v_mul_f32 v124, s39, v124\n"
    "v_mfma_f32_16x16x32_bf16 v[44:47], v[136:139], v[136:139], v[44:47]\n"
    "s_waitcnt vmcnt(5) lgkmcnt(10)\n"
    "v_mul_f32 v126, v124, v124\n"
    "v_fma_f32 v126, v125, s39, -v126\n"
    "v_max_f32 v126, 0, v126\n"
    "v_add_f32 v126, s41, v126\n"
    "buffer_load_dwordx4 v[188:191], v14, s[12:15], s81 offen offset:3072 sc1\n"
    "v_mfma_f32_16x16x32_bf16 v[16:19], v[68:71], v[140:143], v[16:19]\n"
    "ds_read_b128 v[60:63], v4 offset:0\n"
    "v_rsq_f32 v126, v126\n"
    "s_nop 0\n"
    "v_mul_f32 v124, -v124, v126\n"
    "v_fma_f32 v228, v216, v126, v124\n"
    "v_fma_f32 v229, v217, v126, v124\n"
    "v_mfma_f32_16x16x32_bf16 v[20:23], v[72:75], v[140:143], v[20:23]\n"
    "ds_read_b128 v[64:67], v4 offset:1024\n"
    "v_fma_f32 v230, v218, v126, v124\n"
    "v_fma_f32 v231, v219, v126, v124\n"
    "v_lshlrev_b32 v125, 16, v114\n"
    "v_and_b32 v127, 0xffff0000, v114\n"
    "v_fmac_f32 v216, v228, v125\n"
    "v_mfma_f32_16x16x32_bf16 v[24:27], v[76:79], v[140:143], v[24:27]\n"
    "ds_read_b128 v[68:71], v4 offset:2048\n"
    "v_fmac_f32 v217, v229, v127\n"
    "v_lshlrev_b32 v125, 16, v115\n"
    "v_and_b32 v127, 0xffff0000, v115\n"
    "v_fmac_f32 v218, v230, v125\n"
    "v_fmac_f32 v219, v231, v127\n"
    "v_mfma_f32_16x16x32_bf16 v[28:31], v[80:83], v[140:143], v[28:31]\n"
    "ds_read_b128 v[72:75], v4 offset:3072\n"
    "s_waitcnt lgkmcnt(11)\n"
    "v_cvt_pk_bf16_f32 v216, v216, v217\n"
    "v_cvt_pk_bf16_f32 v217, v218, v219\n"
    "v_fma_f32 v228, v220, v126, v124\n"
    "v_fma_f32 v229, v221, v126, v124\n"
    "v_mfma_f32_16x16x32_bf16 v[32:35], v[84:87], v[140:143], v[32:35]\n"
    "ds_read_b128 v[76:79], v4 offset:4096\n"
    "v_fma_f32 v230, v222, v126, v124\n"
    "v_fma_f32 v231, v223, v126, v124\n"
    "v_lshlrev_b32 v125, 16, v118\n"
    "v_and_b32 v127, 0xffff0000, v118\n"
    "v_fmac_f32 v220, v228, v125\n"
    "v_mfma_f32_16x16x32_bf16 v[36:39], v[88:91], v[140:143], v[36:39]\n"
    "ds_read_b128 v[80:83], v4 offset:5120\n"
    "v_fmac_f32 v221, v229, v127\n"
    "v_lshlrev_b32 v125, 16, v119\n"
    "v_and_b32 v127, 0xffff0000, v119\n"
    "v_fmac_f32 v222, v230, v125\n"
    "v_fmac_f32 v223, v231, v127\n"
    "v_mfma_f32_16x16x32_bf16 v[40:43], v[92:95], v[140:143], v[40:43]\n"
    "ds_read_b128 v[84:87], v4 offset:6144\n"
    "v_cvt_pk_bf16_f32 v218, v220, v221\n"
    "v_cvt_pk_bf16_f32 v219, v222, v223\n"
    "s_add_u32 s90, s53, 17448960\n"
    "s_add_u32 s90, s90, s18\n"
    "s_cmp_lg_u32 s9, 2\n"
    "s_cbranch_scc1 Lpair_64_%=\n"
    "v_mov_b32 v218, v216\n"
    "v_mov_b32 v219, v217\n"
    "s_nop 0\n"
    "v_permlane16_swap_b32 v216, v218\n"
    "v_permlane16_swap_b32 v217, v219\n"
    "Lpair_64_%=:\n"
    "v_mfma_f32_16x16x32_bf16 v[44:47], v[140:143], v[140:143], v[44:47]\n"
    "s_waitcnt lgkmcnt(10)\n"
    "buffer_store_dwordx4 v[216:219], v120, s[12:15], s90 offen sc1\n"
    "v_mfma_f32_16x16x32_bf16 v[16:19], v[96:99], v[144:147], v[16:19]\n"
    "ds_read_b128 v[88:91], v5 offset:0\n"
    "v_mfma_f32_16x16x32_bf16 v[20:23], v[100:103], v[144:147], v[20:23]\n"
    "ds_read_b128 v[92:95], v5 offset:1024\n"
    "v_mfma_f32_16x16x32_bf16 v[24:27], v[104:107], v[144:147], v[24:27]\n"
    "ds_read_b128 v[96:99], v5 offset:2048\n"
    "v_mfma_f32_16x16x32_bf16 v[28:31], v[108:111], v[144:147], v[28:31]\n"
    "ds_read_b128 v[100:103], v5 offset:3072\n"
    "s_waitcnt lgkmcnt(11)\n"
    "buffer_load_dwordx4 v[192:195], v14, s[12:15], s82 offen offset:0 sc1\n"
    "v_mfma_f32_16x16x32_bf16 v[32:35], v[48:51], v[144:147], v[32:35]\n"
    "ds_read_b128 v[104:107], v5 offset:4096\n"
    "v_mfma_f32_16x16x32_bf16 v[36:39], v[52:55], v[144:147], v[36:39]\n"
    "ds_read_b128 v[108:111], v5 offset:5120\n"
    "v_mfma_f32_16x16x32_bf16 v[40:43], v[56:59], v[144:147], v[40:43]\n"
    "ds_read_b128 v[48:51], v5 offset:6144\n"
    "v_mfma_f32_16x16x32_bf16 v[44:47], v[144:147], v[144:147], v[44:47]\n"
    "s_waitcnt lgkmcnt(10)\n"
    "buffer_load_dwordx4 v[196:199], v14, s[12:15], s82 offen offset:1024 sc1\n"
    "v_mfma_f32_16x16x32_bf16 v[16:19], v[60:63], v[148:151], v[16:19]\n"
    "ds_read_b128 v[52:55], v6 offset:0\n"
    "v_mfma_f32_16x16x32_bf16 v[20:23], v[64:67], v[148:151], v[20:23]\n"
    "ds_read_b128 v[56:59], v6 offset:1024\n"
    "v_mfma_f32_16x16x32_bf16 v[24:27], v[68:71], v[148:151], v[24:27]\n"
    "ds_read_b128 v[60:63], v6 offset:2048\n"
    "v_mfma_f32_16x16x32_bf16 v[28:31], v[72:75], v[148:151], v[28:31]\n"
    "ds_read_b128 v[64:67], v6 offset:3072\n"
    "s_waitcnt lgkmcnt(11)\n"
    "buffer_load_dwordx4 v[200:203], v14, s[12:15], s82 offen offset:2048 sc1\n"
    "v_mfma_f32_16x16x32_bf16 v[32:35], v[76:79], v[148:151], v[32:35]\n"
    "ds_read_b128 v[68:71], v6 offset:4096\n"
    "v_mfma_f32_16x16x32_bf16 v[36:39], v[80:83], v[148:151], v[36:39]\n"
    "ds_read_b128 v[72:75], v6 offset:5120\n"
    "v_mfma_f32_16x16x32_bf16 v[40:43], v[84:87], v[148:151], v[40:43]\n"
    "ds_read_b128 v[76:79], v6 offset:6144\n"
    "v_mfma_f32_16x16x32_bf16 v[44:47], v[148:151], v[148:151], v[44:47]\n"
    "s_waitcnt lgkmcnt(10)\n"
    "buffer_load_dwordx4 v[204:207], v14, s[12:15], s82 offen offset:3072 sc1\n"
    "v_mfma_f32_16x16x32_bf16 v[16:19], v[88:91], v[152:155], v[16:19]\n"
    "ds_read_b128 v[80:83], v1 offset:7168\n"
    "v_mfma_f32_16x16x32_bf16 v[20:23], v[92:95], v[152:155], v[20:23]\n"
    "ds_read_b128 v[84:87], v1 offset:8192\n"
    "v_mfma_f32_16x16x32_bf16 v[24:27], v[96:99], v[152:155], v[24:27]\n"
    "ds_read_b128 v[88:91], v1 offset:9216\n"
    "v_mfma_f32_16x16x32_bf16 v[28:31], v[100:103], v[152:155], v[28:31]\n"
    "ds_read_b128 v[92:95], v1 offset:10240\n"
    "s_waitcnt lgkmcnt(11)\n"
    "v_mfma_f32_16x16x32_bf16 v[32:35], v[104:107], v[152:155], v[32:35]\n"
    "ds_read_b128 v[96:99], v1 offset:11264\n"
    "v_mfma_f32_16x16x32_bf16 v[36:39], v[108:111], v[152:155], v[36:39]\n"
    "ds_read_b128 v[100:103], v1 offset:12288\n"
    "v_mfma_f32_16x16x32_bf16 v[40:43], v[48:51], v[152:155], v[40:43]\n"
    "ds_read_b128 v[104:107], v1 offset:13312\n"
    "v_mfma_f32_16x16x32_bf16 v[44:47], v[152:155], v[152:155], v[44:47]\n"
    "s_waitcnt lgkmcnt(10)\n"
    "v_mfma_f32_16x16x32_bf16 v[16:19], v[52:55], v[156:159], v[16:19]\n"
    "ds_read_b128 v[108:111], v4 offset:7168\n"
    "v_mfma_f32_16x16x32_bf16 v[20:23], v[56:59], v[156:159], v[20:23]\n"
    "ds_read_b128 v[48:51], v4 offset:8192\n"
    "v_mfma_f32_16x16x32_bf16 v[24:27], v[60:63], v[156:159], v[24:27]\n"
    "ds_read_b128 v[52:55], v4 offset:9216\n"
    "v_mfma_f32_16x16x32_bf16 v[28:31], v[64:67], v[156:159], v[28:31]\n"
    "ds_read_b128 v[56:59], v4 offset:10240\n"
    "s_waitcnt lgkmcnt(11)\n"
    "v_mfma_f32_16x16x32_bf16 v[32:35], v[68:71], v[156:159], v[32:35]\n"
    "ds_read_b128 v[60:63], v4 offset:11264\n"
    "v_mfma_f32_16x16x32_bf16 v[36:39], v[72:75], v[156:159], v[36:39]\n"
    "ds_read_b128 v[64:67], v4 offset:12288\n"
    "v_mfma_f32_16x16x32_bf16 v[40:43], v[76:79], v[156:159], v[40:43]\n"
    "ds_read_b128 v[68:71], v4 offset:13312\n"
    "v_mfma_f32_16x16x32_bf16 v[44:47], v[156:159], v[156:159], v[44:47]\n"
    "s_waitcnt lgkmcnt(10)\n"
    "v_mfma_f32_16x16x32_bf16 v[16:19], v[80:83], v[160:163], v[16:19]\n"
    "ds_read_b128 v[72:75], v5 offset:7168\n"
    "v_mfma_f32_16x16x32_bf16 v[20:23], v[84:87], v[160:163], v[20:23]\n"
    "ds_read_b128 v[76:79], v5 offset:8192\n"
    "v_mfma_f32_16x16x32_bf16 v[24:27], v[88:91], v[160:163], v[24:27]\n"
    "ds_read_b128 v[80:83], v5 offset:9216\n"
    "v_mfma_f32_16x16x32_bf16 v[28:31], v[92:95], v[160:163], v[28:31]\n"
    "ds_read_b128 v[84:87], v5 offset:10240\n"
    "s_waitcnt lgkmcnt(11)\n"
    "s_waitcnt vmcnt(4)\n"
    "s_barrier\n"
    "s_cmp_lg_u32 s9, 0\n"
    "s_cbranch_scc1 Lnosig_65_%=\n"
    "s_mov_b64 exec, 3\n"
    "buffer_atomic_add v123, v122, s[12:15], s58 offen offset:1024\n"
    "s_mov_b64 exec, -1\n"
    "Lnosig_65_%=:\n"
    "v_mfma_f32_16x16x32_bf16 v[32:35], v[96:99], v[160:163], v[32:35]\n"
    "ds_read_b128 v[88:91], v5 offset:11264\n"
    "v_lshrrev_b32 v228, 4, v0\n"
    "v_and_b32 v229, 15, v0\n"
    "v_lshlrev_b32 v112, 8, v228\n"
    "v_add_u32 v112, 6352, v112\n"
    "v_lshrrev_b32 v230, 2, v229\n"
    "v_mfma_f32_16x16x32_bf16 v[36:39], v[100:103], v[160:163], v[36:39]\n"
    "ds_read_b128 v[92:95], v5 offset:12288\n"
    "v_lshl_add_u32 v113, v230, 4, v229\n"
    "v_lshlrev_b32 v113, 2, v113\n"
    "v_lshlrev_b32 v114, 2, v229\n"
    "v_add_u32 v114, 192, v114\n"
    "s_lshl_b32 s84, s5, 1\n"
    "v_mfma_f32_16x16x32_bf16 v[40:43], v[104:107], v[160:163], v[40:43]\n"
    "ds_read_b128 v[96:99], v5 offset:13312\n"
    "v_lshrrev_b32 v230, 1, v228\n"
    "v_add_u32 v230, s84, v230\n"
    "v_lshlrev_b32 v115, 8, v230\n"
    "v_lshl_add_u32 v115, v229, 4, v115\n"
    "v_add_u32 v115, 49152, v115\n"
    "v_mfma_f32_16x16x32_bf16 v[44:47], v[160:163], v[160:163], v[44:47]\n"
    "s_waitcnt lgkmcnt(10)\n"
    "v_and_b32 v228, 1, v228\n"
    "v_mul_u32_u24 v228, 32767, v228\n"
    "v_lshl_add_u32 v115, v228, 16, v115\n"
    "v_mfma_f32_16x16x32_bf16 v[16:19], v[108:111], v[164:167], v[16:19]\n"
    "ds_read_b128 v[100:103], v6 offset:7168\n"
    "v_mfma_f32_16x16x32_bf16 v[20:23], v[48:51], v[164:167], v[20:23]\n"
    "ds_read_b128 v[104:107], v6 offset:8192\n"
    "v_mfma_f32_16x16x32_bf16 v[24:27], v[52:55], v[164:167], v[24:27]\n"
    "ds_read_b128 v[108:111], v6 offset:9216\n"
    "v_mfma_f32_16x16x32_bf16 v[28:31], v[56:59], v[164:167], v[28:31]\n"
    "ds_read_b128 v[48:51], v6 offset:10240\n"
    "s_waitcnt lgkmcnt(11)\n"
    "v_mfma_f32_16x16x32_bf16 v[32:35], v[60:63], v[164:167], v[32:35]\n"
    "ds_read_b128 v[52:55], v6 offset:11264\n"
    "v_mfma_f32_16x16x32_bf16 v[36:39], v[64:67], v[164:167], v[36:39]\n"
    "ds_read_b128 v[56:59], v6 offset:12288\n"
    "v_mfma_f32_16x16x32_bf16 v[40:43], v[68:71], v[164:167], v[40:43]\n"
    "ds_read_b128 v[60:63], v6 offset:13312\n"
    "v_mfma_f32_16x16x32_bf16 v[44:47], v[164:167], v[164:167], v[44:47]\n"
    "s_waitcnt lgkmcnt(10)\n"
    "v_mfma_f32_16x16x32_bf16 v[16:19], v[72:75], v[168:171], v[16:19]\n"
    "ds_read_b128 v[64:67], v1 offset:14336\n"
    "v_mfma_f32_16x16x32_bf16 v[20:23], v[76:79], v[168:171], v[20:23]\n"
    "ds_read_b128 v[68:71], v1 offset:15360\n"
    "v_mfma_f32_16x16x32_bf16 v[24:27], v[80:83], v[168:171], v[24:27]\n"
    "ds_read_b128 v[72:75], v1 offset:16384\n"
    "v_mfma_f32_16x16x32_bf16 v[28:31], v[84:87], v[168:171], v[28:31]\n"
    "ds_read_b128 v[76:79], v1 offset:17408\n"
    "s_waitcnt lgkmcnt(11)\n"
    "v_mfma_f32_16x16x32_bf16 v[32:35], v[88:91], v[168:171], v[32:35]\n"
    "ds_read_b128 v[80:83], v1 offset:18432\n"
    "v_mfma_f32_16x16x32_bf16 v[36:39], v[92:95], v[168:171], v[36:39]\n"
    "ds_read_b128 v[84:87], v1 offset:19456\n"
    "v_mfma_f32_16x16x32_bf16 v[40:43], v[96:99], v[168:171], v[40:43]\n"
    "ds_read_b128 v[88:91], v1 offset:20480\n"
    "v_mfma_f32_16x16x32_bf16 v[44:47], v[168:171], v[168:171], v[44:47]\n"
    "s_waitcnt lgkmcnt(10)\n"
    "v_mfma_f32_16x16x32_bf16 v[16:19], v[100:103], v[172:175], v[16:19]\n"
    "ds_read_b128 v[92:95], v4 offset:14336\n"
    "v_mfma_f32_16x16x32_bf16 v[20:23], v[104:107], v[172:175], v[20:23]\n"
    "ds_read_b128 v[96:99], v4 offset:15360\n"
    "v_mfma_f32_16x16x32_bf16 v[24:27], v[108:111], v[172:175], v[24:27]\n"
    "ds_read_b128 v[100:103], v4 offset:16384\n"
    "v_mfma_f32_16x16x32_bf16 v[28:31], v[48:51], v[172:175], v[28:31]\n"
    "ds_read_b128 v[104:107], v4 offset:17408\n"
    "s_waitcnt lgkmcnt(11)\n"
    "v_mfma_f32_16x16x32_bf16 v[32:35], v[52:55], v[172:175], v[32:35]\n"
    "ds_read_b128 v[108:111], v4 offset:18432\n"
    "v_mfma_f32_16x16x32_bf16 v[36:39], v[56:59], v[172:175], v[36:39]\n"
    "ds_read_b128 v[48:51], v4 offset:19456\n"
    "v_mfma_f32_16x16x32_bf16 v[40:43], v[60:63], v[172:175], v[40:43]\n"
    "ds_read_b128 v[52:55], v4 offset:20480\n"
    "v_mfma_f32_16x16x32_bf16 v[44:47], v[172:175], v[172:175], v[44:47]\n"
    "s_waitcnt lgkmcnt(10)\n"
    "v_mfma_f32_16x16x32_bf16 v[16:19], v[64:67], v[176:179], v[16:19]\n"
    "ds_read_b128 v[56:59], v5 offset:14336\n"
    "v_mfma_f32_16x16x32_bf16 v[20:23], v[68:71], v[176:179], v[20:23]\n"
    "ds_read_b128 v[60:63], v5 offset:15360\n"
    "v_mfma_f32_16x16x32_bf16 v[24:27], v[72:75], v[176:179], v[24:27]\n"
    "ds_read_b128 v[64:67], v5 offset:16384\n"
    "v_mfma_f32_16x16x32_bf16 v[28:31], v[76:79], v[176:179], v[28:31]\n"
    "ds_read_b128 v[68:71], v5 offset:17408\n"
    "s_waitcnt lgkmcnt(11)\n"
    "v_mfma_f32_16x16x32_bf16 v[32:35], v[80:83], v[176:179], v[32:35]\n"
    "ds_read_b128 v[72:75], v5 offset:18432\n"
    "v_mfma_f32_16x16x32_bf16 v[36:39], v[84:87], v[176:179], v[36:39]\n"
    "ds_read_b128 v[76:79], v5 offset:19456\n"
    "v_mfma_f32_16x16x32_bf16 v[40:43], v[88:91], v[176:179], v[40:43]\n"
    "ds_read_b128 v[80:83], v5 offset:20480\n"
    "v_mfma_f32_16x16x32_bf16 v[44:47], v[176:179], v[176:179], v[44:47]\n"
    "s_waitcnt lgkmcnt(10)\n"
    "v_mfma_f32_16x16x32_bf16 v[16:19], v[92:95], v[180:183], v[16:19]\n"
    "ds_read_b128 v[84:87], v6 offset:14336\n"
    "v_mfma_f32_16x16x32_bf16 v[20:23], v[96:99], v[180:183], v[20:23]\n"
    "ds_read_b128 v[88:91], v6 offset:15360\n"
    "v_mfma_f32_16x16x32_bf16 v[24:27], v[100:103], v[180:183], v[24:27]\n"
    "ds_read_b128 v[92:95], v6 offset:16384\n"
    "v_mfma_f32_16x16x32_bf16 v[28:31], v[104:107], v[180:183], v[28:31]\n"
    "ds_read_b128 v[96:99], v6 offset:17408\n"
    "s_waitcnt lgkmcnt(11)\n"
    "v_mfma_f32_16x16x32_bf16 v[32:35], v[108:111], v[180:183], v[32:35]\n"
    "ds_read_b128 v[100:103], v6 offset:18432\n"
    "v_mfma_f32_16x16x32_bf16 v[36:39], v[48:51], v[180:183], v[36:39]\n"
    "ds_read_b128 v[104:107], v6 offset:19456\n"
    "v_mfma_f32_16x16x32_bf16 v[40:43], v[52:55], v[180:183], v[40:43]\n"
    "ds_read_b128 v[108:111], v6 offset:20480\n"
    "v_mfma_f32_16x16x32_bf16 v[44:47], v[180:183], v[180:183], v[44:47]\n"
    "s_waitcnt lgkmcnt(10)\n"
    "buffer_load_dword v224, v12, s[12:15], 0 offen offset:1024 sc1\n"
    "v_mfma_f32_16x16x32_bf16 v[16:19], v[56:59], v[184:187], v[16:19]\n"
    "ds_read_b128 v[48:51], v1 offset:21504\n"
    "v_mfma_f32_16x16x32_bf16 v[20:23], v[60:63], v[184:187], v[20:23]\n"
    "ds_read_b128 v[52:55], v1 offset:22528\n"
    "v_mfma_f32_16x16x32_bf16 v[24:27], v[64:67], v[184:187], v[24:27]\n"
    "ds_read_b128 v[56:59], v1 offset:23552\n"
    "v_mfma_f32_16x16x32_bf16 v[28:31], v[68:71], v[184:187], v[28:31]\n"
    "ds_read_b128 v[60:63], v1 offset:24576\n"
    "s_waitcnt lgkmcnt(11)\n"
    "v_mfma_f32_16x16x32_bf16 v[32:35], v[72:75], v[184:187], v[32:35]\n"
    "ds_read_b128 v[64:67], v1 offset:25600\n"
    "v_mfma_f32_16x16x32_bf16 v[36:39], v[76:79], v[184:187], v[36:39]\n"
    "ds_read_b128 v[68:71], v1 offset:26624\n"
    "v_mfma_f32_16x16x32_bf16 v[40:43], v[80:83], v[184:187], v[40:43]\n"
    "ds_read_b128 v[72:75], v1 offset:27648\n"
    "v_mfma_f32_16x16x32_bf16 v[44:47], v[184:187], v[184:187], v[44:47]\n"
    "s_waitcnt lgkmcnt(10)\n"
    "v_mfma_f32_16x16x32_bf16 v[16:19], v[84:87], v[188:191], v[16:19]\n"
    "ds_read_b128 v[76:79], v4 offset:21504\n"
    "v_mfma_f32_16x16x32_bf16 v[20:23], v[88:91], v[188:191], v[20:23]\n"
    "ds_read_b128 v[80:83], v4 offset:22528\n"
    "v_mfma_f32_16x16x32_bf16 v[24:27], v[92:95], v[188:191], v[24:27]\n"
    "ds_read_b128 v[84:87], v4 offset:23552\n"
    "v_mfma_f32_16x16x32_bf16 v[28:31], v[96:99], v[188:191], v[28:31]\n"
    "ds_read_b128 v[88:91], v4 offset:24576\n"
    "s_waitcnt lgkmcnt(11)\n"
    "v_mfma_f32_16x16x32_bf16 v[32:35], v[100:103], v[188:191], v[32:35]\n"
    "ds_read_b128 v[92:95], v4 offset:25600\n"
    "v_mfma_f32_16x16x32_bf16 v[36:39], v[104:107], v[188:191], v[36:39]\n"
    "ds_read_b128 v[96:99], v4 offset:26624\n"
    "v_mfma_f32_16x16x32_bf16 v[40:43], v[108:111], v[188:191], v[40:43]\n"
    "ds_read_b128 v[100:103], v4 offset:27648\n"
    "v_mfma_f32_16x16x32_bf16 v[44:47], v[188:191], v[188:191], v[44:47]\n"
    "s_waitcnt vmcnt(4) lgkmcnt(10)\n"
    "v_mfma_f32_16x16x32_bf16 v[16:19], v[48:51], v[192:195], v[16:19]\n"
    "ds_read_b128 v[104:107], v5 offset:21504\n"
    "v_mfma_f32_16x16x32_bf16 v[20:23], v[52:55], v[192:195], v[20:23]\n"
    "ds_read_b128 v[108:111], v5 offset:22528\n"
    "v_mfma_f32_16x16x32_bf16 v[24:27], v[56:59], v[192:195], v[24:27]\n"
    "ds_read_b128 v[48:51], v5 offset:23552\n"
    "v_mfma_f32_16x16x32_bf16 v[28:31], v[60:63], v[192:195], v[28:31]\n"
    "ds_read_b128 v[52:55], v5 offset:24576\n"
    "s_waitcnt lgkmcnt(11)\n"
    "v_mfma_f32_16x16x32_bf16 v[32:35], v[64:67], v[192:195], v[32:35]\n"
    "ds_read_b128 v[56:59], v5 offset:25600\n"
    "v_mfma_f32_16x16x32_bf16 v[36:39], v[68:71], v[192:195], v[36:39]\n"
    "ds_read_b128 v[60:63], v5 offset:26624\n"
    "v_mfma_f32_16x16x32_bf16 v[40:43], v[72:75], v[192:195], v[40:43]\n"
    "ds_read_b128 v[64:67], v5 offset:27648\n"
    "v_mfma_f32_16x16x32_bf16 v[44:47], v[192:195], v[192:195], v[44:47]\n"
    "s_waitcnt vmcnt(3) lgkmcnt(10)\n"
    "v_mfma_f32_16x16x32_bf16 v[16:19], v[76:79], v[196:199], v[16:19]\n"
    "ds_read_b128 v[68:71], v6 offset:21504\n"
    "v_mfma_f32_16x16x32_bf16 v[20:23], v[80:83], v[196:199], v[20:23]\n"
    "ds_read_b128 v[72:75], v6 offset:22528\n"
    "v_mfma_f32_16x16x32_bf16 v[24:27], v[84:87], v[196:199], v[24:27]\n"
    "ds_read_b128 v[76:79], v6 offset:23552\n"
    "v_mfma_f32_16x16x32_bf16 v[28:31], v[88:91], v[196:199], v[28:31]\n"
    "ds_read_b128 v[80:83], v6 offset:24576\n"
    "s_waitcnt lgkmcnt(11)\n"
    "v_mfma_f32_16x16x32_bf16 v[32:35], v[92:95], v[196:199], v[32:35]\n"
    "ds_read_b128 v[84:87], v6 offset:25600\n"
    "v_mfma_f32_16x16x32_bf16 v[36:39], v[96:99], v[196:199], v[36:39]\n"
    "ds_read_b128 v[88:91], v6 offset:26624\n"
    "v_mfma_f32_16x16x32_bf16 v[40:43], v[100:103], v[196:199], v[40:43]\n"
    "ds_read_b128 v[92:95], v6 offset:27648\n"
    "v_mfma_f32_16x16x32_bf16 v[44:47], v[196:199], v[196:199], v[44:47]\n"
    "s_waitcnt vmcnt(2) lgkmcnt(10)\n"
    "v_mfma_f32_16x16x32_bf16 v[16:19], v[104:107], v[200:203], v[16:19]\n"
    "ds_read_b128 v[96:99], v1 offset:28672\n"
    "v_mfma_f32_16x16x32_bf16 v[20:23], v[108:111], v[200:203], v[20:23]\n"
    "ds_read_b128 v[100:103], v1 offset:29696\n"
    "v_mfma_f32_16x16x32_bf16 v[24:27], v[48:51], v[200:203], v[24:27]\n"
    "ds_read_b128 v[104:107], v1 offset:30720\n"
    "v_mfma_f32_16x16x32_bf16 v[28:31], v[52:55], v[200:203], v[28:31]\n"
    "ds_read_b128 v[108:111], v1 offset:31744\n"
    "s_waitcnt lgkmcnt(11)\n"
    "v_mfma_f32_16x16x32_bf16 v[32:35], v[56:59], v[200:203], v[32:35]\n"
    "ds_read_b128 v[48:51], v1 offset:32768\n"
    "v_mfma_f32_16x16x32_bf16 v[36:39], v[60:63], v[200:203], v[36:39]\n"
    "ds_read_b128 v[52:55], v1 offset:33792\n"
    "v_mfma_f32_16x16x32_bf16 v[40:43], v[64:67], v[200:203], v[40:43]\n"
    "ds_read_b128 v[56:59], v1 offset:34816\n"
    "v_mfma_f32_16x16x32_bf16 v[44:47], v[200:203], v[200:203], v[44:47]\n"
    "s_waitcnt vmcnt(1) lgkmcnt(10)\n"
    "v_mfma_f32_16x16x32_bf16 v[16:19], v[68:71], v[204:207], v[16:19]\n"
    "ds_read_b128 v[60:63], v4 offset:28672\n"
    "v_mfma_f32_16x16x32_bf16 v[20:23], v[72:75], v[204:207], v[20:23]\n"
    "ds_read_b128 v[64:67], v4 offset:29696\n"
    "v_mfma_f32_16x16x32_bf16 v[24:27], v[76:79], v[204:207], v[24:27]\n"
    "ds_read_b128 v[68:71], v4 offset:30720\n"
    "v_mfma_f32_16x16x32_bf16 v[28:31], v[80:83], v[204:207], v[28:31]\n"
    "ds_read_b128 v[72:75], v4 offset:31744\n"
    "s_waitcnt lgkmcnt(11)\n"
    "v_mfma_f32_16x16x32_bf16 v[32:35], v[84:87], v[204:207], v[32:35]\n"
    "ds_read_b128 v[76:79], v4 offset:32768\n"
    "v_mfma_f32_16x16x32_bf16 v[36:39], v[88:91], v[204:207], v[36:39]\n"
    "ds_read_b128 v[80:83], v4 offset:33792\n"
    "v_mfma_f32_16x16x32_bf16 v[40:43], v[92:95], v[204:207], v[40:43]\n"
    "ds_read_b128 v[84:87], v4 offset:34816\n"
    "v_mfma_f32_16x16x32_bf16 v[44:47], v[204:207], v[204:207], v[44:47]\n"
    "s_waitcnt vmcnt(0)\n"
    "v_cmp_ne_u32 vcc, 8, v224\n"
    "s_cbranch_vccnz Lslow_71_%=\n"
    "Lback_72_%=:\n"
    "buffer_load_dwordx4 v[128:131], v15, s[12:15], s78 offen offset:0 sc1\n"
    "buffer_load_dwordx4 v[132:135], v15, s[12:15], s78 offen offset:1024 sc1\n"
    "ds_read_b128 v[208:211], v112 offset:0\n"
    "ds_read_b128 v[212:215], v112 offset:16\n"
    "s_add_u32 s90, s56, 0\n"
    "s_mul_i32 s90, s90, 57344\n"
    "buffer_load_dwordx4 v[136:139], v15, s[12:15], s78 offen offset:2048 sc1\n"
    "s_add_u32 s90, s90, s49\n"
    "s_mul_i32 s91, s5, 3072\n"
    "s_add_u32 s91, s91, s90\n"
    "v_cndmask_b32 v44, v44, v45, s[32:33]\n"
    "v_cndmask_b32 v44, v44, v46, s[34:35]\n"
    "v_cndmask_b32 v44, v44, v47, s[36:37]\n"
    "ds_bpermute_b32 v46, v114, v40\n"
    "ds_bpermute_b32 v45, v113, v44\n"
    "buffer_load_dwordx4 v[140:143], v15, s[12:15], s78 offen offset:3072 sc1\n"
    "s_waitcnt lgkmcnt(1)\n"
    "v_mul_f32 v46, s39, v46\n"
    "v_mul_f32 v47, v46, v46\n"
    "s_waitcnt lgkmcnt(0)\n"
    "v_fma_f32 v44, v45, s39, -v47\n"
    "v_max_f32 v44, 0, v44\n"
    "v_add_f32 v44, s41, v44\n"
    "v_rsq_f32 v44, v44\n"
    "s_nop 0\n"
    "v_mul_f32 v44, s40, v44\n"
    "buffer_load_dwordx4 v[144:147], v15, s[12:15], s79 offen offset:0 sc1\n"
    "v_mul_f32 v45, -v46, v44\n"
    "ds_read_b128 v[216:219], v112 offset:7168\n"
    "ds_read_b128 v[220:223], v112 offset:7184\n"
    "v_pk_fma_f32 v[212:213], v[208:209], v[44:45], v[212:213] op_sel:[0,1,0] op_sel_hi:[1,1,1]\n"
    "v_pk_fma_f32 v[214:215], v[210:211], v[44:45], v[214:215] op_sel:[0,1,0] op_sel_hi:[1,1,1]\n"
    "v_pk_fma_f32 v[16:17], v[16:17], v[44:45], v[212:213] op_sel_hi:[1,0,1]\n"
    "v_pk_fma_f32 v[18:19], v[18:19], v[44:45], v[214:215] op_sel_hi:[1,0,1]\n"
    "v_exp_f32 v208, v16\n"
    "buffer_load_dwordx4 v[148:151], v15, s[12:15], s79 offen offset:1024 sc1\n"
    "v_exp_f32 v209, v17\n"
    "v_exp_f32 v210, v18\n"
    "v_exp_f32 v211, v19\n"
    "v_pk_fma_f32 v[208:209], v[208:209], s[44:45], s[44:45]\n"
    "v_pk_fma_f32 v[210:211], v[210:211], s[44:45], s[44:45]\n"
    "buffer_load_dwordx4 v[152:155], v15, s[12:15], s79 offen offset:2048 sc1\n"
    "v_pk_fma_f32 v[208:209], v[208:209], v[208:209], s[46:47]\n"
    "v_pk_fma_f32 v[210:211], v[210:211], v[210:211], s[46:47]\n"
    "v_rcp_f32 v208, v208\n"
    "v_rcp_f32 v209, v209\n"
    "v_rcp_f32 v210, v210\n"
    "buffer_load_dwordx4 v[156:159], v15, s[12:15], s79 offen offset:3072 sc1\n"
    "v_rcp_f32 v211, v211\n"
    "v_pk_fma_f32 v[16:17], v[16:17], v[208:209], v[16:17] neg_lo:[1,0,0] neg_hi:[1,0,0]\n"
    "v_pk_fma_f32 v[18:19], v[18:19], v[210:211], v[18:19] neg_lo:[1,0,0] neg_hi:[1,0,0]\n"
    "v_cvt_pk_bf16_f32 v224, v16, v17\n"
    "v_cvt_pk_bf16_f32 v225, v18, v19\n"
    "ds_read_b128 v[208:211], v112 offset:14336\n"
    "ds_read_b128 v[212:215], v112 offset:14352\n"
    "buffer_load_dwordx4 v[160:163], v15, s[12:15], s80 offen offset:0 sc1\n"
    "s_waitcnt lgkmcnt(2)\n"
    "v_pk_fma_f32 v[220:221], v[216:217], v[44:45], v[220:221] op_sel:[0,1,0] op_sel_hi:[1,1,1]\n"
    "v_pk_fma_f32 v[222:223], v[218:219], v[44:45], v[222:223] op_sel:[0,1,0] op_sel_hi:[1,1,1]\n"
    "v_pk_fma_f32 v[20:21], v[20:21], v[44:45], v[220:221] op_sel_hi:[1,0,1]\n"
    "v_pk_fma_f32 v[22:23], v[22:23], v[44:45], v[222:223] op_sel_hi:[1,0,1]\n"
    "v_exp_f32 v216, v20\n"
    "v_exp_f32 v217, v21\n"
    "buffer_load_dwordx4 v[164:167], v15, s[12:15], s80 offen offset:1024 sc1\n"
    "v_exp_f32 v218, v22\n"
    "v_exp_f32 v219, v23\n"
    "v_pk_fma_f32 v[216:217], v[216:217], s[44:45], s[44:45]\n"
    "v_pk_fma_f32 v[218:219], v[218:219], s[44:45], s[44:45]\n"
    "v_pk_fma_f32 v[216:217], v[216:217], v[216:217], s[46:47]\n"
    "v_pk_fma_f32 v[218:219], v[218:219], v[218:219], s[46:47]\n"
    "buffer_load_dwordx4 v[168:171], v15, s[12:15], s80 offen offset:2048 sc1\n"
    "v_rcp_f32 v216, v216\n"
    "v_rcp_f32 v217, v217\n"
    "v_rcp_f32 v218, v218\n"
    "v_rcp_f32 v219, v219\n"
    "v_pk_fma_f32 v[20:21], v[20:21], v[216:217], v[20:21] neg_lo:[1,0,0] neg_hi:[1,0,0]\n"
    "v_pk_fma_f32 v[22:23], v[22:23], v[218:219], v[22:23] neg_lo:[1,0,0] neg_hi:[1,0,0]\n"
    "v_cvt_pk_bf16_f32 v226, v20, v21\n"
    "v_cvt_pk_bf16_f32 v227, v22, v23\n"
    "buffer_store_dwordx4 v[224:227], v1, s[12:15], s91 offen offset:0 sc1\n"
    "ds_read_b128 v[216:219], v112 offset:21504\n"
    "ds_read_b128 v[220:223], v112 offset:21520\n"
    "s_waitcnt lgkmcnt(2)\n"
    "v_pk_fma_f32 v[212:213], v[208:209], v[44:45], v[212:213] op_sel:[0,1,0] op_sel_hi:[1,1,1]\n"
    "v_pk_fma_f32 v[214:215], v[210:211], v[44:45], v[214:215] op_sel:[0,1,0] op_sel_hi:[1,1,1]\n"
    "v_pk_fma_f32 v[24:25], v[24:25], v[44:45], v[212:213] op_sel_hi:[1,0,1]\n"
    "v_pk_fma_f32 v[26:27], v[26:27], v[44:45], v[214:215] op_sel_hi:[1,0,1]\n"
    "v_exp_f32 v208, v24\n"
    "v_exp_f32 v209, v25\n"
    "v_exp_f32 v210, v26\n"
    "v_exp_f32 v211, v27\n"
    "v_pk_fma_f32 v[208:209], v[208:209], s[44:45], s[44:45]\n"
    "v_pk_fma_f32 v[210:211], v[210:211], s[44:45], s[44:45]\n"
    "v_pk_fma_f32 v[208:209], v[208:209], v[208:209], s[46:47]\n"
    "v_pk_fma_f32 v[210:211], v[210:211], v[210:211], s[46:47]\n"
    "v_rcp_f32 v208, v208\n"
    "v_rcp_f32 v209, v209\n"
    "v_rcp_f32 v210, v210\n"
    "v_rcp_f32 v211, v211\n"
    "v_pk_fma_f32 v[24:25], v[24:25], v[208:209], v[24:25] neg_lo:[1,0,0] neg_hi:[1,0,0]\n"
    "v_pk_fma_f32 v[26:27], v[26:27], v[210:211], v[26:27] neg_lo:[1,0,0] neg_hi:[1,0,0]\n"
    "v_cvt_pk_bf16_f32 v228, v24, v25\n"
    "v_cvt_pk_bf16_f32 v229, v26, v27\n"
    "ds_read_b128 v[208:211], v112 offset:28672\n"
    "ds_read_b128 v[212:215], v112 offset:28688\n"
    "s_waitcnt lgkmcnt(2)\n"
    "v_pk_fma_f32 v[220:221], v[216:217], v[44:45], v[220:221] op_sel:[0,1,0] op_sel_hi:[1,1,1]\n"
    "v_pk_fma_f32 v[222:223], v[218:219], v[44:45], v[222:223] op_sel:[0,1,0] op_sel_hi:[1,1,1]\n"
    "v_pk_fma_f32 v[28:29], v[28:29], v[44:45], v[220:221] op_sel_hi:[1,0,1]\n"
    "v_pk_fma_f32 v[30:31], v[30:31], v[44:45], v[222:223] op_sel_hi:[1,0,1]\n"
    "v_exp_f32 v216, v28\n"
    "v_exp_f32 v217, v29\n"
    "v_exp_f32 v218, v30\n"
    "v_exp_f32 v219, v31\n"
    "v_pk_fma_f32 v[216:217], v[216:217], s[44:45], s[44:45]\n"
    "v_pk_fma_f32 v[218:219], v[218:219], s[44:45], s[44:45]\n"
    "v_pk_fma_f32 v[216:217], v[216:217], v[216:217], s[46:47]\n"
    "v_pk_fma_f32 v[218:219], v[218:219], v[218:219], s[46:47]\n"
    "v_rcp_f32 v216, v216\n"
    "v_rcp_f32 v217, v217\n"
    "v_rcp_f32 v218, v218\n"
    "v_rcp_f32 v219, v219\n"
    "v_pk_fma_f32 v[28:29], v[28:29], v[216:217], v[28:29] neg_lo:[1,0,0] neg_hi:[1,0,0]\n"
    "v_pk_fma_f32 v[30:31], v[30:31], v[218:219], v[30:31] neg_lo:[1,0,0] neg_hi:[1,0,0]\n"
    "v_cvt_pk_bf16_f32 v230, v28, v29\n"
    "v_cvt_pk_bf16_f32 v231, v30, v31\n"
    "buffer_store_dwordx4 v[228:231], v1, s[12:15], s91 offen offset:1024 sc1\n"
    "ds_read_b128 v[216:219], v112 offset:35840\n"
    "ds_read_b128 v[220:223], v112 offset:35856\n"
    "s_waitcnt lgkmcnt(2)\n"
    "v_pk_fma_f32 v[212:213], v[208:209], v[44:45], v[212:213] op_sel:[0,1,0] op_sel_hi:[1,1,1]\n"
    "v_pk_fma_f32 v[214:215], v[210:211], v[44:45], v[214:215] op_sel:[0,1,0] op_sel_hi:[1,1,1]\n"
    "v_pk_fma_f32 v[32:33], v[32:33], v[44:45], v[212:213] op_sel_hi:[1,0,1]\n"
    "v_pk_fma_f32 v[34:35], v[34:35], v[44:45], v[214:215] op_sel_hi:[1,0,1]\n"
    "v_exp_f32 v208, v32\n"
    "v_exp_f32 v209, v33\n"
    "v_exp_f32 v210, v34\n"
    "v_exp_f32 v211, v35\n"
    "v_pk_fma_f32 v[208:209], v[208:209], s[44:45], s[44:45]\n"
    "v_pk_fma_f32 v[210:211], v[210:211], s[44:45], s[44:45]\n"
    "v_pk_fma_f32 v[208:209], v[208:209], v[208:209], s[46:47]\n"
    "v_pk_fma_f32 v[210:211], v[210:211], v[210:211], s[46:47]\n"
    "v_rcp_f32 v208, v208\n"
    "v_rcp_f32 v209, v209\n"
    "v_rcp_f32 v210, v210\n"
    "v_rcp_f32 v211, v211\n"
    "v_pk_fma_f32 v[32:33], v[32:33], v[208:209], v[32:33] neg_lo:[1,0,0] neg_hi:[1,0,0]\n"
    "v_pk_fma_f32 v[34:35], v[34:35], v[210:211], v[34:35] neg_lo:[1,0,0] neg_hi:[1,0,0]\n"
    "v_cvt_pk_bf16_f32 v224, v32, v33\n"
    "v_cvt_pk_bf16_f32 v225, v34, v35\n"
    "ds_read_b128 v[208:211], v112 offset:43008\n"
    "ds_read_b128 v[212:215], v112 offset:43024\n"
    "s_waitcnt lgkmcnt(2)\n"
    "v_pk_fma_f32 v[220:221], v[216:217], v[44:45], v[220:221] op_sel:[0,1,0] op_sel_hi:[1,1,1]\n"
    "v_pk_fma_f32 v[222:223], v[218:219], v[44:45], v[222:223] op_sel:[0,1,0] op_sel_hi:[1,1,1]\n"
    "v_pk_fma_f32 v[36:37], v[36:37], v[44:45], v[220:221] op_sel_hi:[1,0,1]\n"
    "v_pk_fma_f32 v[38:39], v[38:39], v[44:45], v[222:223] op_sel_hi:[1,0,1]\n"
    "v_exp_f32 v216, v36\n"
    "v_exp_f32 v217, v37\n"
    "v_exp_f32 v218, v38\n"
    "v_exp_f32 v219, v39\n"
    "v_pk_fma_f32 v[216:217], v[216:217], s[44:45], s[44:45]\n"
    "v_pk_fma_f32 v[218:219], v[218:219], s[44:45], s[44:45]\n"
    "v_pk_fma_f32 v[216:217], v[216:217], v[216:217], s[46:47]\n"
    "v_pk_fma_f32 v[218:219], v[218:219], v[218:219], s[46:47]\n"
    "v_rcp_f32 v216, v216\n"
    "v_rcp_f32 v217, v217\n"
    "v_rcp_f32 v218, v218\n"
    "v_rcp_f32 v219, v219\n"
    "v_pk_fma_f32 v[36:37], v[36:37], v[216:217], v[36:37] neg_lo:[1,0,0] neg_hi:[1,0,0]\n"
    "v_pk_fma_f32 v[38:39], v[38:39], v[218:219], v[38:39] neg_lo:[1,0,0] neg_hi:[1,0,0]\n"
    "v_cvt_pk_bf16_f32 v226, v36, v37\n"
    "v_cvt_pk_bf16_f32 v227, v38, v39\n"
    "buffer_store_dwordx4 v[224:227], v1, s[12:15], s91 offen offset:2048 sc1\n"
    "s_waitcnt lgkmcnt(0)\n"
    "v_pk_fma_f32 v[212:213], v[208:209], v[44:45], v[212:213] op_sel:[0,1,0] op_sel_hi:[1,1,1]\n"
    "v_pk_fma_f32 v[214:215], v[210:211], v[44:45], v[214:215] op_sel:[0,1,0] op_sel_hi:[1,1,1]\n"
    "v_pk_fma_f32 v[40:41], v[40:41], v[44:45], v[212:213] op_sel_hi:[1,0,1]\n"
    "v_pk_fma_f32 v[42:43], v[42:43], v[44:45], v[214:215] op_sel_hi:[1,0,1]\n"
    "v_exp_f32 v208, v40\n"
    "v_exp_f32 v209, v41\n"
    "v_exp_f32 v210, v42\n"
    "v_exp_f32 v211, v43\n"
    "v_pk_fma_f32 v[208:209], v[208:209], s[44:45], s[44:45]\n"
    "v_pk_fma_f32 v[210:211], v[210:211], s[44:45], s[44:45]\n"
    "v_pk_fma_f32 v[208:209], v[208:209], v[208:209], s[46:47]\n"
    "v_pk_fma_f32 v[210:211], v[210:211], v[210:211], s[46:47]\n"
    "v_rcp_f32 v208, v208\n"
    "v_rcp_f32 v209, v209\n"
    "v_rcp_f32 v210, v210\n"
    "v_rcp_f32 v211, v211\n"
    "v_pk_fma_f32 v[40:41], v[40:41], v[208:209], v[40:41] neg_lo:[1,0,0] neg_hi:[1,0,0]\n"
    "v_pk_fma_f32 v[42:43], v[42:43], v[210:211], v[42:43] neg_lo:[1,0,0] neg_hi:[1,0,0]\n"
    "v_cvt_pk_bf16_f32 v228, v40, v41\n"
    "v_cvt_pk_bf16_f32 v229, v42, v43\n"
    "v_cndmask_b32 v228, v228, 0, s[100:101]\n"
    "v_cndmask_b32 v229, v229, 0, s[100:101]\n"
    "v_mov_b32 v230, v228\n"
    "v_mov_b32 v231, v229\n"
    "s_nop 0\n"
    "v_permlane16_swap_b32 v228, v230\n"
    "v_permlane16_swap_b32 v229, v231\n"
    "buffer_store_dwordx4 v[228:231], v115, s[12:15], s90 offen sc1\n"
    "s_waitcnt vmcnt(14)\n"
    "v_mfma_f32_16x16x32_bf16 v[16:19], v[96:99], v[128:131], 0\n"
    "ds_read_b128 v[88:91], v5 offset:28672\n"
    "v_and_b32 v12, 1, v0\n"
    "v_mfma_f32_16x16x32_bf16 v[20:23], v[100:103], v[128:131], 0\n"
    "ds_read_b128 v[92:95], v5 offset:29696\n"
    "v_mul_u32_u24 v12, 384, v12\n"
    "v_mfma_f32_16x16x32_bf16 v[24:27], v[104:107], v[128:131], 0\n"
    "ds_read_b128 v[96:99], v5 offset:30720\n"
    "v_add_u32 v12, s58, v12\n"
    "v_mfma_f32_16x16x32_bf16 v[28:31], v[108:111], v[128:131], 0\n"
    "ds_read_b128 v[100:103], v5 offset:31744\n"
    "v_mfma_f32_16x16x32_bf16 v[32:35], v[48:51], v[128:131], 0\n"
    "ds_read_b128 v[104:107], v5 offset:32768\n"
    "buffer_load_dwordx4 v[172:175], v15, s[12:15], s80 offen offset:3072 sc1\n"
    "v_mfma_f32_16x16x32_bf16 v[36:39], v[52:55], v[128:131], 0\n"
    "ds_read_b128 v[108:111], v5 offset:33792\n"
    "v_mfma_f32_16x16x32_bf16 v[40:43], v[56:59], v[128:131], 0\n"
    "ds_read_b128 v[48:51], v5 offset:34816\n"
    "v_mfma_f32_16x16x32_bf16 v[44:47], v[128:131], v[128:131], 0\n"
    "s_waitcnt vmcnt(14)\n"
    "v_mfma_f32_16x16x32_bf16 v[16:19], v[60:63], v[132:135], v[16:19]\n"
    "ds_read_b128 v[52:55], v6 offset:28672\n"
    "buffer_load_dwordx4 v[176:179], v15, s[12:15], s81 offen offset:0 sc1\n"
    "v_mfma_f32_16x16x32_bf16 v[20:23], v[64:67], v[132:135], v[20:23]\n"
    "ds_read_b128 v[56:59], v6 offset:29696\n"
    "v_mfma_f32_16x16x32_bf16 v[24:27], v[68:71], v[132:135], v[24:27]\n"
    "ds_read_b128 v[60:63], v6 offset:30720\n"
    "v_mfma_f32_16x16x32_bf16 v[28:31], v[72:75], v[132:135], v[28:31]\n"
    "ds_read_b128 v[64:67], v6 offset:31744\n"
    "v_mfma_f32_16x16x32_bf16 v[32:35], v[76:79], v[132:135], v[32:35]\n"
    "ds_read_b128 v[68:71], v6 offset:32768\n"
    "buffer_load_dwordx4 v[180:183], v15, s[12:15], s81 offen offset:1024 sc1\n"
    "v_mfma_f32_16x16x32_bf16 v[36:39], v[80:83], v[132:135], v[36:39]\n"
    "ds_read_b128 v[72:75], v6 offset:33792\n"
    "v_mfma_f32_16x16x32_bf16 v[40:43], v[84:87], v[132:135], v[40:43]\n"
    "ds_read_b128 v[76:79], v6 offset:34816\n"
    "v_mfma_f32_16x16x32_bf16 v[44:47], v[132:135], v[132:135], v[44:47]\n"
    "s_waitcnt vmcnt(15) lgkmcnt(10)\n"
    "v_mfma_f32_16x16x32_bf16 v[16:19], v[88:91], v[136:139], v[16:19]\n"
    "ds_read_b128 v[80:83], v1 offset:0\n"
    "buffer_load_dwordx4 v[184:187], v15, s[12:15], s81 offen offset:2048 sc1\n"
    "v_mfma_f32_16x16x32_bf16 v[20:23], v[92:95], v[136:139], v[20:23]\n"
    "ds_read_b128 v[84:87], v1 offset:1024\n"
    "v_mfma_f32_16x16x32_bf16 v[24:27], v[96:99], v[136:139], v[24:27]\n"
    "ds_read_b128 v[88:91], v1 offset:2048\n"
    "v_mfma_f32_16x16x32_bf16 v[28:31], v[100:103], v[136:139], v[28:31]\n"
    "ds_read_b128 v[92:95], v1 offset:3072\n"
    "s_waitcnt lgkmcnt(11)\n"
    "v_mfma_f32_16x16x32_bf16 v[32:35], v[104:107], v[136:139], v[32:35]\n"
    "ds_read_b128 v[96:99], v1 offset:4096\n"
    "buffer_load_dwordx4 v[188:191], v15, s[12:15], s81 offen offset:3072 sc1\n"
    "v_mfma_f32_16x16x32_bf16 v[36:39], v[108:111], v[136:139], v[36:39]\n"
    "ds_read_b128 v[100:103], v1 offset:5120\n"
    "v_mfma_f32_16x16x32_bf16 v[40:43], v[48:51], v[136:139], v[40:43]\n"
    "ds_read_b128 v[104:107], v1 offset:6144\n"
    "v_mfma_f32_16x16x32_bf16 v[44:47], v[136:139], v[136:139], v[44:47]\n"
    "s_waitcnt vmcnt(16) lgkmcnt(10)\n"
    "v_mfma_f32_16x16x32_bf16 v[16:19], v[52:55], v[140:143], v[16:19]\n"
    "ds_read_b128 v[108:111], v4 offset:0\n"
    "buffer_load_dwordx4 v[192:195], v15, s[12:15], s82 offen offset:0 sc1\n"
    "v_mfma_f32_16x16x32_bf16 v[20:23], v[56:59], v[140:143], v[20:23]\n"
    "ds_read_b128 v[48:51], v4 offset:1024\n"
    "v_mfma_f32_16x16x32_bf16 v[24:27], v[60:63], v[140:143], v[24:27]\n"
    "ds_read_b128 v[52:55], v4 offset:2048\n"
    "v_mfma_f32_16x16x32_bf16 v[28:31], v[64:67], v[140:143], v[28:31]\n"
    "ds_read_b128 v[56:59], v4 offset:3072\n"
    "s_waitcnt lgkmcnt(11)\n"
    "v_mfma_f32_16x16x32_bf16 v[32:35], v[68:71], v[140:143], v[32:35]\n"
    "ds_read_b128 v[60:63], v4 offset:4096\n"
    "buffer_load_dwordx4 v[196:199], v15, s[12:15], s82 offen offset:1024 sc1\n"
    "v_mfma_f32_16x16x32_bf16 v[36:39], v[72:75], v[140:143], v[36:39]\n"
    "ds_read_b128 v[64:67], v4 offset:5120\n"
    "v_mfma_f32_16x16x32_bf16 v[40:43], v[76:79], v[140:143], v[40:43]\n"
    "ds_read_b128 v[68:71], v4 offset:6144\n"
    "v_mfma_f32_16x16x32_bf16 v[44:47], v[140:143], v[140:143], v[44:47]\n"
    "s_waitcnt vmcnt(17) lgkmcnt(10)\n"
    "v_mfma_f32_16x16x32_bf16 v[16:19], v[80:83], v[144:147], v[16:19]\n"
    "ds_read_b128 v[72:75], v5 offset:0\n"
    "buffer_load_dwordx4 v[200:203], v15, s[12:15], s82 offen offset:2048 sc1\n"
    "v_mfma_f32_16x16x32_bf16 v[20:23], v[84:87], v[144:147], v[20:23]\n"
    "ds_read_b128 v[76:79], v5 offset:1024\n"
    "v_mfma_f32_16x16x32_bf16 v[24:27], v[88:91], v[144:147], v[24:27]\n"
    "ds_read_b128 v[80:83], v5 offset:2048\n"
    "v_mfma_f32_16x16x32_bf16 v[28:31], v[92:95], v[144:147], v[28:31]\n"
    "ds_read_b128 v[84:87], v5 offset:3072\n"
    "s_waitcnt lgkmcnt(11)\n"
    "v_mfma_f32_16x16x32_bf16 v[32:35], v[96:99], v[144:147], v[32:35]\n"
    "ds_read_b128 v[88:91], v5 offset:4096\n"
    "buffer_load_dwordx4 v[204:207], v15, s[12:15], s82 offen offset:3072 sc1\n"
    "v_mfma_f32_16x16x32_bf16 v[36:39], v[100:103], v[144:147], v[36:39]\n"
    "ds_read_b128 v[92:95], v5 offset:5120\n"
    "v_mfma_f32_16x16x32_bf16 v[40:43], v[104:107], v[144:147], v[40:43]\n"
    "ds_read_b128 v[96:99], v5 offset:6144\n"
    "v_mfma_f32_16x16x32_bf16 v[44:47], v[144:147], v[144:147], v[44:47]\n"
    "s_waitcnt vmcnt(18) lgkmcnt(10)\n"
    "s_waitcnt vmcnt(9)\n"
    "v_mfma_f32_16x16x32_bf16 v[16:19], v[108:111], v[148:151], v[16:19]\n"
    "ds_read_b128 v[100:103], v6 offset:0\n"
    "s_add_u32 s51, s50, s52\n"
    "v_mfma_f32_16x16x32_bf16 v[20:23], v[48:51], v[148:151], v[20:23]\n"
    "ds_read_b128 v[104:107], v6 offset:1024\n"
    "s_mov_b64 exec, 1\n"
    "buffer_atomic_add v123, v1, s[12:15], s51 offen offset:0\n"
    "s_mov_b64 exec, -1\n"
    "v_mfma_f32_16x16x32_bf16 v[24:27], v[52:55], v[148:151], v[24:27]\n"
    "ds_read_b128 v[108:111], v6 offset:2048\n"
    "v_mfma_f32_16x16x32_bf16 v[28:31], v[56:59], v[148:151], v[28:31]\n"
    "ds_read_b128 v[48:51], v6 offset:3072\n"
    "s_waitcnt lgkmcnt(11)\n"
    "v_mfma_f32_16x16x32_bf16 v[32:35], v[60:63], v[148:151], v[32:35]\n"
    "ds_read_b128 v[52:55], v6 offset:4096\n"
    "v_mfma_f32_16x16x32_bf16 v[36:39], v[64:67], v[148:151], v[36:39]\n"
    "ds_read_b128 v[56:59], v6 offset:5120\n"
    "v_mfma_f32_16x16x32_bf16 v[40:43], v[68:71], v[148:151], v[40:43]\n"
    "ds_read_b128 v[60:63], v6 offset:6144\n"
    "v_mfma_f32_16x16x32_bf16 v[44:47], v[148:151], v[148:151], v[44:47]\n"
    "s_waitcnt lgkmcnt(10)\n"
    "v_mfma_f32_16x16x32_bf16 v[16:19], v[72:75], v[152:155], v[16:19]\n"
    "ds_read_b128 v[64:67], v1 offset:7168\n"
    "v_mfma_f32_16x16x32_bf16 v[20:23], v[76:79], v[152:155], v[20:23]\n"
    "ds_read_b128 v[68:71], v1 offset:8192\n"
    "v_mfma_f32_16x16x32_bf16 v[24:27], v[80:83], v[152:155], v[24:27]\n"
    "ds_read_b128 v[72:75], v1 offset:9216\n"
    "v_mfma_f32_16x16x32_bf16 v[28:31], v[84:87], v[152:155], v[28:31]\n"
    "ds_read_b128 v[76:79], v1 offset:10240\n"
    "s_waitcnt lgkmcnt(11)\n"
    "v_mfma_f32_16x16x32_bf16 v[32:35], v[88:91], v[152:155], v[32:35]\n"
    "ds_read_b128 v[80:83], v1 offset:11264\n"
    "v_mfma_f32_16x16x32_bf16 v[36:39], v[92:95], v[152:155], v[36:39]\n"
    "ds_read_b128 v[84:87], v1 offset:12288\n"
    "v_mfma_f32_16x16x32_bf16 v[40:43], v[96:99], v[152:155], v[40:43]\n"
    "ds_read_b128 v[88:91], v1 offset:13312\n"
    "v_mfma_f32_16x16x32_bf16 v[44:47], v[152:155], v[152:155], v[44:47]\n"
    "s_waitcnt lgkmcnt(10)\n"
    "v_mfma_f32_16x16x32_bf16 v[16:19], v[100:103], v[156:159], v[16:19]\n"
    "ds_read_b128 v[92:95], v4 offset:7168\n"
    "v_mfma_f32_16x16x32_bf16 v[20:23], v[104:107], v[156:159], v[20:23]\n"
    "ds_read_b128 v[96:99], v4 offset:8192\n"
    "v_mfma_f32_16x16x32_bf16 v[24:27], v[108:111], v[156:159], v[24:27]\n"
    "ds_read_b128 v[100:103], v4 offset:9216\n"
    "v_mfma_f32_16x16x32_bf16 v[28:31], v[48:51], v[156:159], v[28:31]\n"
    "ds_read_b128 v[104:107], v4 offset:10240\n"
    "s_waitcnt lgkmcnt(11)\n"
    "v_mfma_f32_16x16x32_bf16 v[32:35], v[52:55], v[156:159], v[32:35]\n"
    "ds_read_b128 v[108:111], v4 offset:11264\n"
    "s_lshl_b32 s83, s9, 10\n"
    "v_mfma_f32_16x16x32_bf16 v[36:39], v[56:59], v[156:159], v[36:39]\n"
    "ds_read_b128 v[48:51], v4 offset:12288\n"
    "s_add_u32 s83, s83, 143360\n"
    "v_mfma_f32_16x16x32_bf16 v[40:43], v[60:63], v[156:159], v[40:43]\n"
    "ds_read_b128 v[52:55], v4 offset:13312\n"
    "v_add_u32 v8, s83, v1\n"
    "v_mfma_f32_16x16x32_bf16 v[44:47], v[156:159], v[156:159], v[44:47]\n"
    "s_waitcnt lgkmcnt(10)\n"
    "s_and_b32 s86, s55, 7\n"
    "v_mfma_f32_16x16x32_bf16 v[16:19], v[64:67], v[160:163], v[16:19]\n"
    "ds_read_b128 v[56:59], v5 offset:7168\n"
    "s_mul_i32 s86, s86, 5\n"
    "v_mfma_f32_16x16x32_bf16 v[20:23], v[68:71], v[160:163], v[20:23]\n"
    "ds_read_b128 v[60:63], v5 offset:8192\n"
    "s_sub_u32 s84, s19, s86\n"
    "v_mfma_f32_16x16x32_bf16 v[24:27], v[72:75], v[160:163], v[24:27]\n"
    "ds_read_b128 v[64:67], v5 offset:9216\n"
    "s_sub_u32 s85, s20, s86\n"
    "v_mfma_f32_16x16x32_bf16 v[28:31], v[76:79], v[160:163], v[28:31]\n"
    "ds_read_b128 v[68:71], v5 offset:10240\n"
    "s_waitcnt lgkmcnt(11)\n"
    "v_mfma_f32_16x16x32_bf16 v[32:35], v[80:83], v[160:163], v[32:35]\n"
    "ds_read_b128 v[72:75], v5 offset:11264\n"
    "s_lshl_b32 s83, s84, 12\n"
    "v_mfma_f32_16x16x32_bf16 v[36:39], v[84:87], v[160:163], v[36:39]\n"
    "ds_read_b128 v[76:79], v5 offset:12288\n"
    "s_add_u32 s83, s83, 143360\n"
    "v_mfma_f32_16x16x32_bf16 v[40:43], v[88:91], v[160:163], v[40:43]\n"
    "ds_read_b128 v[80:83], v5 offset:13312\n"
    "v_add_u32 v9, s83, v1\n"
    "v_mfma_f32_16x16x32_bf16 v[44:47], v[160:163], v[160:163], v[44:47]\n"
    "s_waitcnt lgkmcnt(10)\n"
    "s_lshl_b32 s83, s85, 12\n"
    "v_mfma_f32_16x16x32_bf16 v[16:19], v[92:95], v[164:167], v[16:19]\n"
    "ds_read_b128 v[84:87], v6 offset:7168\n"
    "s_add_u32 s83, s83, 143360\n"
    "v_mfma_f32_16x16x32_bf16 v[20:23], v[96:99], v[164:167], v[20:23]\n"
    "ds_read_b128 v[88:91], v6 offset:8192\n"
    "v_add_u32 v10, s83, v1\n"
    "v_mfma_f32_16x16x32_bf16 v[24:27], v[100:103], v[164:167], v[24:27]\n"
    "ds_read_b128 v[92:95], v6 offset:9216\n"
    "s_cmp_eq_u32 s16, 0\n"
    "s_cselect_b32 s84, 2147418112, 0\n"
    "v_mfma_f32_16x16x32_bf16 v[28:31], v[104:107], v[164:167], v[28:31]\n"
    "ds_read_b128 v[96:99], v6 offset:10240\n"
    "s_waitcnt lgkmcnt(11)\n"
    "v_mfma_f32_16x16x32_bf16 v[32:35], v[108:111], v[164:167], v[32:35]\n"
    "ds_read_b128 v[100:103], v6 offset:11264\n"
    "s_cmp_eq_u32 s17, 0\n"
    "s_cselect_b32 s85, 2147418112, 0\n"
    "v_mfma_f32_16x16x32_bf16 v[36:39], v[48:51], v[164:167], v[36:39]\n"
    "ds_read_b128 v[104:107], v6 offset:12288\n"
    "v_and_b32 v125, 15, v0\n"
    "v_mfma_f32_16x16x32_bf16 v[40:43], v[52:55], v[164:167], v[40:43]\n"
    "ds_read_b128 v[108:111], v6 offset:13312\n"
    "v_lshrrev_b32 v126, 4, v0\n"
    "v_mfma_f32_16x16x32_bf16 v[44:47], v[164:167], v[164:167], v[44:47]\n"
    "s_waitcnt lgkmcnt(10)\n"
    "v_lshrrev_b32 v121, 1, v126\n"
    "v_mfma_f32_16x16x32_bf16 v[16:19], v[56:59], v[168:171], v[16:19]\n"
    "ds_read_b128 v[48:51], v1 offset:14336\n"
    "v_lshlrev_b32 v120, 8, v121\n"
    "v_mfma_f32_16x16x32_bf16 v[20:23], v[60:63], v[168:171], v[20:23]\n"
    "ds_read_b128 v[52:55], v1 offset:15360\n"
    "v_lshl_add_u32 v120, v125, 4, v120\n"
    "v_mfma_f32_16x16x32_bf16 v[24:27], v[64:67], v[168:171], v[24:27]\n"
    "ds_read_b128 v[56:59], v1 offset:16384\n"
    "v_add_u32 v120, s21, v120\n"
    "v_mfma_f32_16x16x32_bf16 v[28:31], v[68:71], v[168:171], v[28:31]\n"
    "ds_read_b128 v[60:63], v1 offset:17408\n"
    "s_waitcnt lgkmcnt(11)\n"
    "v_mfma_f32_16x16x32_bf16 v[32:35], v[72:75], v[168:171], v[32:35]\n"
    "ds_read_b128 v[64:67], v1 offset:18432\n"
    "v_and_b32 v122, 1, v126\n"
    "v_mfma_f32_16x16x32_bf16 v[36:39], v[76:79], v[168:171], v[36:39]\n"
    "ds_read_b128 v[68:71], v1 offset:19456\n"
    "v_lshl_add_u32 v13, v122, 3, v120\n"
    "v_mfma_f32_16x16x32_bf16 v[40:43], v[80:83], v[168:171], v[40:43]\n"
    "ds_read_b128 v[72:75], v1 offset:20480\n"
    "s_cmp_ge_u32 s9, 2\n"
    "s_cselect_b64 vcc, -1, 0\n"
    "v_cndmask_b32 v13, v1, v13, vcc\n"
    "v_mfma_f32_16x16x32_bf16 v[44:47], v[168:171], v[168:171], v[44:47]\n"
    "s_waitcnt vmcnt(9) lgkmcnt(10)\n"
    "v_mul_u32_u24 v125, 1280, v125\n"
    "v_mfma_f32_16x16x32_bf16 v[16:19], v[84:87], v[172:175], v[16:19]\n"
    "ds_read_b128 v[76:79], v4 offset:14336\n"
    "v_lshl_add_u32 v125, v121, 4, v125\n"
    "v_mfma_f32_16x16x32_bf16 v[20:23], v[88:91], v[172:175], v[20:23]\n"
    "ds_read_b128 v[80:83], v4 offset:15360\n"
    "s_lshl_b32 s83, s19, 5\n"
    "v_mfma_f32_16x16x32_bf16 v[24:27], v[92:95], v[172:175], v[24:27]\n"
    "ds_read_b128 v[84:87], v4 offset:16384\n"
    "s_add_u32 s84, s84, s83\n"
    "v_mfma_f32_16x16x32_bf16 v[28:31], v[96:99], v[172:175], v[28:31]\n"
    "ds_read_b128 v[88:91], v4 offset:17408\n"
    "s_waitcnt lgkmcnt(11)\n"
    "v_mfma_f32_16x16x32_bf16 v[32:35], v[100:103], v[172:175], v[32:35]\n"
    "ds_read_b128 v[92:95], v4 offset:18432\n"
    "s_lshl_b32 s83, s20, 5\n"
    "v_mfma_f32_16x16x32_bf16 v[36:39], v[104:107], v[172:175], v[36:39]\n"
    "ds_read_b128 v[96:99], v4 offset:19456\n"
    "s_add_u32 s85, s85, s83\n"
    "v_mfma_f32_16x16x32_bf16 v[40:43], v[108:111], v[172:175], v[40:43]\n"
    "ds_read_b128 v[100:103], v4 offset:20480\n"
    "v_mov_b32 v120, s84\n"
    "v_mfma_f32_16x16x32_bf16 v[44:47], v[172:175], v[172:175], v[44:47]\n"
    "s_waitcnt vmcnt(8) lgkmcnt(10)\n"
    "v_mov_b32 v121, s85\n"
    "v_mfma_f32_16x16x32_bf16 v[16:19], v[48:51], v[176:179], v[16:19]\n"
    "ds_read_b128 v[104:107], v5 offset:14336\n"
    "v_cmp_eq_u32 vcc, 1, v122\n"
    "s_nop 1\n"
    "v_cndmask_b32 v120, v120, v121, vcc\n"
    "v_mfma_f32_16x16x32_bf16 v[20:23], v[52:55], v[176:179], v[20:23]\n"
    "ds_read_b128 v[108:111], v5 offset:15360\n"
    "v_add_u32 v120, v120, v125\n"
    "v_mfma_f32_16x16x32_bf16 v[24:27], v[56:59], v[176:179], v[24:27]\n"
    "ds_read_b128 v[48:51], v5 offset:16384\n"
    "v_mfma_f32_16x16x32_bf16 v[28:31], v[60:63], v[176:179], v[28:31]\n"
    "ds_read_b128 v[52:55], v5 offset:17408\n"
    "s_waitcnt lgkmcnt(11)\n"
    "v_mfma_f32_16x16x32_bf16 v[32:35], v[64:67], v[176:179], v[32:35]\n"
    "ds_read_b128 v[56:59], v5 offset:18432\n"
    "v_mfma_f32_16x16x32_bf16 v[36:39], v[68:71], v[176:179], v[36:39]\n"
    "ds_read_b128 v[60:63], v5 offset:19456\n"
    "v_mfma_f32_16x16x32_bf16 v[40:43], v[72:75], v[176:179], v[40:43]\n"
    "ds_read_b128 v[64:67], v5 offset:20480\n"
    "v_mfma_f32_16x16x32_bf16 v[44:47], v[176:179], v[176:179], v[44:47]\n"
    "s_waitcnt vmcnt(7) lgkmcnt(10)\n"
    "v_mfma_f32_16x16x32_bf16 v[16:19], v[76:79], v[180:183], v[16:19]\n"
    "ds_read_b128 v[68:71], v6 offset:14336\n"
    "v_mfma_f32_16x16x32_bf16 v[20:23], v[80:83], v[180:183], v[20:23]\n"
    "ds_read_b128 v[72:75], v6 offset:15360\n"
    "v_mfma_f32_16x16x32_bf16 v[24:27], v[84:87], v[180:183], v[24:27]\n"
    "ds_read_b128 v[76:79], v6 offset:16384\n"
    "v_mfma_f32_16x16x32_bf16 v[28:31], v[88:91], v[180:183], v[28:31]\n"
    "ds_read_b128 v[80:83], v6 offset:17408\n"
    "s_waitcnt lgkmcnt(11)\n"
    "v_mfma_f32_16x16x32_bf16 v[32:35], v[92:95], v[180:183], v[32:35]\n"
    "ds_read_b128 v[84:87], v6 offset:18432\n"
    "v_mfma_f32_16x16x32_bf16 v[36:39], v[96:99], v[180:183], v[36:39]\n"
    "ds_read_b128 v[88:91], v6 offset:19456\n"
    "v_mfma_f32_16x16x32_bf16 v[40:43], v[100:103], v[180:183], v[40:43]\n"
    "ds_read_b128 v[92:95], v6 offset:20480\n"
    "v_mfma_f32_16x16x32_bf16 v[44:47], v[180:183], v[180:183], v[44:47]\n"
    "s_waitcnt vmcnt(6) lgkmcnt(10)\n"
    "v_mfma_f32_16x16x32_bf16 v[16:19], v[104:107], v[184:187], v[16:19]\n"
    "ds_read_b128 v[96:99], v1 offset:21504\n"
    "v_mfma_f32_16x16x32_bf16 v[20:23], v[108:111], v[184:187], v[20:23]\n"
    "ds_read_b128 v[100:103], v1 offset:22528\n"
    "v_mfma_f32_16x16x32_bf16 v[24:27], v[48:51], v[184:187], v[24:27]\n"
    "ds_read_b128 v[104:107], v1 offset:23552\n"
    "v_mfma_f32_16x16x32_bf16 v[28:31], v[52:55], v[184:187], v[28:31]\n"
    "ds_read_b128 v[108:111], v1 offset:24576\n"
    "s_waitcnt lgkmcnt(11)\n"
    "v_mfma_f32_16x16x32_bf16 v[32:35], v[56:59], v[184:187], v[32:35]\n"
    "ds_read_b128 v[48:51], v1 offset:25600\n"
    "buffer_load_dword v117, v12, s[12:15], s52 offen offset:0 sc1\n"
    "v_mfma_f32_16x16x32_bf16 v[36:39], v[60:63], v[184:187], v[36:39]\n"
    "ds_read_b128 v[52:55], v1 offset:26624\n"
    "v_mfma_f32_16x16x32_bf16 v[40:43], v[64:67], v[184:187], v[40:43]\n"
    "ds_read_b128 v[56:59], v1 offset:27648\n"
    "v_mfma_f32_16x16x32_bf16 v[44:47], v[184:187], v[184:187], v[44:47]\n"
    "s_waitcnt vmcnt(6) lgkmcnt(10)\n"
    "v_mfma_f32_16x16x32_bf16 v[16:19], v[68:71], v[188:191], v[16:19]\n"
    "ds_read_b128 v[60:63], v4 offset:21504\n"
    "v_mfma_f32_16x16x32_bf16 v[20:23], v[72:75], v[188:191], v[20:23]\n"
    "ds_read_b128 v[64:67], v4 offset:22528\n"
    "v_mfma_f32_16x16x32_bf16 v[24:27], v[76:79], v[188:191], v[24:27]\n"
    "ds_read_b128 v[68:71], v4 offset:23552\n"
    "v_mfma_f32_16x16x32_bf16 v[28:31], v[80:83], v[188:191], v[28:31]\n"
    "ds_read_b128 v[72:75], v4 offset:24576\n"
    "s_waitcnt lgkmcnt(11)\n"
    "v_mfma_f32_16x16x32_bf16 v[32:35], v[84:87], v[188:191], v[32:35]\n"
    "ds_read_b128 v[76:79], v4 offset:25600\n"
    "v_mfma_f32_16x16x32_bf16 v[36:39], v[88:91], v[188:191], v[36:39]\n"
    "ds_read_b128 v[80:83], v4 offset:26624\n"
    "v_mfma_f32_16x16x32_bf16 v[40:43], v[92:95], v[188:191], v[40:43]\n"
    "ds_read_b128 v[84:87], v4 offset:27648\n"
    "v_mfma_f32_16x16x32_bf16 v[44:47], v[188:191], v[188:191], v[44:47]\n"
    "s_waitcnt vmcnt(5) lgkmcnt(10)\n"
    "v_mfma_f32_16x16x32_bf16 v[16:19], v[96:99], v[192:195], v[16:19]\n"
    "ds_read_b128 v[88:91], v5 offset:21504\n"
    "v_mfma_f32_16x16x32_bf16 v[20:23], v[100:103], v[192:195], v[20:23]\n"
    "ds_read_b128 v[92:95], v5 offset:22528\n"
    "v_mfma_f32_16x16x32_bf16 v[24:27], v[104:107], v[192:195], v[24:27]\n"
    "ds_read_b128 v[96:99], v5 offset:23552\n"
    "v_mfma_f32_16x16x32_bf16 v[28:31], v[108:111], v[192:195], v[28:31]\n"
    "ds_read_b128 v[100:103], v5 offset:24576\n"
    "s_waitcnt lgkmcnt(11)\n"
    "v_mfma_f32_16x16x32_bf16 v[32:35], v[48:51], v[192:195], v[32:35]\n"
    "ds_read_b128 v[104:107], v5 offset:25600\n"
    "v_mfma_f32_16x16x32_bf16 v[36:39], v[52:55], v[192:195], v[36:39]\n"
    "ds_read_b128 v[108:111], v5 offset:26624\n"
    "v_mfma_f32_16x16x32_bf16 v[40:43], v[56:59], v[192:195], v[40:43]\n"
    "ds_read_b128 v[48:51], v5 offset:27648\n"
    "v_mfma_f32_16x16x32_bf16 v[44:47], v[192:195], v[192:195], v[44:47]\n"
    "s_waitcnt vmcnt(4) lgkmcnt(10)\n"
    "v_mfma_f32_16x16x32_bf16 v[16:19], v[60:63], v[196:199], v[16:19]\n"
    "ds_read_b128 v[52:55], v6 offset:21504\n"
    "v_mfma_f32_16x16x32_bf16 v[20:23], v[64:67], v[196:199], v[20:23]\n"
    "ds_read_b128 v[56:59], v6 offset:22528\n"
    "v_mfma_f32_16x16x32_bf16 v[24:27], v[68:71], v[196:199], v[24:27]\n"
    "ds_read_b128 v[60:63], v6 offset:23552\n"
    "v_mfma_f32_16x16x32_bf16 v[28:31], v[72:75], v[196:199], v[28:31]\n"
    "ds_read_b128 v[64:67], v6 offset:24576\n"
    "s_waitcnt lgkmcnt(11)\n"
    "v_mfma_f32_16x16x32_bf16 v[32:35], v[76:79], v[196:199], v[32:35]\n"
    "ds_read_b128 v[68:71], v6 offset:25600\n"
    "v_mfma_f32_16x16x32_bf16 v[36:39], v[80:83], v[196:199], v[36:39]\n"
    "ds_read_b128 v[72:75], v6 offset:26624\n"
    "v_mfma_f32_16x16x32_bf16 v[40:43], v[84:87], v[196:199], v[40:43]\n"
    "ds_read_b128 v[76:79], v6 offset:27648\n"
    "v_mfma_f32_16x16x32_bf16 v[44:47], v[196:199], v[196:199], v[44:47]\n"
    "s_waitcnt vmcnt(3) lgkmcnt(10)\n"
    "v_mfma_f32_16x16x32_bf16 v[16:19], v[88:91], v[200:203], v[16:19]\n"
    "v_mfma_f32_16x16x32_bf16 v[20:23], v[92:95], v[200:203], v[20:23]\n"
    "v_mfma_f32_16x16x32_bf16 v[24:27], v[96:99], v[200:203], v[24:27]\n"
    "v_mfma_f32_16x16x32_bf16 v[28:31], v[100:103], v[200:203], v[28:31]\n"
    "s_waitcnt lgkmcnt(7)\n"
    "v_mfma_f32_16x16x32_bf16 v[32:35], v[104:107], v[200:203], v[32:35]\n"
    "v_mfma_f32_16x16x32_bf16 v[36:39], v[108:111], v[200:203], v[36:39]\n"
    "v_mfma_f32_16x16x32_bf16 v[40:43], v[48:51], v[200:203], v[40:43]\n"
    "v_mfma_f32_16x16x32_bf16 v[44:47], v[200:203], v[200:203], v[44:47]\n"
    "s_waitcnt vmcnt(2) lgkmcnt(3)\n"
    "v_mfma_f32_16x16x32_bf16 v[16:19], v[52:55], v[204:207], v[16:19]\n"
    "v_mfma_f32_16x16x32_bf16 v[20:23], v[56:59], v[204:207], v[20:23]\n"
    "v_mfma_f32_16x16x32_bf16 v[24:27], v[60:63], v[204:207], v[24:27]\n"
    "v_mfma_f32_16x16x32_bf16 v[28:31], v[64:67], v[204:207], v[28:31]\n"
    "s_waitcnt lgkmcnt(0)\n"
    "v_mfma_f32_16x16x32_bf16 v[32:35], v[68:71], v[204:207], v[32:35]\n"
    "v_mfma_f32_16x16x32_bf16 v[36:39], v[72:75], v[204:207], v[36:39]\n"
    "v_mfma_f32_16x16x32_bf16 v[40:43], v[76:79], v[204:207], v[40:43]\n"
    "v_mfma_f32_16x16x32_bf16 v[44:47], v[204:207], v[204:207], v[44:47]\n"
    "s_waitcnt vmcnt(0)\n"
    "v_cmp_ne_u32 vcc, 16, v117\n"
    "s_cbranch_vccnz Lslow_75_%=\n"
    "Lback_76_%=:\n"
    "s_add_u32 s92, s57, 0\n"
    "s_mul_i32 s92, s92, 57344\n"
    "s_add_u32 s92, s92, s49\n"
    "v_add_u32 v14, s92, v1\n"
    "s_add_u32 s92, s57, 1\n"
    "s_mul_i32 s92, s92, 57344\n"
    "s_add_u32 s92, s92, s49\n"
    "v_add_u32 v15, s92, v1\n"
    "ds_read_b128 v[208:211], v112 offset:0\n"
    "ds_read_b128 v[212:215], v112 offset:16\n"
    "s_add_u32 s90, s56, 2\n"
    "s_mul_i32 s90, s90, 57344\n"
    "buffer_load_dwordx4 v[128:131], v14, s[12:15], s64 offen sc1\n"
    "s_add_u32 s90, s90, s49\n"
    "s_mul_i32 s91, s5, 3072\n"
    "s_add_u32 s91, s91, s90\n"
    "v_cndmask_b32 v44, v44, v45, s[32:33]\n"
    "v_cndmask_b32 v44, v44, v46, s[34:35]\n"
    "v_cndmask_b32 v44, v44, v47, s[36:37]\n"
    "ds_bpermute_b32 v46, v114, v40\n"
    "ds_bpermute_b32 v45, v113, v44\n"
    "buffer_load_dwordx4 v[132:135], v14, s[12:15], s65 offen sc1\n"
    "s_waitcnt lgkmcnt(1)\n"
    "v_mul_f32 v46, s39, v46\n"
    "v_mul_f32 v47, v46, v46\n"
    "s_waitcnt lgkmcnt(0)\n"
    "v_fma_f32 v44, v45, s39, -v47\n"
    "v_max_f32 v44, 0, v44\n"
    "v_add_f32 v44, s41, v44\n"
    "v_rsq_f32 v44, v44\n"
    "s_nop 0\n"
    "v_mul_f32 v44, s40, v44\n"
    "buffer_load_dwordx4 v[136:139], v14, s[12:15], s66 offen sc1\n"
    "v_mul_f32 v45, -v46, v44\n"
    "ds_read_b128 v[216:219], v112 offset:7168\n"
    "ds_read_b128 v[220:223], v112 offset:7184\n"
    "v_pk_fma_f32 v[212:213], v[208:209], v[44:45], v[212:213] op_sel:[0,1,0] op_sel_hi:[1,1,1]\n"
    "v_pk_fma_f32 v[214:215], v[210:211], v[44:45], v[214:215] op_sel:[0,1,0] op_sel_hi:[1,1,1]\n"
    "v_pk_fma_f32 v[16:17], v[16:17], v[44:45], v[212:213] op_sel_hi:[1,0,1]\n"
    "v_pk_fma_f32 v[18:19], v[18:19], v[44:45], v[214:215] op_sel_hi:[1,0,1]\n"
    "v_exp_f32 v208, v16\n"
    "buffer_load_dwordx4 v[140:143], v14, s[12:15], s67 offen sc1\n"
    "v_exp_f32 v209, v17\n"
    "v_exp_f32 v210, v18\n"
    "v_exp_f32 v211, v19\n"
    "v_pk_fma_f32 v[208:209], v[208:209], s[44:45], s[44:45]\n"
    "v_pk_fma_f32 v[210:211], v[210:211], s[44:45], s[44:45]\n"
    "buffer_load_dwordx4 v[144:147], v14, s[12:15], s68 offen sc1\n"
    "v_pk_fma_f32 v[208:209], v[208:209], v[208:209], s[46:47]\n"
    "v_pk_fma_f32 v[210:211], v[210:211], v[210:211], s[46:47]\n"
    "v_rcp_f32 v208, v208\n"
    "v_rcp_f32 v209, v209\n"
    "v_rcp_f32 v210, v210\n"
    "buffer_load_dwordx4 v[148:151], v14, s[12:15], s69 offen sc1\n"
    "v_rcp_f32 v211, v211\n"
    "v_pk_fma_f32 v[16:17], v[16:17], v[208:209], v[16:17] neg_lo:[1,0,0] neg_hi:[1,0,0]\n"
    "v_pk_fma_f32 v[18:19], v[18:19], v[210:211], v[18:19] neg_lo:[1,0,0] neg_hi:[1,0,0]\n"
    "v_cvt_pk_bf16_f32 v224, v16, v17\n"
    "v_cvt_pk_bf16_f32 v225, v18, v19\n"
    "ds_read_b128 v[208:211], v112 offset:14336\n"
    "ds_read_b128 v[212:215], v112 offset:14352\n"
    "buffer_load_dwordx4 v[152:155], v14, s[12:15], s70 offen sc1\n"
    "s_waitcnt lgkmcnt(2)\n"
    "v_pk_fma_f32 v[220:221], v[216:217], v[44:45], v[220:221] op_sel:[0,1,0] op_sel_hi:[1,1,1]\n"
    "v_pk_fma_f32 v[222:223], v[218:219], v[44:45], v[222:223] op_sel:[0,1,0] op_sel_hi:[1,1,1]\n"
    "v_pk_fma_f32 v[20:21], v[20:21], v[44:45], v[220:221] op_sel_hi:[1,0,1]\n"
    "v_pk_fma_f32 v[22:23], v[22:23], v[44:45], v[222:223] op_sel_hi:[1,0,1]\n"
    "v_exp_f32 v216, v20\n"
    "v_exp_f32 v217, v21\n"
    "buffer_load_dwordx4 v[156:159], v14, s[12:15], s71 offen sc1\n"
    "v_exp_f32 v218, v22\n"
    "v_exp_f32 v219, v23\n"
    "v_pk_fma_f32 v[216:217], v[216:217], s[44:45], s[44:45]\n"
    "v_pk_fma_f32 v[218:219], v[218:219], s[44:45], s[44:45]\n"
    "v_pk_fma_f32 v[216:217], v[216:217], v[216:217], s[46:47]\n"
    "v_pk_fma_f32 v[218:219], v[218:219], v[218:219], s[46:47]\n"
    "buffer_load_dwordx4 v[160:163], v14, s[12:15], s72 offen sc1\n"
    "v_rcp_f32 v216, v216\n"
    "v_rcp_f32 v217, v217\n"
    "v_rcp_f32 v218, v218\n"
    "v_rcp_f32 v219, v219\n"
    "buffer_load_dwordx4 v[164:167], v14, s[12:15], s73 offen sc1\n"
    "v_pk_fma_f32 v[20:21], v[20:21], v[216:217], v[20:21] neg_lo:[1,0,0] neg_hi:[1,0,0]\n"
    "v_pk_fma_f32 v[22:23], v[22:23], v[218:219], v[22:23] neg_lo:[1,0,0] neg_hi:[1,0,0]\n"
    "v_cvt_pk_bf16_f32 v226, v20, v21\n"
    "v_cvt_pk_bf16_f32 v227, v22, v23\n"
    "buffer_store_dwordx4 v[224:227], v1, s[12:15], s91 offen offset:0 sc1\n"
    "ds_read_b128 v[216:219], v112 offset:21504\n"
    "ds_read_b128 v[220:223], v112 offset:21520\n"
    "s_waitcnt lgkmcnt(2)\n"
    "v_pk_fma_f32 v[212:213], v[208:209], v[44:45], v[212:213] op_sel:[0,1,0] op_sel_hi:[1,1,1]\n"
    "v_pk_fma_f32 v[214:215], v[210:211], v[44:45], v[214:215] op_sel:[0,1,0] op_sel_hi:[1,1,1]\n"
    "v_pk_fma_f32 v[24:25], v[24:25], v[44:45], v[212:213] op_sel_hi:[1,0,1]\n"
    "v_pk_fma_f32 v[26:27], v[26:27], v[44:45], v[214:215] op_sel_hi:[1,0,1]\n"
    "v_exp_f32 v208, v24\n"
    "v_exp_f32 v209, v25\n"
    "v_exp_f32 v210, v26\n"
    "v_exp_f32 v211, v27\n"
    "v_pk_fma_f32 v[208:209], v[208:209], s[44:45], s[44:45]\n"
    "v_pk_fma_f32 v[210:211], v[210:211], s[44:45], s[44:45]\n"
    "v_pk_fma_f32 v[208:209], v[208:209], v[208:209], s[46:47]\n"
    "v_pk_fma_f32 v[210:211], v[210:211], v[210:211], s[46:47]\n"
    "v_rcp_f32 v208, v208\n"
    "v_rcp_f32 v209, v209\n"
    "v_rcp_f32 v210, v210\n"
    "v_rcp_f32 v211, v211\n"
    "v_pk_fma_f32 v[24:25], v[24:25], v[208:209], v[24:25] neg_lo:[1,0,0] neg_hi:[1,0,0]\n"
    "v_pk_fma_f32 v[26:27], v[26:27], v[210:211], v[26:27] neg_lo:[1,0,0] neg_hi:[1,0,0]\n"
    "v_cvt_pk_bf16_f32 v228, v24, v25\n"
    "v_cvt_pk_bf16_f32 v229, v26, v27\n"
    "ds_read_b128 v[208:211], v112 offset:28672\n"
    "ds_read_b128 v[212:215], v112 offset:28688\n"
    "s_waitcnt lgkmcnt(2)\n"
    "v_pk_fma_f32 v[220:221], v[216:217], v[44:45], v[220:221] op_sel:[0,1,0] op_sel_hi:[1,1,1]\n"
    "v_pk_fma_f32 v[222:223], v[218:219], v[44:45], v[222:223] op_sel:[0,1,0] op_sel_hi:[1,1,1]\n"
    "v_pk_fma_f32 v[28:29], v[28:29], v[44:45], v[220:221] op_sel_hi:[1,0,1]\n"
    "v_pk_fma_f32 v[30:31], v[30:31], v[44:45], v[222:223] op_sel_hi:[1,0,1]\n"
    "v_exp_f32 v216, v28\n"
    "v_exp_f32 v217, v29\n"
    "v_exp_f32 v218, v30\n"
    "v_exp_f32 v219, v31\n"
    "v_pk_fma_f32 v[216:217], v[216:217], s[44:45], s[44:45]\n"
    "v_pk_fma_f32 v[218:219], v[218:219], s[44:45], s[44:45]\n"
    "v_pk_fma_f32 v[216:217], v[216:217], v[216:217], s[46:47]\n"
    "v_pk_fma_f32 v[218:219], v[218:219], v[218:219], s[46:47]\n"
    "v_rcp_f32 v216, v216\n"
    "v_rcp_f32 v217, v217\n"
    "v_rcp_f32 v218, v218\n"
    "v_rcp_f32 v219, v219\n"
    "v_pk_fma_f32 v[28:29], v[28:29], v[216:217], v[28:29] neg_lo:[1,0,0] neg_hi:[1,0,0]\n"
    "v_pk_fma_f32 v[30:31], v[30:31], v[218:219], v[30:31] neg_lo:[1,0,0] neg_hi:[1,0,0]\n"
    "v_cvt_pk_bf16_f32 v230, v28, v29\n"
    "v_cvt_pk_bf16_f32 v231, v30, v31\n"
    "buffer_store_dwordx4 v[228:231], v1, s[12:15], s91 offen offset:1024 sc1\n"
    "ds_read_b128 v[216:219], v112 offset:35840\n"
    "ds_read_b128 v[220:223], v112 offset:35856\n"
    "s_waitcnt lgkmcnt(2)\n"
    "v_pk_fma_f32 v[212:213], v[208:209], v[44:45], v[212:213] op_sel:[0,1,0] op_sel_hi:[1,1,1]\n"
    "v_pk_fma_f32 v[214:215], v[210:211], v[44:45], v[214:215] op_sel:[0,1,0] op_sel_hi:[1,1,1]\n"
    "v_pk_fma_f32 v[32:33], v[32:33], v[44:45], v[212:213] op_sel_hi:[1,0,1]\n"
    "v_pk_fma_f32 v[34:35], v[34:35], v[44:45], v[214:215] op_sel_hi:[1,0,1]\n"
    "v_exp_f32 v208, v32\n"
    "v_exp_f32 v209, v33\n"
    "v_exp_f32 v210, v34\n"
    "v_exp_f32 v211, v35\n"
    "v_pk_fma_f32 v[208:209], v[208:209], s[44:45], s[44:45]\n"
    "v_pk_fma_f32 v[210:211], v[210:211], s[44:45], s[44:45]\n"
    "v_pk_fma_f32 v[208:209], v[208:209], v[208:209], s[46:47]\n"
    "v_pk_fma_f32 v[210:211], v[210:211], v[210:211], s[46:47]\n"
    "v_rcp_f32 v208, v208\n"
    "v_rcp_f32 v209, v209\n"
    "v_rcp_f32 v210, v210\n"
    "v_rcp_f32 v211, v211\n"
    "v_pk_fma_f32 v[32:33], v[32:33], v[208:209], v[32:33] neg_lo:[1,0,0] neg_hi:[1,0,0]\n"
    "v_pk_fma_f32 v[34:35], v[34:35], v[210:211], v[34:35] neg_lo:[1,0,0] neg_hi:[1,0,0]\n"
    "v_cvt_pk_bf16_f32 v224, v32, v33\n"
    "v_cvt_pk_bf16_f32 v225, v34, v35\n"
    "ds_read_b128 v[208:211], v112 offset:43008\n"
    "ds_read_b128 v[212:215], v112 offset:43024\n"
    "s_waitcnt lgkmcnt(2)\n"
    "v_pk_fma_f32 v[220:221], v[216:217], v[44:45], v[220:221] op_sel:[0,1,0] op_sel_hi:[1,1,1]\n"
    "v_pk_fma_f32 v[222:223], v[218:219], v[44:45], v[222:223] op_sel:[0,1,0] op_sel_hi:[1,1,1]\n"
    "v_pk_fma_f32 v[36:37], v[36:37], v[44:45], v[220:221] op_sel_hi:[1,0,1]\n"
    "v_pk_fma_f32 v[38:39], v[38:39], v[44:45], v[222:223] op_sel_hi:[1,0,1]\n"
    "v_exp_f32 v216, v36\n"
    "v_exp_f32 v217, v37\n"
    "v_exp_f32 v218, v38\n"
    "v_exp_f32 v219, v39\n"
    "v_pk_fma_f32 v[216:217], v[216:217], s[44:45], s[44:45]\n"
    "v_pk_fma_f32 v[218:219], v[218:219], s[44:45], s[44:45]\n"
    "v_pk_fma_f32 v[216:217], v[216:217], v[216:217], s[46:47]\n"
    "v_pk_fma_f32 v[218:219], v[218:219], v[218:219], s[46:47]\n"
    "v_rcp_f32 v216, v216\n"
    "v_rcp_f32 v217, v217\n"
    "v_rcp_f32 v218, v218\n"
    "v_rcp_f32 v219, v219\n"
    "v_pk_fma_f32 v[36:37], v[36:37], v[216:217], v[36:37] neg_lo:[1,0,0] neg_hi:[1,0,0]\n"
    "v_pk_fma_f32 v[38:39], v[38:39], v[218:219], v[38:39] neg_lo:[1,0,0] neg_hi:[1,0,0]\n"
    "v_cvt_pk_bf16_f32 v226, v36, v37\n"
    "v_cvt_pk_bf16_f32 v227, v38, v39\n"
    "buffer_store_dwordx4 v[224:227], v1, s[12:15], s91 offen offset:2048 sc1\n"
    "s_waitcnt lgkmcnt(0)\n"
    "v_pk_fma_f32 v[212:213], v[208:209], v[44:45], v[212:213] op_sel:[0,1,0] op_sel_hi:[1,1,1]\n"
    "v_pk_fma_f32 v[214:215], v[210:211], v[44:45], v[214:215] op_sel:[0,1,0] op_sel_hi:[1,1,1]\n"
    "v_pk_fma_f32 v[40:41], v[40:41], v[44:45], v[212:213] op_sel_hi:[1,0,1]\n"
    "v_pk_fma_f32 v[42:43], v[42:43], v[44:45], v[214:215] op_sel_hi:[1,0,1]\n"
    "v_exp_f32 v208, v40\n"
    "v_exp_f32 v209, v41\n"
    "v_exp_f32 v210, v42\n"
    "v_exp_f32 v211, v43\n"
    "v_pk_fma_f32 v[208:209], v[208:209], s[44:45], s[44:45]\n"
    "v_pk_fma_f32 v[210:211], v[210:211], s[44:45], s[44:45]\n"
    "v_pk_fma_f32 v[208:209], v[208:209], v[208:209], s[46:47]\n"
    "v_pk_fma_f32 v[210:211], v[210:211], v[210:211], s[46:47]\n"
    "v_rcp_f32 v208, v208\n"
    "v_rcp_f32 v209, v209\n"
    "v_rcp_f32 v210, v210\n"
    "v_rcp_f32 v211, v211\n"
    "v_pk_fma_f32 v[40:41], v[40:41], v[208:209], v[40:41] neg_lo:[1,0,0] neg_hi:[1,0,0]\n"
    "v_pk_fma_f32 v[42:43], v[42:43], v[210:211], v[42:43] neg_lo:[1,0,0] neg_hi:[1,0,0]\n"
    "v_cvt_pk_bf16_f32 v228, v40, v41\n"
    "v_cvt_pk_bf16_f32 v229, v42, v43\n"
    "v_cndmask_b32 v228, v228, 0, s[100:101]\n"
    "v_cndmask_b32 v229, v229, 0, s[100:101]\n"
    "v_mov_b32 v230, v228\n"
    "v_mov_b32 v231, v229\n"
    "s_nop 0\n"
    "v_permlane16_swap_b32 v228, v230\n"
    "v_permlane16_swap_b32 v229, v231\n"
    "buffer_store_dwordx4 v[228:231], v115, s[12:15], s90 offen sc1\n"
    "buffer_load_dwordx4 v[168:171], v14, s[12:15], s74 offen sc1\n"
    "buffer_load_dwordx4 v[172:175], v14, s[12:15], s75 offen sc1\n"
    "buffer_load_dwordx4 v[176:179], v14, s[12:15], s76 offen sc1\n"
    "buffer_load_dwordx4 v[180:183], v14, s[12:15], s77 offen sc1\n"
    "s_waitcnt vmcnt(17)\n"
    "v_mfma_f32_16x16x32_bf16 v[16:19], a[0:3], v[128:131], 0\n"
    "s_add_u32 s88, s53, 17387520\n"
    "s_add_u32 s88, s88, s18\n"
    "v_mfma_f32_16x16x32_bf16 v[20:23], a[4:7], v[128:131], 0\n"
    "buffer_load_dwordx4 v[124:127], v13, s[12:15], s88 offen sc1\n"
    "v_mfma_f32_16x16x32_bf16 v[24:27], a[8:11], v[128:131], 0\n"
    "v_mfma_f32_16x16x32_bf16 v[28:31], a[12:15], v[128:131], 0\n"
    "v_mfma_f32_16x16x32_bf16 v[32:35], a[16:19], v[128:131], 0\n"
    "buffer_load_dwordx4 v[184:187], v15, s[12:15], s64 offen sc1\n"
    "s_waitcnt vmcnt(18)\n"
    "v_mfma_f32_16x16x32_bf16 v[16:19], a[20:23], v[132:135], v[16:19]\n"
    "v_mfma_f32_16x16x32_bf16 v[20:23], a[24:27], v[132:135], v[20:23]\n"
    "v_mfma_f32_16x16x32_bf16 v[24:27], a[28:31], v[132:135], v[24:27]\n"
    "v_mfma_f32_16x16x32_bf16 v[28:31], a[32:35], v[132:135], v[28:31]\n"
    "v_mfma_f32_16x16x32_bf16 v[32:35], a[36:39], v[132:135], v[32:35]\n"
    "buffer_load_dwordx4 v[188:191], v15, s[12:15], s65 offen sc1\n"
    "s_waitcnt vmcnt(18)\n"
    "v_mfma_f32_16x16x32_bf16 v[16:19], a[40:43], v[136:139], v[16:19]\n"
    "v_mfma_f32_16x16x32_bf16 v[20:23], a[44:47], v[136:139], v[20:23]\n"
    "v_mfma_f32_16x16x32_bf16 v[24:27], a[48:51], v[136:139], v[24:27]\n"
    "v_mfma_f32_16x16x32_bf16 v[28:31], a[52:55], v[136:139], v[28:31]\n"
    "v_mfma_f32_16x16x32_bf16 v[32:35], a[56:59], v[136:139], v[32:35]\n"
    "buffer_load_dwordx4 v[192:195], v15, s[12:15], s66 offen sc1\n"
    "s_waitcnt vmcnt(18)\n"
    "v_mfma_f32_16x16x32_bf16 v[16:19], a[60:63], v[140:143], v[16:19]\n"
    "v_mfma_f32_16x16x32_bf16 v[20:23], a[64:67], v[140:143], v[20:23]\n"
    "v_mfma_f32_16x16x32_bf16 v[24:27], a[68:71], v[140:143], v[24:27]\n"
    "v_mfma_f32_16x16x32_bf16 v[28:31], a[72:75], v[140:143], v[28:31]\n"
    "v_mfma_f32_16x16x32_bf16 v[32:35], a[76:79], v[140:143], v[32:35]\n"
    "buffer_load_dwordx4 v[196:199], v15, s[12:15], s67 offen sc1\n"
    "s_waitcnt vmcnt(18)\n"
    "v_mfma_f32_16x16x32_bf16 v[16:19], a[80:83], v[144:147], v[16:19]\n"
    "v_mfma_f32_16x16x32_bf16 v[20:23], a[84:87], v[144:147], v[20:23]\n"
    "v_mfma_f32_16x16x32_bf16 v[24:27], a[88:91], v[144:147], v[24:27]\n"
    "v_mfma_f32_16x16x32_bf16 v[28:31], a[92:95], v[144:147], v[28:31]\n"
    "v_mfma_f32_16x16x32_bf16 v[32:35], a[96:99], v[144:147], v[32:35]\n"
    "buffer_load_dwordx4 v[200:203], v15, s[12:15], s68 offen sc1\n"
    "s_waitcnt vmcnt(18)\n"
    "v_mfma_f32_16x16x32_bf16 v[16:19], a[100:103], v[148:151], v[16:19]\n"
    "v_mfma_f32_16x16x32_bf16 v[20:23], a[104:107], v[148:151], v[20:23]\n"
    "v_mfma_f32_16x16x32_bf16 v[24:27], a[108:111], v[148:151], v[24:27]\n"
    "v_mfma_f32_16x16x32_bf16 v[28:31], a[112:115], v[148:151], v[28:31]\n"
    "v_mfma_f32_16x16x32_bf16 v[32:35], a[116:119], v[148:151], v[32:35]\n"
    "buffer_load_dwordx4 v[204:207], v15, s[12:15], s69 offen sc1\n"
    "s_waitcnt vmcnt(18)\n"
    "v_mfma_f32_16x16x32_bf16 v[16:19], a[120:123], v[152:155], v[16:19]\n"
    "v_mfma_f32_16x16x32_bf16 v[20:23], a[124:127], v[152:155], v[20:23]\n"
    "v_mfma_f32_16x16x32_bf16 v[24:27], a[128:131], v[152:155], v[24:27]\n"
    "v_mfma_f32_16x16x32_bf16 v[28:31], a[132:135], v[152:155], v[28:31]\n"
    "v_mfma_f32_16x16x32_bf16 v[32:35], a[136:139], v[152:155], v[32:35]\n"
    "buffer_load_dwordx4 v[128:131], v15, s[12:15], s70 offen sc1\n"
    "s_waitcnt vmcnt(18)\n"
    "v_mfma_f32_16x16x32_bf16 v[16:19], a[140:143], v[156:159], v[16:19]\n"
    "s_waitcnt vmcnt(12)\n"
    "s_add_u32 s51, s50, s52\n"
    "v_mfma_f32_16x16x32_bf16 v[20:23], a[144:147], v[156:159], v[20:23]\n"
    "s_mov_b64 exec, 1\n"
    "buffer_atomic_add v123, v1, s[12:15], s51 offen offset:768\n"
    "s_mov_b64 exec, -1\n"
    "v_mfma_f32_16x16x32_bf16 v[24:27], a[148:151], v[156:159], v[24:27]\n"
    "v_mfma_f32_16x16x32_bf16 v[28:31], a[152:155], v[156:159], v[28:31]\n"
    "v_mfma_f32_16x16x32_bf16 v[32:35], a[156:159], v[156:159], v[32:35]\n"
    "buffer_load_dwordx4 v[132:135], v15, s[12:15], s71 offen sc1\n"
    "v_mfma_f32_16x16x32_bf16 v[16:19], a[160:163], v[160:163], v[16:19]\n"
    "v_mfma_f32_16x16x32_bf16 v[20:23], a[164:167], v[160:163], v[20:23]\n"
    "v_mfma_f32_16x16x32_bf16 v[24:27], a[168:171], v[160:163], v[24:27]\n"
    "v_mfma_f32_16x16x32_bf16 v[28:31], a[172:175], v[160:163], v[28:31]\n"
    "v_mfma_f32_16x16x32_bf16 v[32:35], a[176:179], v[160:163], v[32:35]\n"
    "buffer_load_dwordx4 v[136:139], v15, s[12:15], s72 offen sc1\n"
    "v_mfma_f32_16x16x32_bf16 v[16:19], a[180:183], v[164:167], v[16:19]\n"
    "v_mfma_f32_16x16x32_bf16 v[20:23], a[184:187], v[164:167], v[20:23]\n"
    "v_mfma_f32_16x16x32_bf16 v[24:27], a[188:191], v[164:167], v[24:27]\n"
    "v_mfma_f32_16x16x32_bf16 v[28:31], a[192:195], v[164:167], v[28:31]\n"
    "v_mfma_f32_16x16x32_bf16 v[32:35], a[196:199], v[164:167], v[32:35]\n"
    "buffer_load_dwordx4 v[140:143], v15, s[12:15], s73 offen sc1\n"
    "s_waitcnt vmcnt(15)\n"
    "v_mfma_f32_16x16x32_bf16 v[16:19], a[200:203], v[168:171], v[16:19]\n"
    "v_mfma_f32_16x16x32_bf16 v[20:23], a[204:207], v[168:171], v[20:23]\n"
    "v_mfma_f32_16x16x32_bf16 v[24:27], a[208:211], v[168:171], v[24:27]\n"
    "v_mfma_f32_16x16x32_bf16 v[28:31], a[212:215], v[168:171], v[28:31]\n"
    "v_mfma_f32_16x16x32_bf16 v[32:35], a[216:219], v[168:171], v[32:35]\n"
    "buffer_load_dwordx4 v[144:147], v15, s[12:15], s74 offen sc1\n"
    "s_waitcnt vmcnt(15)\n"
    "v_mfma_f32_16x16x32_bf16 v[16:19], a[220:223], v[172:175], v[16:19]\n"
    "v_mfma_f32_16x16x32_bf16 v[20:23], a[224:227], v[172:175], v[20:23]\n"
    "v_mfma_f32_16x16x32_bf16 v[24:27], a[228:231], v[172:175], v[24:27]\n"
    "v_mfma_f32_16x16x32_bf16 v[28:31], a[232:235], v[172:175], v[28:31]\n"
    "v_mfma_f32_16x16x32_bf16 v[32:35], a[236:239], v[172:175], v[32:35]\n"
    "buffer_load_dwordx4 v[148:151], v15, s[12:15], s75 offen sc1\n"
    "s_waitcnt vmcnt(15)\n"
    "v_mfma_f32_16x16x32_bf16 v[16:19], a[240:243], v[176:179], v[16:19]\n"
    "v_mfma_f32_16x16x32_bf16 v[20:23], a[244:247], v[176:179], v[20:23]\n"
    "s_waitcnt vmcnt(13)\n"
    "v_lshlrev_b32 v56, 16, v124\n"
    "v_and_b32 v57, 0xffff0000, v124\n"
    "v_mfma_f32_16x16x32_bf16 v[24:27], a[248:251], v[176:179], v[24:27]\n"
    "v_lshlrev_b32 v58, 16, v125\n"
    "v_and_b32 v59, 0xffff0000, v125\n"
    "v_mfma_f32_16x16x32_bf16 v[28:31], a[252:255], v[176:179], v[28:31]\n"
    "v_lshlrev_b32 v60, 16, v126\n"
    "v_and_b32 v61, 0xffff0000, v126\n"
    "v_mfma_f32_16x16x32_bf16 v[32:35], v[232:235], v[176:179], v[32:35]\n"
    "buffer_load_dwordx4 v[152:155], v15, s[12:15], s76 offen sc1\n"
    "v_lshlrev_b32 v62, 16, v127\n"
    "v_mfma_f32_16x16x32_bf16 v[16:19], v[236:239], v[180:183], v[16:19]\n"
    "v_and_b32 v63, 0xffff0000, v127\n"
    "s_add_u32 s88, s53, 17408000\n"
    "v_mfma_f32_16x16x32_bf16 v[20:23], v[240:243], v[180:183], v[20:23]\n"
    "s_add_u32 s88, s88, s18\n"
    "buffer_load_dwordx4 v[124:127], v13, s[12:15], s88 offen sc1\n"
    "v_mfma_f32_16x16x32_bf16 v[24:27], v[244:247], v[180:183], v[24:27]\n"
    "v_mfma_f32_16x16x32_bf16 v[28:31], v[248:251], v[180:183], v[28:31]\n"
    "v_mfma_f32_16x16x32_bf16 v[32:35], v[252:255], v[180:183], v[32:35]\n"
    "buffer_load_dwordx4 v[156:159], v15, s[12:15], s77 offen sc1\n"
    "s_waitcnt vmcnt(15)\n"
    "v_mfma_f32_16x16x32_bf16 v[36:39], a[0:3], v[184:187], 0\n"
    "v_mfma_f32_16x16x32_bf16 v[40:43], a[4:7], v[184:187], 0\n"
    "ds_write_b128 v8, v[16:19] offset:0\n"
    "v_mfma_f32_16x16x32_bf16 v[44:47], a[8:11], v[184:187], 0\n"
    "v_mfma_f32_16x16x32_bf16 v[48:51], a[12:15], v[184:187], 0\n"
    "ds_write_b128 v8, v[20:23] offset:4096\n"
    "v_mfma_f32_16x16x32_bf16 v[52:55], a[16:19], v[184:187], 0\n"
    "s_waitcnt vmcnt(14)\n"
    "v_mfma_f32_16x16x32_bf16 v[36:39], a[20:23], v[188:191], v[36:39]\n"
    "ds_write_b128 v8, v[24:27] offset:8192\n"
    "v_mfma_f32_16x16x32_bf16 v[40:43], a[24:27], v[188:191], v[40:43]\n"
    "v_mfma_f32_16x16x32_bf16 v[44:47], a[28:31], v[188:191], v[44:47]\n"
    "ds_write_b128 v8, v[28:31] offset:12288\n"
    "v_mfma_f32_16x16x32_bf16 v[48:51], a[32:35], v[188:191], v[48:51]\n"
    "v_mfma_f32_16x16x32_bf16 v[52:55], a[36:39], v[188:191], v[52:55]\n"
    "ds_write_b128 v8, v[32:35] offset:16384\n"
    "s_add_u32 s87, s53, 0\n"
    "s_waitcnt vmcnt(13)\n"
    "v_mfma_f32_16x16x32_bf16 v[36:39], a[40:43], v[192:195], v[36:39]\n"
    "v_mfma_f32_16x16x32_bf16 v[40:43], a[44:47], v[192:195], v[40:43]\n"
    "v_mfma_f32_16x16x32_bf16 v[44:47], a[48:51], v[192:195], v[44:47]\n"
    "v_mfma_f32_16x16x32_bf16 v[48:51], a[52:55], v[192:195], v[48:51]\n"
    "v_mfma_f32_16x16x32_bf16 v[52:55], a[56:59], v[192:195], v[52:55]\n"
    "s_waitcnt vmcnt(12)\n"
    "v_mfma_f32_16x16x32_bf16 v[36:39], a[60:63], v[196:199], v[36:39]\n"
    "v_mfma_f32_16x16x32_bf16 v[40:43], a[64:67], v[196:199], v[40:43]\n"
    "v_mfma_f32_16x16x32_bf16 v[44:47], a[68:71], v[196:199], v[44:47]\n"
    "v_mfma_f32_16x16x32_bf16 v[48:51], a[72:75], v[196:199], v[48:51]\n"
    "s_waitcnt lgkmcnt(0)\n"
    "s_barrier\n"
    "v_mfma_f32_16x16x32_bf16 v[52:55], a[76:79], v[196:199], v[52:55]\n"
    "ds_read_b128 v[80:83], v9 offset:0\n"
    "ds_read_b128 v[84:87], v9 offset:1024\n"
    "s_waitcnt vmcnt(11)\n"
    "v_mfma_f32_16x16x32_bf16 v[36:39], a[80:83], v[200:203], v[36:39]\n"
    "ds_read_b128 v[88:91], v9 offset:2048\n"
    "ds_read_b128 v[92:95], v9 offset:3072\n"
    "v_mfma_f32_16x16x32_bf16 v[40:43], a[84:87], v[200:203], v[40:43]\n"
    "ds_read_b128 v[96:99], v10 offset:0\n"
    "ds_read_b128 v[100:103], v10 offset:1024\n"
    "v_mfma_f32_16x16x32_bf16 v[44:47], a[88:91], v[200:203], v[44:47]\n"
    "ds_read_b128 v[104:107], v10 offset:2048\n"
    "ds_read_b128 v[108:111], v10 offset:3072\n"
    "v_mfma_f32_16x16x32_bf16 v[48:51], a[92:95], v[200:203], v[48:51]\n"
    "v_mfma_f32_16x16x32_bf16 v[52:55], a[96:99], v[200:203], v[52:55]\n"
    "s_waitcnt vmcnt(10)\n"
    "v_mfma_f32_16x16x32_bf16 v[36:39], a[100:103], v[204:207], v[36:39]\n"
    "v_mfma_f32_16x16x32_bf16 v[40:43], a[104:107], v[204:207], v[40:43]\n"
    "v_mfma_f32_16x16x32_bf16 v[44:47], a[108:111], v[204:207], v[44:47]\n"
    "v_mfma_f32_16x16x32_bf16 v[48:51], a[112:115], v[204:207], v[48:51]\n"
    "v_mfma_f32_16x16x32_bf16 v[52:55], a[116:119], v[204:207], v[52:55]\n"
    "buffer_load_dword v229, v12, s[12:15], s52 offen offset:768 sc1\n"
    "s_waitcnt vmcnt(10)\n"
    "v_mfma_f32_16x16x32_bf16 v[36:39], a[120:123], v[128:131], v[36:39]\n"
    "v_mfma_f32_16x16x32_bf16 v[40:43], a[124:127], v[128:131], v[40:43]\n"
    "s_waitcnt lgkmcnt(0)\n"
    "v_add_f32 v80, v80, v84\n"
    "v_mfma_f32_16x16x32_bf16 v[44:47], a[128:131], v[128:131], v[44:47]\n"
    "v_add_f32 v81, v81, v85\n"
    "v_add_f32 v82, v82, v86\n"
    "v_mfma_f32_16x16x32_bf16 v[48:51], a[132:135], v[128:131], v[48:51]\n"
    "v_add_f32 v83, v83, v87\n"
    "v_add_f32 v88, v88, v92\n"
    "v_mfma_f32_16x16x32_bf16 v[52:55], a[136:139], v[128:131], v[52:55]\n"
    "v_add_f32 v89, v89, v93\n"
    "v_add_f32 v90, v90, v94\n"
    "s_waitcnt vmcnt(8)\n"
    "v_mfma_f32_16x16x32_bf16 v[36:39], a[140:143], v[132:135], v[36:39]\n"
    "v_add_f32 v91, v91, v95\n"
    "v_add_f32 v80, v80, v88\n"
    "v_mfma_f32_16x16x32_bf16 v[40:43], a[144:147], v[132:135], v[40:43]\n"
    "v_add_f32 v81, v81, v89\n"
    "v_add_f32 v82, v82, v90\n"
    "v_mfma_f32_16x16x32_bf16 v[44:47], a[148:151], v[132:135], v[44:47]\n"
    "v_add_f32 v83, v83, v91\n"
    "v_fmac_f32 v56, s42, v80\n"
    "v_mfma_f32_16x16x32_bf16 v[48:51], a[152:155], v[132:135], v[48:51]\n"
    "v_fmac_f32 v57, s42, v81\n"
    "v_fmac_f32 v58, s42, v82\n"
    "v_mfma_f32_16x16x32_bf16 v[52:55], a[156:159], v[132:135], v[52:55]\n"
    "v_fmac_f32 v59, s42, v83\n"
    "v_add_f32 v96, v96, v100\n"
    "s_waitcnt vmcnt(7)\n"
    "v_mfma_f32_16x16x32_bf16 v[36:39], a[160:163], v[136:139], v[36:39]\n"
    "v_add_f32 v97, v97, v101\n"
    "v_add_f32 v98, v98, v102\n"
    "v_mfma_f32_16x16x32_bf16 v[40:43], a[164:167], v[136:139], v[40:43]\n"
    "v_add_f32 v99, v99, v103\n"
    "v_add_f32 v104, v104, v108\n"
    "v_mfma_f32_16x16x32_bf16 v[44:47], a[168:171], v[136:139], v[44:47]\n"
    "v_add_f32 v105, v105, v109\n"
    "v_add_f32 v106, v106, v110\n"
    "v_mfma_f32_16x16x32_bf16 v[48:51], a[172:175], v[136:139], v[48:51]\n"
    "v_add_f32 v107, v107, v111\n"
    "v_add_f32 v96, v96, v104\n"
    "v_mfma_f32_16x16x32_bf16 v[52:55], a[176:179], v[136:139], v[52:55]\n"
    "v_add_f32 v97, v97, v105\n"
    "v_add_f32 v98, v98, v106\n"
    "s_waitcnt vmcnt(6)\n"
    "v_mfma_f32_16x16x32_bf16 v[36:39], a[180:183], v[140:143], v[36:39]\n"
    "v_add_f32 v99, v99, v107\n"
    "v_fmac_f32 v60, s42, v96\n"
    "v_mfma_f32_16x16x32_bf16 v[40:43], a[184:187], v[140:143], v[40:43]\n"
    "v_fmac_f32 v61, s42, v97\n"
    "v_fmac_f32 v62, s42, v98\n"
    "v_mfma_f32_16x16x32_bf16 v[44:47], a[188:191], v[140:143], v[44:47]\n"
    "v_fmac_f32 v63, s42, v99\n"
    "s_barrier\n"
    "v_mfma_f32_16x16x32_bf16 v[48:51], a[192:195], v[140:143], v[48:51]\n"
    "v_cvt_pk_bf16_f32 v224, v56, v57\n"
    "v_cvt_pk_bf16_f32 v225, v58, v59\n"
    "v_mfma_f32_16x16x32_bf16 v[52:55], a[196:199], v[140:143], v[52:55]\n"
    "v_cvt_pk_bf16_f32 v226, v60, v61\n"
    "v_cvt_pk_bf16_f32 v227, v62, v63\n"
    "s_waitcnt vmcnt(5)\n"
    "v_mfma_f32_16x16x32_bf16 v[36:39], a[200:203], v[144:147], v[36:39]\n"
    "v_permlane16_swap_b32 v224, v226\n"
    "v_permlane16_swap_b32 v225, v227\n"
    "v_mfma_f32_16x16x32_bf16 v[40:43], a[204:207], v[144:147], v[40:43]\n"
    "buffer_store_dwordx4 v[224:227], v120, s[28:31], s87 offen\n"
    "v_mfma_f32_16x16x32_bf16 v[44:47], a[208:211], v[144:147], v[44:47]\n"
    "v_mfma_f32_16x16x32_bf16 v[48:51], a[212:215], v[144:147], v[48:51]\n"
    "v_mfma_f32_16x16x32_bf16 v[52:55], a[216:219], v[144:147], v[52:55]\n"
    "s_waitcnt vmcnt(5)\n"
    "v_mfma_f32_16x16x32_bf16 v[36:39], a[220:223], v[148:151], v[36:39]\n"
    "v_mfma_f32_16x16x32_bf16 v[40:43], a[224:227], v[148:151], v[40:43]\n"
    "s_waitcnt vmcnt(3)\n"
    "v_lshlrev_b32 v64, 16, v124\n"
    "v_and_b32 v65, 0xffff0000, v124\n"
    "v_mfma_f32_16x16x32_bf16 v[44:47], a[228:231], v[148:151], v[44:47]\n"
    "v_lshlrev_b32 v66, 16, v125\n"
    "v_and_b32 v67, 0xffff0000, v125\n"
    "v_mfma_f32_16x16x32_bf16 v[48:51], a[232:235], v[148:151], v[48:51]\n"
    "v_lshlrev_b32 v68, 16, v126\n"
    "v_and_b32 v69, 0xffff0000, v126\n"
    "v_mfma_f32_16x16x32_bf16 v[52:55], a[236:239], v[148:151], v[52:55]\n"
    "v_lshlrev_b32 v70, 16, v127\n"
    "v_and_b32 v71, 0xffff0000, v127\n"
    "v_mfma_f32_16x16x32_bf16 v[36:39], a[240:243], v[152:155], v[36:39]\n"
    "v_mfma_f32_16x16x32_bf16 v[40:43], a[244:247], v[152:155], v[40:43]\n"
    "v_mfma_f32_16x16x32_bf16 v[44:47], a[248:251], v[152:155], v[44:47]\n"
    "v_mfma_f32_16x16x32_bf16 v[48:51], a[252:255], v[152:155], v[48:51]\n"
    "v_mfma_f32_16x16x32_bf16 v[52:55], v[232:235], v[152:155], v[52:55]\n"
    "s_waitcnt vmcnt(1)\n"
    "v_cmp_ne_u32 vcc, 16, v229\n"
    "s_cbranch_vccnz Lslow_79_%=\n"
    "Lback_80_%=:\n"
    "s_add_u32 s92, s57, 2\n"
    "s_mul_i32 s92, s92, 57344\n"
    "s_add_u32 s92, s92, s49\n"
    "v_add_u32 v14, s92, v1\n"
    "s_add_u32 s92, s57, 3\n"
    "s_mul_i32 s92, s92, 57344\n"
    "s_add_u32 s92, s92, s49\n"
    "v_add_u32 v15, s92, v1\n"
    "buffer_load_dwordx4 v[160:163], v14, s[12:15], s64 offen sc1\n"
    "v_mfma_f32_16x16x32_bf16 v[36:39], v[236:239], v[156:159], v[36:39]\n"
    "v_mfma_f32_16x16x32_bf16 v[40:43], v[240:243], v[156:159], v[40:43]\n"
    "v_mfma_f32_16x16x32_bf16 v[44:47], v[244:247], v[156:159], v[44:47]\n"
    "v_mfma_f32_16x16x32_bf16 v[48:51], v[248:251], v[156:159], v[48:51]\n"
    "v_mfma_f32_16x16x32_bf16 v[52:55], v[252:255], v[156:159], v[52:55]\n"
    "buffer_load_dwordx4 v[164:167], v14, s[12:15], s65 offen sc1\n"
    "buffer_load_dwordx4 v[168:171], v14, s[12:15], s66 offen sc1\n"
    "s_nop 1\n"
    "ds_write_b128 v8, v[36:39] offset:0\n"
    "ds_write_b128 v8, v[40:43] offset:4096\n"
    "ds_write_b128 v8, v[44:47] offset:8192\n"
    "ds_write_b128 v8, v[48:51] offset:12288\n"
    "ds_write_b128 v8, v[52:55] offset:16384\n"
    "s_add_u32 s87, s53, 20480\n"
    "buffer_load_dwordx4 v[172:175], v14, s[12:15], s67 offen sc1\n"
    "buffer_load_dwordx4 v[176:179], v14, s[12:15], s68 offen sc1\n"
    "buffer_load_dwordx4 v[180:183], v14, s[12:15], s69 offen sc1\n"
    "s_waitcnt lgkmcnt(0)\n"
    "s_barrier\n"
    "ds_read_b128 v[80:83], v9 offset:0\n"
    "ds_read_b128 v[84:87], v9 offset:1024\n"
    "ds_read_b128 v[88:91], v9 offset:2048\n"
    "ds_read_b128 v[92:95], v9 offset:3072\n"
    "ds_read_b128 v[96:99], v10 offset:0\n"
    "ds_read_b128 v[100:103], v10 offset:1024\n"
    "ds_read_b128 v[104:107], v10 offset:2048\n"
    "ds_read_b128 v[108:111], v10 offset:3072\n"
    "buffer_load_dwordx4 v[184:187], v14, s[12:15], s70 offen sc1\n"
    "buffer_load_dwordx4 v[188:191], v14, s[12:15], s71 offen sc1\n"
    "buffer_load_dwordx4 v[192:195], v14, s[12:15], s72 offen sc1\n"
    "s_waitcnt lgkmcnt(0)\n"
    "v_pk_add_f32 v[80:81], v[80:81], v[84:85]\n"
    "v_pk_add_f32 v[88:89], v[88:89], v[92:93]\n"
    "v_pk_add_f32 v[82:83], v[82:83], v[86:87]\n"
    "v_pk_add_f32 v[90:91], v[90:91], v[94:95]\n"
    "buffer_load_dwordx4 v[196:199], v14, s[12:15], s73 offen sc1\n"
    "v_pk_add_f32 v[80:81], v[80:81], v[88:89]\n"
    "v_pk_add_f32 v[82:83], v[82:83], v[90:91]\n"
    "v_pk_fma_f32 v[64:65], v[80:81], s[42:43], v[64:65]\n"
    "v_pk_fma_f32 v[66:67], v[82:83], s[42:43], v[66:67]\n"
    "v_pk_add_f32 v[96:97], v[96:97], v[100:101]\n"
    "v_pk_add_f32 v[104:105], v[104:105], v[108:109]\n"
    "v_pk_add_f32 v[98:99], v[98:99], v[102:103]\n"
    "v_pk_add_f32 v[106:107], v[106:107], v[110:111]\n"
    "v_pk_add_f32 v[96:97], v[96:97], v[104:105]\n"
    "v_pk_add_f32 v[98:99], v[98:99], v[106:107]\n"
    "v_pk_fma_f32 v[68:69], v[96:97], s[42:43], v[68:69]\n"
    "v_pk_fma_f32 v[70:71], v[98:99], s[42:43], v[70:71]\n"
    "s_barrier\n"
    "v_cvt_pk_bf16_f32 v224, v64, v65\n"
    "buffer_load_dwordx4 v[200:203], v14, s[12:15], s74 offen sc1\n"
    "v_cvt_pk_bf16_f32 v225, v66, v67\n"
    "v_cvt_pk_bf16_f32 v226, v68, v69\n"
    "v_cvt_pk_bf16_f32 v227, v70, v71\n"
    "s_nop 0\n"
    "v_permlane16_swap_b32 v224, v226\n"
    "v_permlane16_swap_b32 v225, v227\n"
    "buffer_store_dwordx4 v[224:227], v120, s[28:31], s87 offen\n"
    "buffer_load_dwordx4 v[204:207], v14, s[12:15], s75 offen sc1\n"
    "buffer_load_dwordx4 v[128:131], v14, s[12:15], s76 offen sc1\n"
    "buffer_load_dwordx4 v[132:135], v14, s[12:15], s77 offen sc1\n"
    "s_waitcnt vmcnt(14)\n"
    "v_mfma_f32_16x16x32_bf16 v[16:19], a[0:3], v[160:163], 0\n"
    "v_mfma_f32_16x16x32_bf16 v[20:23], a[4:7], v[160:163], 0\n"
    "v_mfma_f32_16x16x32_bf16 v[24:27], a[8:11], v[160:163], 0\n"
    "v_mfma_f32_16x16x32_bf16 v[28:31], a[12:15], v[160:163], 0\n"
    "s_add_u32 s88, s53, 17428480\n"
    "s_add_u32 s88, s88, s18\n"
    "v_mfma_f32_16x16x32_bf16 v[32:35], a[16:19], v[160:163], 0\n"
    "buffer_load_dwordx4 v[136:139], v15, s[12:15], s64 offen sc1\n"
    "s_waitcnt vmcnt(14)\n"
    "v_mfma_f32_16x16x32_bf16 v[16:19], a[20:23], v[164:167], v[16:19]\n"
    "v_mfma_f32_16x16x32_bf16 v[20:23], a[24:27], v[164:167], v[20:23]\n"
    "buffer_load_dwordx4 v[124:127], v13, s[12:15], s88 offen sc1\n"
    "v_mfma_f32_16x16x32_bf16 v[24:27], a[28:31], v[164:167], v[24:27]\n"
    "v_mfma_f32_16x16x32_bf16 v[28:31], a[32:35], v[164:167], v[28:31]\n"
    "v_mfma_f32_16x16x32_bf16 v[32:35], a[36:39], v[164:167], v[32:35]\n"
    "buffer_load_dwordx4 v[140:143], v15, s[12:15], s65 offen sc1\n"
    "s_waitcnt vmcnt(15)\n"
    "v_mfma_f32_16x16x32_bf16 v[16:19], a[40:43], v[168:171], v[16:19]\n"
    "v_mfma_f32_16x16x32_bf16 v[20:23], a[44:47], v[168:171], v[20:23]\n"
    "v_mfma_f32_16x16x32_bf16 v[24:27], a[48:51], v[168:171], v[24:27]\n"
    "v_mfma_f32_16x16x32_bf16 v[28:31], a[52:55], v[168:171], v[28:31]\n"
    "v_mfma_f32_16x16x32_bf16 v[32:35], a[56:59], v[168:171], v[32:35]\n"
    "buffer_load_dwordx4 v[144:147], v15, s[12:15], s66 offen sc1\n"
    "s_waitcnt vmcnt(15)\n"
    "v_mfma_f32_16x16x32_bf16 v[16:19], a[60:63], v[172:175], v[16:19]\n"
    "v_mfma_f32_16x16x32_bf16 v[20:23], a[64:67], v[172:175], v[20:23]\n"
    "v_mfma_f32_16x16x32_bf16 v[24:27], a[68:71], v[172:175], v[24:27]\n"
    "v_mfma_f32_16x16x32_bf16 v[28:31], a[72:75], v[172:175], v[28:31]\n"
    "v_mfma_f32_16x16x32_bf16 v[32:35], a[76:79], v[172:175], v[32:35]\n"
    "buffer_load_dwordx4 v[148:151], v15, s[12:15], s67 offen sc1\n"
    "s_waitcnt vmcnt(15)\n"
    "v_mfma_f32_16x16x32_bf16 v[16:19], a[80:83], v[176:179], v[16:19]\n"
    "v_mfma_f32_16x16x32_bf16 v[20:23], a[84:87], v[176:179], v[20:23]\n"
    "v_mfma_f32_16x16x32_bf16 v[24:27], a[88:91], v[176:179], v[24:27]\n"
    "v_mfma_f32_16x16x32_bf16 v[28:31], a[92:95], v[176:179], v[28:31]\n"
    "v_mfma_f32_16x16x32_bf16 v[32:35], a[96:99], v[176:179], v[32:35]\n"
    "buffer_load_dwordx4 v[152:155], v15, s[12:15], s68 offen sc1\n"
    "s_waitcnt vmcnt(15)\n"
    "v_mfma_f32_16x16x32_bf16 v[16:19], a[100:103], v[180:183], v[16:19]\n"
    "v_mfma_f32_16x16x32_bf16 v[20:23], a[104:107], v[180:183], v[20:23]\n"
    "v_mfma_f32_16x16x32_bf16 v[24:27], a[108:111], v[180:183], v[24:27]\n"
    "v_mfma_f32_16x16x32_bf16 v[28:31], a[112:115], v[180:183], v[28:31]\n"
    "v_mfma_f32_16x16x32_bf16 v[32:35], a[116:119], v[180:183], v[32:35]\n"
    "buffer_load_dwordx4 v[156:159], v15, s[12:15], s69 offen sc1\n"
    "s_waitcnt vmcnt(15)\n"
    "v_mfma_f32_16x16x32_bf16 v[16:19], a[120:123], v[184:187], v[16:19]\n"
    "v_mfma_f32_16x16x32_bf16 v[20:23], a[124:127], v[184:187], v[20:23]\n"
    "v_mfma_f32_16x16x32_bf16 v[24:27], a[128:131], v[184:187], v[24:27]\n"
    "v_mfma_f32_16x16x32_bf16 v[28:31], a[132:135], v[184:187], v[28:31]\n"
    "v_mfma_f32_16x16x32_bf16 v[32:35], a[136:139], v[184:187], v[32:35]\n"
    "buffer_load_dwordx4 v[160:163], v15, s[12:15], s70 offen sc1\n"
    "s_waitcnt vmcnt(15)\n"
    "v_mfma_f32_16x16x32_bf16 v[16:19], a[140:143], v[188:191], v[16:19]\n"
    "v_mfma_f32_16x16x32_bf16 v[20:23], a[144:147], v[188:191], v[20:23]\n"
    "v_mfma_f32_16x16x32_bf16 v[24:27], a[148:151], v[188:191], v[24:27]\n"
    "v_mfma_f32_16x16x32_bf16 v[28:31], a[152:155], v[188:191], v[28:31]\n"
    "v_mfma_f32_16x16x32_bf16 v[32:35], a[156:159], v[188:191], v[32:35]\n"
    "buffer_load_dwordx4 v[164:167], v15, s[12:15], s71 offen sc1\n"
    "s_waitcnt vmcnt(15)\n"
    "v_mfma_f32_16x16x32_bf16 v[16:19], a[160:163], v[192:195], v[16:19]\n"
    "v_mfma_f32_16x16x32_bf16 v[20:23], a[164:167], v[192:195], v[20:23]\n"
    "v_mfma_f32_16x16x32_bf16 v[24:27], a[168:171], v[192:195], v[24:27]\n"
    "v_mfma_f32_16x16x32_bf16 v[28:31], a[172:175], v[192:195], v[28:31]\n"
    "v_mfma_f32_16x16x32_bf16 v[32:35], a[176:179], v[192:195], v[32:35]\n"
    "buffer_load_dwordx4 v[168:171], v15, s[12:15], s72 offen sc1\n"
    "s_waitcnt vmcnt(15)\n"
    "v_mfma_f32_16x16x32_bf16 v[16:19], a[180:183], v[196:199], v[16:19]\n"
    "v_mfma_f32_16x16x32_bf16 v[20:23], a[184:187], v[196:199], v[20:23]\n"
    "v_mfma_f32_16x16x32_bf16 v[24:27], a[188:191], v[196:199], v[24:27]\n"
    "v_mfma_f32_16x16x32_bf16 v[28:31], a[192:195], v[196:199], v[28:31]\n"
    "v_mfma_f32_16x16x32_bf16 v[32:35], a[196:199], v[196:199], v[32:35]\n"
    "buffer_load_dwordx4 v[172:175], v15, s[12:15], s73 offen sc1\n"
    "s_waitcnt vmcnt(15)\n"
    "v_mfma_f32_16x16x32_bf16 v[16:19], a[200:203], v[200:203], v[16:19]\n"
    "v_mfma_f32_16x16x32_bf16 v[20:23], a[204:207], v[200:203], v[20:23]\n"
    "v_mfma_f32_16x16x32_bf16 v[24:27], a[208:211], v[200:203], v[24:27]\n"
    "v_mfma_f32_16x16x32_bf16 v[28:31], a[212:215], v[200:203], v[28:31]\n"
    "v_mfma_f32_16x16x32_bf16 v[32:35], a[216:219], v[200:203], v[32:35]\n"
    "buffer_load_dwordx4 v[176:179], v15, s[12:15], s74 offen sc1\n"
    "s_waitcnt vmcnt(14)\n"
    "v_mfma_f32_16x16x32_bf16 v[16:19], a[220:223], v[204:207], v[16:19]\n"
    "v_mfma_f32_16x16x32_bf16 v[20:23], a[224:227], v[204:207], v[20:23]\n"
    "v_mfma_f32_16x16x32_bf16 v[24:27], a[228:231], v[204:207], v[24:27]\n"
    "v_mfma_f32_16x16x32_bf16 v[28:31], a[232:235], v[204:207], v[28:31]\n"
    "v_mfma_f32_16x16x32_bf16 v[32:35], a[236:239], v[204:207], v[32:35]\n"
    "buffer_load_dwordx4 v[180:183], v15, s[12:15], s75 offen sc1\n"
    "s_waitcnt vmcnt(14)\n"
    "v_mfma_f32_16x16x32_bf16 v[16:19], a[240:243], v[128:131], v[16:19]\n"
    "v_mfma_f32_16x16x32_bf16 v[20:23], a[244:247], v[128:131], v[20:23]\n"
    "v_mfma_f32_16x16x32_bf16 v[24:27], a[248:251], v[128:131], v[24:27]\n"
    "v_mfma_f32_16x16x32_bf16 v[28:31], a[252:255], v[128:131], v[28:31]\n"
    "v_mfma_f32_16x16x32_bf16 v[32:35], v[232:235], v[128:131], v[32:35]\n"
    "buffer_load_dwordx4 v[184:187], v15, s[12:15], s76 offen sc1\n"
    "s_waitcnt vmcnt(14)\n"
    "v_mfma_f32_16x16x32_bf16 v[16:19], v[236:239], v[132:135], v[16:19]\n"
    "v_mfma_f32_16x16x32_bf16 v[20:23], v[240:243], v[132:135], v[20:23]\n"
    "s_waitcnt vmcnt(12)\n"
    "v_lshlrev_b32 v208, 16, v124\n"
    "v_and_b32 v209, 0xffff0000, v124\n"
    "v_mfma_f32_16x16x32_bf16 v[24:27], v[244:247], v[132:135], v[24:27]\n"
    "v_lshlrev_b32 v210, 16, v125\n"
    "v_and_b32 v211, 0xffff0000, v125\n"
    "v_mfma_f32_16x16x32_bf16 v[28:31], v[248:251], v[132:135], v[28:31]\n"
    "v_lshlrev_b32 v212, 16, v126\n"
    "v_and_b32 v213, 0xffff0000, v126\n"
    "v_mfma_f32_16x16x32_bf16 v[32:35], v[252:255], v[132:135], v[32:35]\n"
    "buffer_load_dwordx4 v[188:191], v15, s[12:15], s77 offen sc1\n"
    "v_lshlrev_b32 v214, 16, v127\n"
    "v_mfma_f32_16x16x32_bf16 v[36:39], a[0:3], v[136:139], 0\n"
    "v_and_b32 v215, 0xffff0000, v127\n"
    "s_add_u32 s88, s53, 17448960\n"
    "v_mfma_f32_16x16x32_bf16 v[40:43], a[4:7], v[136:139], 0\n"
    "ds_write_b128 v8, v[16:19] offset:0\n"
    "s_add_u32 s88, s88, s18\n"
    "v_mfma_f32_16x16x32_bf16 v[44:47], a[8:11], v[136:139], 0\n"
    "buffer_load_dwordx4 v[124:127], v13, s[12:15], s88 offen sc1\n"
    "v_mfma_f32_16x16x32_bf16 v[48:51], a[12:15], v[136:139], 0\n"
    "ds_write_b128 v8, v[20:23] offset:4096\n"
    "v_mfma_f32_16x16x32_bf16 v[52:55], a[16:19], v[136:139], 0\n"
    "s_waitcnt vmcnt(13)\n"
    "v_mfma_f32_16x16x32_bf16 v[36:39], a[20:23], v[140:143], v[36:39]\n"
    "ds_write_b128 v8, v[24:27] offset:8192\n"
    "v_mfma_f32_16x16x32_bf16 v[40:43], a[24:27], v[140:143], v[40:43]\n"
    "v_mfma_f32_16x16x32_bf16 v[44:47], a[28:31], v[140:143], v[44:47]\n"
    "ds_write_b128 v8, v[28:31] offset:12288\n"
    "v_mfma_f32_16x16x32_bf16 v[48:51], a[32:35], v[140:143], v[48:51]\n"
    "v_mfma_f32_16x16x32_bf16 v[52:55], a[36:39], v[140:143], v[52:55]\n"
    "ds_write_b128 v8, v[32:35] offset:16384\n"
    "s_add_u32 s87, s53, 40960\n"
    "s_waitcnt vmcnt(12)\n"
    "v_mfma_f32_16x16x32_bf16 v[36:39], a[40:43], v[144:147], v[36:39]\n"
    "v_mfma_f32_16x16x32_bf16 v[40:43], a[44:47], v[144:147], v[40:43]\n"
    "v_mfma_f32_16x16x32_bf16 v[44:47], a[48:51], v[144:147], v[44:47]\n"
    "v_mfma_f32_16x16x32_bf16 v[48:51], a[52:55], v[144:147], v[48:51]\n"
    "v_mfma_f32_16x16x32_bf16 v[52:55], a[56:59], v[144:147], v[52:55]\n"
    "s_waitcnt vmcnt(11)\n"
    "v_mfma_f32_16x16x32_bf16 v[36:39], a[60:63], v[148:151], v[36:39]\n"
    "v_mfma_f32_16x16x32_bf16 v[40:43], a[64:67], v[148:151], v[40:43]\n"
    "v_mfma_f32_16x16x32_bf16 v[44:47], a[68:71], v[148:151], v[44:47]\n"
    "v_mfma_f32_16x16x32_bf16 v[48:51], a[72:75], v[148:151], v[48:51]\n"
    "s_waitcnt lgkmcnt(0)\n"
    "s_barrier\n"
    "v_mfma_f32_16x16x32_bf16 v[52:55], a[76:79], v[148:151], v[52:55]\n"
    "ds_read_b128 v[80:83], v9 offset:0\n"
    "ds_read_b128 v[84:87], v9 offset:1024\n"
    "s_waitcnt vmcnt(10)\n"
    "v_mfma_f32_16x16x32_bf16 v[36:39], a[80:83], v[152:155], v[36:39]\n"
    "ds_read_b128 v[88:91], v9 offset:2048\n"
    "ds_read_b128 v[92:95], v9 offset:3072\n"
    "v_mfma_f32_16x16x32_bf16 v[40:43], a[84:87], v[152:155], v[40:43]\n"
    "ds_read_b128 v[96:99], v10 offset:0\n"
    "ds_read_b128 v[100:103], v10 offset:1024\n"
    "v_mfma_f32_16x16x32_bf16 v[44:47], a[88:91], v[152:155], v[44:47]\n"
    "ds_read_b128 v[104:107], v10 offset:2048\n"
    "ds_read_b128 v[108:111], v10 offset:3072\n"
    "v_mfma_f32_16x16x32_bf16 v[48:51], a[92:95], v[152:155], v[48:51]\n"
    "v_mfma_f32_16x16x32_bf16 v[52:55], a[96:99], v[152:155], v[52:55]\n"
    "s_waitcnt vmcnt(9)\n"
    "v_mfma_f32_16x16x32_bf16 v[36:39], a[100:103], v[156:159], v[36:39]\n"
    "v_mfma_f32_16x16x32_bf16 v[40:43], a[104:107], v[156:159], v[40:43]\n"
    "v_mfma_f32_16x16x32_bf16 v[44:47], a[108:111], v[156:159], v[44:47]\n"
    "v_mfma_f32_16x16x32_bf16 v[48:51], a[112:115], v[156:159], v[48:51]\n"
    "v_mfma_f32_16x16x32_bf16 v[52:55], a[116:119], v[156:159], v[52:55]\n"
    "s_waitcnt vmcnt(8)\n"
    "v_mfma_f32_16x16x32_bf16 v[36:39], a[120:123], v[160:163], v[36:39]\n"
    "v_mfma_f32_16x16x32_bf16 v[40:43], a[124:127], v[160:163], v[40:43]\n"
    "s_waitcnt lgkmcnt(0)\n"
    "v_add_f32 v80, v80, v84\n"
    "v_mfma_f32_16x16x32_bf16 v[44:47], a[128:131], v[160:163], v[44:47]\n"
    "v_add_f32 v81, v81, v85\n"
    "v_add_f32 v82, v82, v86\n"
    "v_mfma_f32_16x16x32_bf16 v[48:51], a[132:135], v[160:163], v[48:51]\n"
    "v_add_f32 v83, v83, v87\n"
    "v_add_f32 v88, v88, v92\n"
    "v_mfma_f32_16x16x32_bf16 v[52:55], a[136:139], v[160:163], v[52:55]\n"
    "v_add_f32 v89, v89, v93\n"
    "v_add_f32 v90, v90, v94\n"
    "s_waitcnt vmcnt(7)\n"
    "v_mfma_f32_16x16x32_bf16 v[36:39], a[140:143], v[164:167], v[36:39]\n"
    "v_add_f32 v91, v91, v95\n"
    "v_add_f32 v80, v80, v88\n"
    "v_mfma_f32_16x16x32_bf16 v[40:43], a[144:147], v[164:167], v[40:43]\n"
    "v_add_f32 v81, v81, v89\n"
    "v_add_f32 v82, v82, v90\n"
    "v_mfma_f32_16x16x32_bf16 v[44:47], a[148:151], v[164:167], v[44:47]\n"
    "v_add_f32 v83, v83, v91\n"
    "v_fmac_f32 v208, s42, v80\n"
    "v_mfma_f32_16x16x32_bf16 v[48:51], a[152:155], v[164:167], v[48:51]\n"
    "v_fmac_f32 v209, s42, v81\n"
    "v_fmac_f32 v210, s42, v82\n"
    "v_mfma_f32_16x16x32_bf16 v[52:55], a[156:159], v[164:167], v[52:55]\n"
    "v_fmac_f32 v211, s42, v83\n"
    "v_add_f32 v96, v96, v100\n"
    "s_waitcnt vmcnt(6)\n"
    "v_mfma_f32_16x16x32_bf16 v[36:39], a[160:163], v[168:171], v[36:39]\n"
    "v_add_f32 v97, v97, v101\n"
    "v_add_f32 v98, v98, v102\n"
    "v_mfma_f32_16x16x32_bf16 v[40:43], a[164:167], v[168:171], v[40:43]\n"
    "v_add_f32 v99, v99, v103\n"
    "v_add_f32 v104, v104, v108\n"
    "v_mfma_f32_16x16x32_bf16 v[44:47], a[168:171], v[168:171], v[44:47]\n"
    "v_add_f32 v105, v105, v109\n"
    "v_add_f32 v106, v106, v110\n"
    "v_mfma_f32_16x16x32_bf16 v[48:51], a[172:175], v[168:171], v[48:51]\n"
    "v_add_f32 v107, v107, v111\n"
    "v_add_f32 v96, v96, v104\n"
    "v_mfma_f32_16x16x32_bf16 v[52:55], a[176:179], v[168:171], v[52:55]\n"
    "v_add_f32 v97, v97, v105\n"
    "v_add_f32 v98, v98, v106\n"
    "s_waitcnt vmcnt(5)\n"
    "v_mfma_f32_16x16x32_bf16 v[36:39], a[180:183], v[172:175], v[36:39]\n"
    "v_add_f32 v99, v99, v107\n"
    "v_fmac_f32 v212, s42, v96\n"
    "v_mfma_f32_16x16x32_bf16 v[40:43], a[184:187], v[172:175], v[40:43]\n"
    "v_fmac_f32 v213, s42, v97\n"
    "v_fmac_f32 v214, s42, v98\n"
    "v_mfma_f32_16x16x32_bf16 v[44:47], a[188:191], v[172:175], v[44:47]\n"
    "v_fmac_f32 v215, s42, v99\n"
    "s_barrier\n"
    "v_mfma_f32_16x16x32_bf16 v[48:51], a[192:195], v[172:175], v[48:51]\n"
    "v_cvt_pk_bf16_f32 v224, v208, v209\n"
    "v_cvt_pk_bf16_f32 v225, v210, v211\n"
    "v_mfma_f32_16x16x32_bf16 v[52:55], a[196:199], v[172:175], v[52:55]\n"
    "v_cvt_pk_bf16_f32 v226, v212, v213\n"
    "v_cvt_pk_bf16_f32 v227, v214, v215\n"
    "s_waitcnt vmcnt(4)\n"
    "v_mfma_f32_16x16x32_bf16 v[36:39], a[200:203], v[176:179], v[36:39]\n"
    "v_permlane16_swap_b32 v224, v226\n"
    "v_permlane16_swap_b32 v225, v227\n"
    "v_mfma_f32_16x16x32_bf16 v[40:43], a[204:207], v[176:179], v[40:43]\n"
    "buffer_store_dwordx4 v[224:227], v120, s[28:31], s87 offen\n"
    "v_mfma_f32_16x16x32_bf16 v[44:47], a[208:211], v[176:179], v[44:47]\n"
    "v_mfma_f32_16x16x32_bf16 v[48:51], a[212:215], v[176:179], v[48:51]\n"
    "v_mfma_f32_16x16x32_bf16 v[52:55], a[216:219], v[176:179], v[52:55]\n"
    "s_waitcnt vmcnt(4)\n"
    "v_mfma_f32_16x16x32_bf16 v[36:39], a[220:223], v[180:183], v[36:39]\n"
    "v_mfma_f32_16x16x32_bf16 v[40:43], a[224:227], v[180:183], v[40:43]\n"
    "v_mfma_f32_16x16x32_bf16 v[44:47], a[228:231], v[180:183], v[44:47]\n"
    "v_mfma_f32_16x16x32_bf16 v[48:51], a[232:235], v[180:183], v[48:51]\n"
    "v_mfma_f32_16x16x32_bf16 v[52:55], a[236:239], v[180:183], v[52:55]\n"
    "s_waitcnt vmcnt(3)\n"
    "v_mfma_f32_16x16x32_bf16 v[36:39], a[240:243], v[184:187], v[36:39]\n"
    "v_mfma_f32_16x16x32_bf16 v[40:43], a[244:247], v[184:187], v[40:43]\n"
    "v_mfma_f32_16x16x32_bf16 v[44:47], a[248:251], v[184:187], v[44:47]\n"
    "s_waitcnt vmcnt(1)\n"
    "v_lshlrev_b32 v216, 16, v124\n"
    "v_and_b32 v217, 0xffff0000, v124\n"
    "v_mfma_f32_16x16x32_bf16 v[48:51], a[252:255], v[184:187], v[48:51]\n"
    "v_lshlrev_b32 v218, 16, v125\n"
    "v_and_b32 v219, 0xffff0000, v125\n"
    "v_mfma_f32_16x16x32_bf16 v[52:55], v[232:235], v[184:187], v[52:55]\n"
    "v_lshlrev_b32 v220, 16, v126\n"
    "v_and_b32 v221, 0xffff0000, v126\n"
    "v_mfma_f32_16x16x32_bf16 v[36:39], v[236:239], v[188:191], v[36:39]\n"
    "v_lshlrev_b32 v222, 16, v127\n"
    "v_and_b32 v223, 0xffff0000, v127\n"
    "v_mfma_f32_16x16x32_bf16 v[40:43], v[240:243], v[188:191], v[40:43]\n"
    "v_mfma_f32_16x16x32_bf16 v[44:47], v[244:247], v[188:191], v[44:47]\n"
    "v_mfma_f32_16x16x32_bf16 v[48:51], v[248:251], v[188:191], v[48:51]\n"
    "v_mfma_f32_16x16x32_bf16 v[52:55], v[252:255], v[188:191], v[52:55]\n"
    "s_nop 1\n"
    "ds_write_b128 v8, v[36:39] offset:0\n"
    "s_nop 1\n"
    "ds_write_b128 v8, v[40:43] offset:4096\n"
    "ds_write_b128 v8, v[44:47] offset:8192\n"
    "ds_write_b128 v8, v[48:51] offset:12288\n"
    "ds_write_b128 v8, v[52:55] offset:16384\n"
    "s_add_u32 s87, s53, 61440\n"
    "s_waitcnt lgkmcnt(0)\n"
    "s_barrier\n"
    "ds_read_b128 v[80:83], v9 offset:0\n"
    "ds_read_b128 v[84:87], v9 offset:1024\n"
    "ds_read_b128 v[88:91], v9 offset:2048\n"
    "ds_read_b128 v[92:95], v9 offset:3072\n"
    "ds_read_b128 v[96:99], v10 offset:0\n"
    "ds_read_b128 v[100:103], v10 offset:1024\n"
    "ds_read_b128 v[104:107], v10 offset:2048\n"
    "ds_read_b128 v[108:111], v10 offset:3072\n"
    "s_waitcnt lgkmcnt(0)\n"
    "v_pk_add_f32 v[80:81], v[80:81], v[84:85]\n"
    "v_pk_add_f32 v[88:89], v[88:89], v[92:93]\n"
    "v_pk_add_f32 v[82:83], v[82:83], v[86:87]\n"
    "v_pk_add_f32 v[90:91], v[90:91], v[94:95]\n"
    "v_pk_add_f32 v[80:81], v[80:81], v[88:89]\n"
    "v_pk_add_f32 v[82:83], v[82:83], v[90:91]\n"
    "v_pk_fma_f32 v[216:217], v[80:81], s[42:43], v[216:217]\n"
    "v_pk_fma_f32 v[218:219], v[82:83], s[42:43], v[218:219]\n"
    "v_pk_add_f32 v[96:97], v[96:97], v[100:101]\n"
    "v_pk_add_f32 v[104:105], v[104:105], v[108:109]\n"
    "v_pk_add_f32 v[98:99], v[98:99], v[102:103]\n"
    "v_pk_add_f32 v[106:107], v[106:107], v[110:111]\n"
    "v_pk_add_f32 v[96:97], v[96:97], v[104:105]\n"
    "v_pk_add_f32 v[98:99], v[98:99], v[106:107]\n"
    "v_pk_fma_f32 v[220:221], v[96:97], s[42:43], v[220:221]\n"
    "v_pk_fma_f32 v[222:223], v[98:99], s[42:43], v[222:223]\n"
    "v_cvt_pk_bf16_f32 v224, v216, v217\n"
    "v_cvt_pk_bf16_f32 v225, v218, v219\n"
    "v_cvt_pk_bf16_f32 v226, v220, v221\n"
    "v_cvt_pk_bf16_f32 v227, v222, v223\n"
    "s_nop 0\n"
    "v_permlane16_swap_b32 v224, v226\n"
    "v_permlane16_swap_b32 v225, v227\n"
    "buffer_store_dwordx4 v[224:227], v120, s[28:31], s87 offen\n"
    "Lfin_%=:\n"
    "v_mov_b32 v16, s60\n"
    "v_mov_b32 v17, s61\n"
    "s_mov_b32 s83, 0\n"
    "s_cmp_eq_u32 s8, 0\n"
    "s_cselect_b32 s83, s83, 2147418112\n"
    "v_mov_b32 v18, s83\n"
    "buffer_store_dwordx2 v[16:17], v18, s[12:15], 0 offen sc1\n"
    "s_endpgm\n"
    "Lslow_3_%=:\n"
    "s_mov_b32 s96, 0x40000\n"
    "Lspin_5_%=:\n"
    "s_sleep 16\n"
    "buffer_load_dwordx2 v[16:17], v11, s[12:15], s90 offen offset:0 sc1\n"
    "s_waitcnt vmcnt(0)\n"
    "v_cmp_ne_u64 vcc, s[60:61], v[16:17]\n"
    "s_cbranch_vccz Ldone_6_%=\n"
    "s_sub_u32 s96, s96, 1\n"
    "s_cbranch_scc0 Lspin_5_%=\n"
    "Ldone_6_%=:\n"
    "s_branch Lback_4_%=\n"
    "Lslow_11_%=:\n"
    "s_mov_b32 s96, 0x40000\n"
    "Lspin_13_%=:\n"
    "s_sleep 2\n"
    "buffer_load_dwordx2 v[16:17], v10, s[12:15], 0 offen offset:0 sc1\n"
    "s_waitcnt vmcnt(0)\n"
    "v_cmp_ne_u64 vcc, s[60:61], v[16:17]\n"
    "s_cbranch_vccz Ldone_14_%=\n"
    "s_sub_u32 s96, s96, 1\n"
    "s_cbranch_scc0 Lspin_13_%=\n"
    "Ldone_14_%=:\n"
    "s_branch Lback_12_%=\n"
    "Lslow_15_%=:\n"
    "s_mov_b32 s96, 0x40000\n"
    "Lspin_17_%=:\n"
    "s_sleep 8\n"
    "buffer_load_dwordx2 v[208:209], v230, s[12:15], s90 offen offset:0 sc1\n"
    "s_waitcnt vmcnt(0)\n"
    "v_cmp_ne_u64 vcc, s[60:61], v[208:209]\n"
    "s_cbranch_vccz Ldone_18_%=\n"
    "s_sub_u32 s96, s96, 1\n"
    "s_cbranch_scc0 Lspin_17_%=\n"
    "Ldone_18_%=:\n"
    "s_branch Lback_16_%=\n"
    "Lslow_19_%=:\n"
    "s_mov_b32 s96, 0x40000\n"
    "Lspin_21_%=:\n"
    "s_sleep 8\n"
    "buffer_load_dwordx2 v[210:211], v12, s[12:15], s91 offen offset:0 sc1\n"
    "s_waitcnt vmcnt(0)\n"
    "v_cmp_ne_u64 vcc, s[60:61], v[210:211]\n"
    "s_cbranch_vccz Ldone_22_%=\n"
    "s_sub_u32 s96, s96, 1\n"
    "s_cbranch_scc0 Lspin_21_%=\n"
    "Ldone_22_%=:\n"
    "s_branch Lback_20_%=\n"
    "Lslow_23_%=:\n"
    "s_mov_b32 s96, 0x40000\n"
    "Lspin_25_%=:\n"
    "s_sleep 8\n"
    "buffer_load_dwordx2 v[226:227], v12, s[12:15], s90 offen offset:0 sc1\n"
    "s_waitcnt vmcnt(0)\n"
    "v_cmp_ne_u64 vcc, s[60:61], v[226:227]\n"
    "s_cbranch_vccz Ldone_26_%=\n"
    "s_sub_u32 s96, s96, 1\n"
    "s_cbranch_scc0 Lspin_25_%=\n"
    "Ldone_26_%=:\n"
    "s_branch Lback_24_%=\n"
    "Lslow_28_%=:\n"
    "s_mov_b32 s96, 0x40000\n"
    "Lspin_30_%=:\n"
    "s_sleep 6\n"
    "buffer_load_dwordx2 v[16:17], v12, s[12:15], 0 offen offset:0 sc1\n"
    "s_waitcnt vmcnt(0)\n"
    "v_cmp_ne_u64 vcc, s[62:63], v[16:17]\n"
    "s_cbranch_vccz Ldone_31_%=\n"
    "s_sub_u32 s96, s96, 1\n"
    "s_cbranch_scc0 Lspin_30_%=\n"
    "Ldone_31_%=:\n"
    "s_branch Lback_29_%=\n"
    "Lslow_33_%=:\n"
    "s_mov_b32 s96, 0x40000\n"
    "Lspin_35_%=:\n"
    "s_sleep 3\n"
    "buffer_load_dword v117, v12, s[12:15], s52 offen offset:0 sc1\n"
    "s_waitcnt vmcnt(0)\n"
    "v_cmp_ne_u32 vcc, 16, v117\n"
    "s_cbranch_vccz Ldone_36_%=\n"
    "s_sub_u32 s96, s96, 1\n"
    "s_cbranch_scc0 Lspin_35_%=\n"
    "Ldone_36_%=:\n"
    "s_branch Lback_34_%=\n"
    "Lslow_37_%=:\n"
    "s_mov_b32 s96, 0x40000\n"
    "Lspin_39_%=:\n"
    "s_sleep 8\n"
    "buffer_load_dwordx2 v[118:119], v116, s[12:15], 0 offen offset:128 sc1\n"
    "s_waitcnt vmcnt(0)\n"
    "v_cmp_ne_u64 vcc, s[62:63], v[118:119]\n"
    "s_cbranch_vccz Ldone_40_%=\n"
    "s_sub_u32 s96, s96, 1\n"
    "s_cbranch_scc0 Lspin_39_%=\n"
    "Ldone_40_%=:\n"
    "s_branch Lback_38_%=\n"
    "Lslow_41_%=:\n"
    "s_mov_b32 s96, 0x40000\n"
    "Lspin_43_%=:\n"
    "s_sleep 3\n"
    "buffer_load_dword v229, v12, s[12:15], s52 offen offset:768 sc1\n"
    "s_waitcnt vmcnt(0)\n"
    "v_cmp_ne_u32 vcc, 16, v229\n"
    "s_cbranch_vccz Ldone_44_%=\n"
    "s_sub_u32 s96, s96, 1\n"
    "s_cbranch_scc0 Lspin_43_%=\n"
    "Ldone_44_%=:\n"
    "s_branch Lback_42_%=\n"
    "Lslow_45_%=:\n"
    "s_mov_b32 s96, 0x40000\n"
    "Lspin_47_%=:\n"
    "s_sleep 3\n"
    "buffer_load_dwordx2 v[224:225], v2, s[12:15], s22 offen offset:0 sc1\n"
    "buffer_load_dwordx2 v[226:227], v2, s[12:15], s22 offen offset:512 sc1\n"
    "s_waitcnt vmcnt(0)\n"
    "v_max3_u32 v228, v224, v225, v226\n"
    "v_max_u32 v228, v228, v227\n"
    "v_cmp_eq_u32 vcc, -1, v228\n"
    "s_cbranch_vccz Ldone_48_%=\n"
    "s_sub_u32 s96, s96, 1\n"
    "s_cbranch_scc0 Lspin_47_%=\n"
    "Ldone_48_%=:\n"
    "s_branch Lback_46_%=\n"
    "Lslow_50_%=:\n"
    "s_mov_b32 s96, 0x40000\n"
    "Lspin_52_%=:\n"
    "s_sleep 3\n"
    "buffer_load_dwordx2 v[124:125], v2, s[12:15], s22 offen offset:1024 sc1\n"
    "buffer_load_dwordx2 v[126:127], v2, s[12:15], s22 offen offset:1536 sc1\n"
    "s_waitcnt vmcnt(0)\n"
    "v_max3_u32 v228, v124, v125, v126\n"
    "v_max_u32 v228, v228, v127\n"
    "v_cmp_eq_u32 vcc, -1, v228\n"
    "s_cbranch_vccz Ldone_53_%=\n"
    "s_sub_u32 s96, s96, 1\n"
    "s_cbranch_scc0 Lspin_52_%=\n"
    "Ldone_53_%=:\n"
    "s_branch Lback_51_%=\n"
    "Lslow_56_%=:\n"
    "s_mov_b32 s96, 0x40000\n"
    "Lspin_58_%=:\n"
    "s_sleep 3\n"
    "buffer_load_dwordx2 v[224:225], v2, s[12:15], s22 offen offset:2048 sc1\n"
    "buffer_load_dwordx2 v[226:227], v2, s[12:15], s22 offen offset:2560 sc1\n"
    "s_waitcnt vmcnt(0)\n"
    "v_max3_u32 v228, v224, v225, v226\n"
    "v_max_u32 v228, v228, v227\n"
    "v_cmp_eq_u32 vcc, -1, v228\n"
    "s_cbranch_vccz Ldone_59_%=\n"
    "s_sub_u32 s96, s96, 1\n"
    "s_cbranch_scc0 Lspin_58_%=\n"
    "Ldone_59_%=:\n"
    "s_branch Lback_57_%=\n"
    "Lslow_60_%=:\n"
    "s_mov_b32 s96, 0x40000\n"
    "Lspin_62_%=:\n"
    "s_sleep 3\n"
    "buffer_load_dwordx2 v[124:125], v2, s[12:15], s22 offen offset:3072 sc1\n"
    "buffer_load_dwordx2 v[126:127], v2, s[12:15], s22 offen offset:3584 sc1\n"
    "s_waitcnt vmcnt(0)\n"
    "v_max3_u32 v228, v124, v125, v126\n"
    "v_max_u32 v228, v228, v127\n"
    "v_cmp_eq_u32 vcc, -1, v228\n"
    "s_cbranch_vccz Ldone_63_%=\n"
    "s_sub_u32 s96, s96, 1\n"
    "s_cbranch_scc0 Lspin_62_%=\n"
    "Ldone_63_%=:\n"
    "s_branch Lback_61_%=\n"
    "Lslow_66_%=:\n"
    "s_mov_b32 s96, 0x40000\n"
    "Lspin_68_%=:\n"
    "s_sleep 3\n"
    "buffer_load_dword v19, v12, s[12:15], 0 offen offset:256 sc1\n"
    "s_waitcnt vmcnt(0)\n"
    "v_cmp_ne_u32 vcc, 8, v19\n"
    "s_cbranch_vccz Ldone_69_%=\n"
    "s_sub_u32 s96, s96, 1\n"
    "s_cbranch_scc0 Lspin_68_%=\n"
    "Ldone_69_%=:\n"
    "s_branch Lback_67_%=\n"
    "Lslow_71_%=:\n"
    "s_mov_b32 s96, 0x40000\n"
    "Lspin_73_%=:\n"
    "s_sleep 3\n"
    "buffer_load_dword v224, v12, s[12:15], 0 offen offset:1024 sc1\n"
    "s_waitcnt vmcnt(0)\n"
    "v_cmp_ne_u32 vcc, 8, v224\n"
    "s_cbranch_vccz Ldone_74_%=\n"
    "s_sub_u32 s96, s96, 1\n"
    "s_cbranch_scc0 Lspin_73_%=\n"
    "Ldone_74_%=:\n"
    "s_branch Lback_72_%=\n"
    "Lslow_75_%=:\n"
    "s_mov_b32 s96, 0x40000\n"
    "Lspin_77_%=:\n"
    "s_sleep 3\n"
    "buffer_load_dword v117, v12, s[12:15], s52 offen offset:0 sc1\n"
    "s_waitcnt vmcnt(0)\n"
    "v_cmp_ne_u32 vcc, 16, v117\n"
    "s_cbranch_vccz Ldone_78_%=\n"
    "s_sub_u32 s96, s96, 1\n"
    "s_cbranch_scc0 Lspin_77_%=\n"
    "Ldone_78_%=:\n"
    "s_branch Lback_76_%=\n"
    "Lslow_79_%=:\n"
    "s_mov_b32 s96, 0x40000\n"
    "Lspin_81_%=:\n"
    "s_sleep 3\n"
    "buffer_load_dword v229, v12, s[12:15], s52 offen offset:768 sc1\n"
    "s_waitcnt vmcnt(0)\n"
    "v_cmp_ne_u32 vcc, 16, v229\n"
    "s_cbranch_vccz Ldone_82_%=\n"
    "s_sub_u32 s96, s96, 1\n"
    "s_cbranch_scc0 Lspin_81_%=\n"
    "Ldone_82_%=:\n"
    "s_branch Lback_80_%=\n"
    "Lend_%=:\n"
    :: "s"(__builtin_amdgcn_kernarg_segment_ptr()), "s"(blockIdx.x), "v"(threadIdx.x), "s"((unsigned)(size_t)(__attribute__((address_space(3))) char*)lds)
    : "v1", "v2", "v3", "v4", "v5", "v6", "v7", "v8", "v9", "v10", "v11", "v12", "v13", "v14", "v15", "v16", "v17", "v18", "v19", "v20", "v21", "v22", "v23", "v24",
      "v25", "v26", "v27", "v28", "v29", "v30", "v31", "v32", "v33", "v34", "v35", "v36", "v37", "v38", "v39", "v40", "v41", "v42", "v43", "v44", "v45", "v46", "v47", "v48",
      "v49", "v50", "v51", "v52", "v53", "v54", "v55", "v56", "v57", "v58", "v59", "v60", "v61", "v62", "v63", "v64", "v65", "v66", "v67", "v68", "v69", "v70", "v71", "v72",
      "v73", "v74", "v75", "v76", "v77", "v78", "v79", "v80", "v81", "v82", "v83", "v84", "v85", "v86", "v87", "v88", "v89", "v90", "v91", "v92", "v93", "v94", "v95", "v96",
      "v97", "v98", "v99", "v100", "v101", "v102", "v103", "v104", "v105", "v106", "v107", "v108", "v109", "v110", "v111", "v112", "v113", "v114", "v115", "v116", "v117", "v118", "v119", "v120",
      "v121", "v122", "v123", "v124", "v125", "v126", "v127", "v128", "v129", "v130", "v131", "v132", "v133", "v134", "v135", "v136", "v137", "v138", "v139", "v140", "v141", "v142", "v143", "v144",
      "v145", "v146", "v147", "v148", "v149", "v150", "v151", "v152", "v153", "v154", "v155", "v156", "v157", "v158", "v159", "v160", "v161", "v162", "v163", "v164", "v165", "v166", "v167", "v168",
      "v169", "v170", "v171", "v172", "v173", "v174", "v175", "v176", "v177", "v178", "v179", "v180", "v181", "v182", "v183", "v184", "v185", "v186", "v187", "v188", "v189", "v190", "v191", "v192",
      "v193", "v194", "v195", "v196", "v197", "v198", "v199", "v200", "v201", "v202", "v203", "v204", "v205", "v206", "v207", "v208", "v209", "v210", "v211", "v212", "v213", "v214", "v215", "v216",
      "v217", "v218", "v219", "v220", "v221", "v222", "v223", "v224", "v225", "v226", "v227", "v228", "v229", "v230", "v231", "v232", "v233", "v234", "v235", "v236", "v237", "v238", "v239", "v240",
      "v241", "v242", "v243", "v244", "v245", "v246", "v247", "v248", "v249", "v250", "v251", "v252", "v253", "v254", "v255", "a0", "a1", "a2", "a3", "a4", "a5", "a6", "a7", "a8",
      "a9", "a10", "a11", "a12", "a13", "a14", "a15", "a16", "a17", "a18", "a19", "a20", "a21", "a22", "a23", "a24", "a25", "a26", "a27", "a28", "a29", "a30", "a31", "a32",
      "a33", "a34", "a35", "a36", "a37", "a38", "a39", "a40", "a41", "a42", "a43", "a44", "a45", "a46", "a47", "a48", "a49", "a50", "a51", "a52", "a53", "a54", "a55", "a56",
      "a57", "a58", "a59", "a60", "a61", "a62", "a63", "a64", "a65", "a66", "a67", "a68", "a69", "a70", "a71", "a72", "a73", "a74", "a75", "a76", "a77", "a78", "a79", "a80",
      "a81", "a82", "a83", "a84", "a85", "a86", "a87", "a88", "a89", "a90", "a91", "a92", "a93", "a94", "a95", "a96", "a97", "a98", "a99", "a100", "a101", "a102", "a103", "a104",
      "a105", "a106", "a107", "a108", "a109", "a110", "a111", "a112", "a113", "a114", "a115", "a116", "a117", "a118", "a119", "a120", "a121", "a122", "a123", "a124", "a125", "a126", "a127", "a128",
      "a129", "a130", "a131", "a132", "a133", "a134", "a135", "a136", "a137", "a138", "a139", "a140", "a141", "a142", "a143", "a144", "a145", "a146", "a147", "a148", "a149", "a150", "a151", "a152",
      "a153", "a154", "a155", "a156", "a157", "a158", "a159", "a160", "a161", "a162", "a163", "a164", "a165", "a166", "a167", "a168", "a169", "a170", "a171", "a172", "a173", "a174", "a175", "a176",
      "a177", "a178", "a179", "a180", "a181", "a182", "a183", "a184", "a185", "a186", "a187", "a188", "a189", "a190", "a191", "a192", "a193", "a194", "a195", "a196", "a197", "a198", "a199", "a200",
      "a201", "a202", "a203", "a204", "a205", "a206", "a207", "a208", "a209", "a210", "a211", "a212", "a213", "a214", "a215", "a216", "a217", "a218", "a219", "a220", "a221", "a222", "a223", "a224",
      "a225", "a226", "a227", "a228", "a229", "a230", "a231", "a232", "a233", "a234", "a235", "a236", "a237", "a238", "a239", "a240", "a241", "a242", "a243", "a244", "a245", "a246", "a247", "a248",
      "a249", "a250", "a251", "a252", "a253", "a254", "a255", "s4", "s5", "s6", "s7", "s8", "s9", "s10", "s11", "s12", "s13", "s14", "s15", "s16", "s17", "s18", "s19", "s20",
      "s21", "s22", "s23", "s24", "s25", "s26", "s27", "s28", "s29", "s30", "s31", "s32", "s33", "s34", "s35", "s36", "s37", "s38", "s39", "s40", "s41", "s42", "s43", "s44",
      "s45", "s46", "s47", "s48", "s49", "s50", "s51", "s52", "s53", "s54", "s55", "s56", "s57", "s58", "s59", "s60", "s61", "s62", "s63", "s64", "s65", "s66", "s67", "s68",
      "s69", "s70", "s71", "s72", "s73", "s74", "s75", "s76", "s77", "s78", "s79", "s80", "s81", "s82", "s83", "s84", "s85", "s86", "s87", "s88", "s89", "s90", "s91", "s92",
      "s93", "s94", "s95", "s96", "s97", "s98", "s99", "s100", "s101", "vcc", "scc", "memory");
}

extern "C" void kernel_launch(void* const* d_in, const int* in_sizes, int n_in, void* d_out, int out_size, void* d_ws, size_t ws_size,
                              hipStream_t stream) {
  h3_mish_fused<<<256, 256, 0, stream>>>(d_in[0], d_in[1], d_in[2], d_in[3], d_in[4], d_in[5], d_in[7], d_in[8], d_in[9], d_out, d_ws);
}
```
